```python
import torch

def _softmax(x, axis=-1):
    return torch.softmax(x, dim=axis)
def _gelu(x):
    return torch.nn.functional.gelu(x, approximate="tanh")
def _silu(x):
    return torch.nn.functional.silu(x)
def _sigmoid(x):
    return torch.sigmoid(x)
def _layernorm(x, axis=-1, eps=1e-5):
    m = x.mean(dim=axis, keepdim=True)
    v = x.var(dim=axis, keepdim=True, unbiased=False)
    return (x - m) / torch.sqrt(v + eps)
def _scatter_add(tgt, idx, src, axis=0):
    return tgt.clone().index_add_(axis, idx, src)
def _segment_sum(src, seg, n):
    out = torch.zeros((n,) + src.shape[1:], dtype=src.dtype)
    return out.index_add_(0, seg, src)
def _pad(x, pw):
    flat = tuple(v for pair in reversed(pw) for v in pair)
    return torch.nn.functional.pad(x, flat)

kernel_name = 'standalone__mha_softmax__near_ridge'

def setup_inputs():

    _w = (lambda a: (lambda t: t.to(torch.bfloat16) if t.is_floating_point() else t)(torch.as_tensor(a)))
    B, H, Sq, Sk, D = 8, 32, 368, 1024, 128
    return {
        "q": _w((lambda *s: torch.randn(*s, dtype=torch.bfloat16) * 0.8)(B, H, Sq, D)),
        "k": _w((lambda *s: torch.randn(*s, dtype=torch.bfloat16) * 0.8)(B, H, Sk, D)),
        "v": _w((lambda *s: torch.randn(*s, dtype=torch.bfloat16) * 0.05)(B, H, Sk, D)),
    }

def reference(q, k, v):
    scale = q.shape[-1] ** -0.5
    scores = torch.einsum("bhqd,bhkd->bhqk", q, k) * scale
    p = _softmax(scores, axis=-1)
    return torch.einsum("bhqk,bhkd->bhqd", p, v)
```

```cpp
#include <hip/hip_runtime.h>
#include <hip/hip_bf16.h>

namespace {
constexpr int SQ = 368, SK = 1024, HD = 128, NHEAD = 256;
constexpr int LDS_BYTES = 163840;
typedef float f32x4 __attribute__((ext_vector_type(4)));
typedef float f32x2 __attribute__((ext_vector_type(2)));
typedef __bf16 bf16x8 __attribute__((ext_vector_type(8)));
typedef __bf16 bf16x2 __attribute__((ext_vector_type(2)));
typedef unsigned u32x4 __attribute__((ext_vector_type(4)));

__device__ __forceinline__ unsigned lane_id() {
  unsigned l;
  asm volatile("v_mbcnt_lo_u32_b32 %0, -1, 0\n\tv_mbcnt_hi_u32_b32 %0, -1, %0" : "=v"(l));
  return l;
}

__device__ __forceinline__ float true_row_max(const char* qh, const char* kh, int row0, unsigned lane) {
  const unsigned c = lane & 15u, g = lane >> 4;
  const int row = min(row0 + (int)c, SQ - 1);
  const __amdgpu_buffer_rsrc_t rq = __builtin_amdgcn_make_buffer_rsrc((void*)qh, (short)0, SQ * HD * 2, 0x20000);
  const __amdgpu_buffer_rsrc_t rk = __builtin_amdgcn_make_buffer_rsrc((void*)kh, (short)0, SK * HD * 2, 0x20000);
  const float sc = __uint_as_float(0x3e0293eeu);
  bf16x8 qf[4];
#pragma unroll
  for (int d0 = 0; d0 < 4; ++d0) {
    const u32x4 w = __builtin_amdgcn_raw_buffer_load_b128(rq, row * 256 + d0 * 64 + g * 16, 0, 0);
    u32x4 r;
#pragma unroll
    for (int j = 0; j < 4; ++j) {
      const f32x2 x = {__uint_as_float(w[j] << 16) * sc, __uint_as_float(w[j] & 0xffff0000u) * sc};
      r[j] = __builtin_bit_cast(unsigned, __builtin_convertvector(x, bf16x2));
    }
    qf[d0] = __builtin_bit_cast(bf16x8, r);
  }
  float m = -__builtin_inff();
#pragma unroll 2
  for (int kb = 0; kb < SK / 16; ++kb) {
    f32x4 acc = {0.f, 0.f, 0.f, 0.f};
#pragma unroll
    for (int d0 = 0; d0 < 4; ++d0) {
      const bf16x8 kf = __builtin_bit_cast(bf16x8, __builtin_amdgcn_raw_buffer_load_b128(rk, c * 256 + d0 * 64 + g * 16, kb * 4096, 0));
      acc = __builtin_amdgcn_mfma_f32_16x16x32_bf16(kf, qf[d0], acc, 0, 0, 0);
    }
    m = fmaxf(fmaxf(m, fmaxf(acc[0], acc[1])), fmaxf(acc[2], acc[3]));
  }
  m = fmaxf(m, __int_as_float(__builtin_amdgcn_ds_bpermute((int)((lane ^ 16u) << 2), __float_as_int(m))));
  m = fmaxf(m, __int_as_float(__builtin_amdgcn_ds_bpermute((int)((lane ^ 32u) << 2), __float_as_int(m))));
  return m;
}
}

extern "C" __global__ __launch_bounds__(256, 1) void mha_fwd(const char* __restrict__ q, const char* __restrict__ k, const char* __restrict__ v,
                                                             char* __restrict__ o) {
  __shared__ __attribute__((aligned(16))) char lds[LDS_BYTES];
  const unsigned wid = __builtin_amdgcn_readfirstlane(threadIdx.x >> 6);
  const unsigned head = blockIdx.x;
  float m0 = -__builtin_inff(), m1 = m0, m2 = m0, m3 = m0, m4 = m0, m5 = m0;
#pragma nounroll
  for (int pass = 0; pass < 2; ++pass) {
    unsigned flag;
    asm volatile(
"s_load_dwordx8 s[16:23], %[karg], 0\n"
"v_mbcnt_lo_u32_b32 v128, -1, 0\n"
"v_mbcnt_hi_u32_b32 v128, -1, v128\n"
"s_lshl_b32 s52, %[wid], 11\n"
"s_lshl_b32 s53, %[wid], 12\n"
"s_and_b32 s55, %[wid], 1\n"
"s_lshl_b32 s55, s55, 2\n"
"s_lshl_b32 s58, %[blk], 18\n"
"s_mul_i32 s59, %[blk], 94208\n"
"v_lshrrev_b32 v129, 3, v128\n"
"v_lshrrev_b32 v130, 4, v128\n"
"v_and_b32 v131, 7, v128\n"
"v_xor_b32 v131, v131, v130\n"
"v_lshlrev_b32 v129, 8, v129\n"
"v_add_u32 v129, 4096, v129\n"
"v_xor_b32 v132, s55, v131\n"
"v_lshl_add_u32 v248, v132, 4, v129\n"
"v_subrev_u32 v249, 896, v248\n"
"v_bfe_u32 v132, v128, 4, 1\n"
"v_lshlrev_b32 v132, 1, v132\n"
"v_and_b32 v130, 3, v128\n"
"v_xor_b32 v132, v132, v130\n"
"v_lshlrev_b32 v132, 4, v132\n"
"v_bfe_u32 v130, v128, 2, 3\n"
"v_lshl_add_u32 v132, v130, 8, v132\n"
"v_lshrrev_b32 v130, 5, v128\n"
"v_lshl_add_u32 v132, v130, 6, v132\n"
"v_add_u32 v250, 2048, v132\n"
"v_add_u32 v251, 1152, v132\n"
"v_lshl_add_u32 v136, v131, 4, v129\n"
"v_subrev_u32 v137, 896, v136\n"
"v_xor_b32 v132, 4, v131\n"
"v_lshl_add_u32 v138, v132, 4, v129\n"
"v_subrev_u32 v139, 896, v138\n"
"s_mul_i32 s54, %[wid], 24576\n"
"s_getpc_b64 s[56:57]\n"
"L_pc_%=:\n"
"s_lshr_b32 s55, %[blk], 3\n"
"s_and_b32 s55, s55, 31\n"
"s_lshl_b32 s55, s55, 2\n"
"s_add_u32 s55, s55, %[wid]\n"
"s_lshl_b32 s55, s55, 10\n"
"s_min_u32 s55, s55, L_end_%=-L_pc_%=-1024\n"
"v_lshl_add_u32 v132, v128, 4, s55\n"
"global_load_dwordx4 v[140:143], v132, s[56:57]\n"
"s_waitcnt lgkmcnt(0)\n"
"s_add_u32 s36, s18, s58\n"
"s_addc_u32 s37, s19, 0\n"
"s_sub_u32 s36, s36, 4096\n"
"s_subb_u32 s37, s37, 0\n"
"s_and_b32 s37, s37, 0xffff\n"
"s_mov_b32 s38, 266240\n"
"s_mov_b32 s39, 0x20000\n"
"s_mov_b32 m0, s53\n"
"s_add_u32 s24, s52, 0\n"
"buffer_load_dwordx4 v248, s[36:39], s24 offen nt lds\n"
"buffer_load_dwordx4 v249, s[36:39], s24 offen offset:1024 nt lds\n"
"s_add_u32 m0, s53, 16384\n"
"s_add_u32 s24, s52, 8192\n"
"buffer_load_dwordx4 v248, s[36:39], s24 offen nt lds\n"
"buffer_load_dwordx4 v249, s[36:39], s24 offen offset:1024 nt lds\n"
"s_add_u32 s44, s16, s59\n"
"s_addc_u32 s45, s17, 0\n"
"s_sub_u32 s44, s44, 4096\n"
"s_subb_u32 s45, s45, 0\n"
"s_and_b32 s45, s45, 0xffff\n"
"s_mov_b32 s46, 98304\n"
"s_mov_b32 s47, 0x20000\n"
"s_add_u32 m0, s53, 65536\n"
"s_nop 0\n"
"buffer_load_dwordx4 v136, s[44:47], s54 offen nt lds\n"
"buffer_load_dwordx4 v137, s[44:47], s54 offen offset:1024 nt lds\n"
"buffer_load_dwordx4 v138, s[44:47], s54 offen offset:2048 nt lds\n"
"buffer_load_dwordx4 v139, s[44:47], s54 offen offset:3072 nt lds\n"
"s_add_u32 s54, s54, 4096\n"
"s_add_u32 s40, s20, s58\n"
"s_addc_u32 s41, s21, 0\n"
"s_sub_u32 s40, s40, 4096\n"
"s_subb_u32 s41, s41, 0\n"
"s_and_b32 s41, s41, 0xffff\n"
"s_mov_b32 s42, 266240\n"
"s_mov_b32 s43, 0x20000\n"
"s_add_u32 s48, s22, s59\n"
"s_addc_u32 s49, s23, 0\n"
"s_and_b32 s49, s49, 0xffff\n"
"s_mov_b32 s50, 94208\n"
"s_mov_b32 s51, 0x20000\n"
"v_and_b32 v129, 15, v128\n"
"v_lshrrev_b32 v130, 4, v128\n"
"v_bfe_u32 v131, v128, 1, 3\n"
"v_xor_b32 v131, v131, v130\n"
"v_lshlrev_b32 v131, 4, v131\n"
"v_and_b32 v132, 7, v128\n"
"v_lshl_add_u32 v131, v132, 7, v131\n"
"v_bfe_u32 v132, v128, 3, 1\n"
"v_lshl_add_u32 v244, v132, 12, v131\n"
"v_xor_b32 v245, 64, v244\n"
"v_lshl_add_u32 v134, v132, 11, v131\n"
"v_add_u32 v134, s53, v134\n"
"v_add_u32 v134, 65536, v134\n"
"v_xor_b32 v135, 64, v134\n"
"v_lshrrev_b32 v131, 5, v128\n"
"v_lshlrev_b32 v131, 12, v131\n"
"v_bfe_u32 v132, v128, 4, 1\n"
"v_lshl_add_u32 v131, v132, 8, v131\n"
"v_lshl_add_u32 v131, v132, 5, v131\n"
"v_bfe_u32 v132, v128, 2, 2\n"
"v_lshl_add_u32 v131, v132, 6, v131\n"
"v_and_b32 v132, 3, v128\n"
"v_lshl_add_u32 v246, v132, 3, v131\n"
"v_xor_b32 v247, 32, v246\n"
"v_mov_b32 v132, 0x3f803f80\n"
"v_and_b32 v129, 1, v128\n"
"v_mul_u32_u24 v129, 0xaaaa, v129\n"
"v_add_u32 v252, 0x4444, v129\n"
"v_accvgpr_write_b32 a248, v132\n"
"v_accvgpr_write_b32 a249, v132\n"
"v_accvgpr_write_b32 a250, v132\n"
"v_accvgpr_write_b32 a251, v132\n"
"v_accvgpr_write_b32 a224, 0\n"
"v_accvgpr_write_b32 a225, 0\n"
"v_accvgpr_write_b32 a226, 0\n"
"v_accvgpr_write_b32 a227, 0\n"
"v_accvgpr_write_b32 a228, 0\n"
"v_accvgpr_write_b32 a229, 0\n"
"v_accvgpr_write_b32 a230, 0\n"
"v_accvgpr_write_b32 a231, 0\n"
"v_accvgpr_write_b32 a232, 0\n"
"v_accvgpr_write_b32 a233, 0\n"
"v_accvgpr_write_b32 a234, 0\n"
"v_accvgpr_write_b32 a235, 0\n"
"v_accvgpr_write_b32 a236, 0\n"
"v_accvgpr_write_b32 a237, 0\n"
"v_accvgpr_write_b32 a238, 0\n"
"v_accvgpr_write_b32 a239, 0\n"
"v_accvgpr_write_b32 a240, 0\n"
"v_accvgpr_write_b32 a241, 0\n"
"v_accvgpr_write_b32 a242, 0\n"
"v_accvgpr_write_b32 a243, 0\n"
"v_accvgpr_write_b32 a244, 0\n"
"v_accvgpr_write_b32 a245, 0\n"
"v_accvgpr_write_b32 a246, 0\n"
"v_accvgpr_write_b32 a247, 0\n"
"s_mov_b32 s56, 0x3e0293ee\n"
"s_mov_b32 s57, 0x3e0293ee\n"
"v_mov_b32 v255, 0\n"
"s_lshl_b32 s55, %[wid], 2\n"
"s_add_u32 s55, s55, 161792\n"
"v_mov_b32 v129, s55\n"
"ds_write_b32 v129, v255\n"
"s_mov_b32 s27, 0\n"
"s_waitcnt lgkmcnt(0)\n"
"s_cmp_eq_u32 %[wid], 3\n"
"s_cbranch_scc1 L_w3_%=\n"
"s_waitcnt vmcnt(4)\n"
"s_barrier\n"
"ds_read_b128 a[96:99], v244 offset:0\n"
"ds_read_b128 a[100:103], v245 offset:0\n"
"ds_read_b128 a[104:107], v244 offset:1024\n"
"ds_read_b128 a[108:111], v245 offset:1024\n"
"ds_read_b128 a[112:115], v245 offset:9216\n"
"ds_read_b128 a[116:119], v244 offset:9216\n"
"ds_read_b128 a[120:123], v245 offset:8192\n"
"ds_read_b128 a[124:127], v244 offset:8192\n"
"ds_read_b128 a[128:131], v244 offset:16384\n"
"ds_read_b128 a[132:135], v245 offset:16384\n"
"ds_read_b128 a[136:139], v244 offset:17408\n"
"ds_read_b128 a[140:143], v245 offset:17408\n"
"ds_read_b128 a[144:147], v245 offset:25600\n"
"ds_read_b128 a[148:151], v244 offset:25600\n"
"ds_read_b128 a[152:155], v245 offset:24576\n"
"ds_read_b128 a[156:159], v244 offset:24576\n"
"s_add_u32 m0, s53, 81920\n"
"s_nop 0\n"
"buffer_load_dwordx4 v136, s[44:47], s54 offen nt lds\n"
"buffer_load_dwordx4 v137, s[44:47], s54 offen offset:1024 nt lds\n"
"buffer_load_dwordx4 v138, s[44:47], s54 offen offset:2048 nt lds\n"
"buffer_load_dwordx4 v139, s[44:47], s54 offen offset:3072 nt lds\n"
"s_add_u32 s54, s54, 4096\n"
"s_waitcnt vmcnt(4)\n"
"s_mov_b32 m0, s53\n"
"s_add_u32 s24, s52, 0\n"
"buffer_load_dwordx4 v250, s[40:43], s24 offen offset:2048 nt lds\n"
"buffer_load_dwordx4 v251, s[40:43], s24 offen offset:3072 nt lds\n"
"s_add_u32 m0, s53, 16384\n"
"s_add_u32 s24, s52, 8192\n"
"buffer_load_dwordx4 v250, s[40:43], s24 offen offset:2048 nt lds\n"
"buffer_load_dwordx4 v251, s[40:43], s24 offen offset:3072 nt lds\n"
"ds_read_b128 v[160:163], v134 offset:0\n"
"ds_read_b128 v[164:167], v135 offset:0\n"
"ds_read_b128 v[168:171], v134 offset:1024\n"
"ds_read_b128 v[172:175], v135 offset:1024\n"
"s_waitcnt lgkmcnt(3)\n"
"v_lshlrev_b32 v176, 16, v160\n"
"v_and_b32 v177, 0xffff0000, v160\n"
"v_lshlrev_b32 v178, 16, v161\n"
"v_and_b32 v179, 0xffff0000, v161\n"
"v_lshlrev_b32 v180, 16, v162\n"
"v_and_b32 v181, 0xffff0000, v162\n"
"v_lshlrev_b32 v182, 16, v163\n"
"v_and_b32 v183, 0xffff0000, v163\n"
"v_pk_mul_f32 v[176:177], v[176:177], s[56:57]\n"
"v_pk_mul_f32 v[178:179], v[178:179], s[56:57]\n"
"v_pk_mul_f32 v[180:181], v[180:181], s[56:57]\n"
"v_pk_mul_f32 v[182:183], v[182:183], s[56:57]\n"
"v_cvt_pk_bf16_f32 v160, v176, v177\n"
"v_cvt_pk_bf16_f32 v161, v178, v179\n"
"v_cvt_pk_bf16_f32 v162, v180, v181\n"
"v_cvt_pk_bf16_f32 v163, v182, v183\n"
"v_accvgpr_write_b32 a0, v160\n"
"v_accvgpr_write_b32 a1, v161\n"
"v_accvgpr_write_b32 a2, v162\n"
"v_accvgpr_write_b32 a3, v163\n"
"s_waitcnt lgkmcnt(2)\n"
"v_lshlrev_b32 v184, 16, v164\n"
"v_and_b32 v185, 0xffff0000, v164\n"
"v_lshlrev_b32 v186, 16, v165\n"
"v_and_b32 v187, 0xffff0000, v165\n"
"v_lshlrev_b32 v188, 16, v166\n"
"v_and_b32 v189, 0xffff0000, v166\n"
"v_lshlrev_b32 v190, 16, v167\n"
"v_and_b32 v191, 0xffff0000, v167\n"
"v_pk_mul_f32 v[184:185], v[184:185], s[56:57]\n"
"v_pk_mul_f32 v[186:187], v[186:187], s[56:57]\n"
"v_pk_mul_f32 v[188:189], v[188:189], s[56:57]\n"
"v_pk_mul_f32 v[190:191], v[190:191], s[56:57]\n"
"v_cvt_pk_bf16_f32 v164, v184, v185\n"
"v_cvt_pk_bf16_f32 v165, v186, v187\n"
"v_cvt_pk_bf16_f32 v166, v188, v189\n"
"v_cvt_pk_bf16_f32 v167, v190, v191\n"
"v_accvgpr_write_b32 a4, v164\n"
"v_accvgpr_write_b32 a5, v165\n"
"v_accvgpr_write_b32 a6, v166\n"
"v_accvgpr_write_b32 a7, v167\n"
"s_waitcnt lgkmcnt(1)\n"
"v_lshlrev_b32 v176, 16, v168\n"
"v_and_b32 v177, 0xffff0000, v168\n"
"v_lshlrev_b32 v178, 16, v169\n"
"v_and_b32 v179, 0xffff0000, v169\n"
"v_lshlrev_b32 v180, 16, v170\n"
"v_and_b32 v181, 0xffff0000, v170\n"
"v_lshlrev_b32 v182, 16, v171\n"
"v_and_b32 v183, 0xffff0000, v171\n"
"v_pk_mul_f32 v[176:177], v[176:177], s[56:57]\n"
"v_pk_mul_f32 v[178:179], v[178:179], s[56:57]\n"
"v_pk_mul_f32 v[180:181], v[180:181], s[56:57]\n"
"v_pk_mul_f32 v[182:183], v[182:183], s[56:57]\n"
"v_cvt_pk_bf16_f32 v168, v176, v177\n"
"v_cvt_pk_bf16_f32 v169, v178, v179\n"
"v_cvt_pk_bf16_f32 v170, v180, v181\n"
"v_cvt_pk_bf16_f32 v171, v182, v183\n"
"v_accvgpr_write_b32 a8, v168\n"
"v_accvgpr_write_b32 a9, v169\n"
"v_accvgpr_write_b32 a10, v170\n"
"v_accvgpr_write_b32 a11, v171\n"
"s_waitcnt lgkmcnt(0)\n"
"v_lshlrev_b32 v184, 16, v172\n"
"v_and_b32 v185, 0xffff0000, v172\n"
"v_lshlrev_b32 v186, 16, v173\n"
"v_and_b32 v187, 0xffff0000, v173\n"
"v_lshlrev_b32 v188, 16, v174\n"
"v_and_b32 v189, 0xffff0000, v174\n"
"v_lshlrev_b32 v190, 16, v175\n"
"v_and_b32 v191, 0xffff0000, v175\n"
"v_pk_mul_f32 v[184:185], v[184:185], s[56:57]\n"
"v_pk_mul_f32 v[186:187], v[186:187], s[56:57]\n"
"v_pk_mul_f32 v[188:189], v[188:189], s[56:57]\n"
"v_pk_mul_f32 v[190:191], v[190:191], s[56:57]\n"
"v_cvt_pk_bf16_f32 v172, v184, v185\n"
"v_cvt_pk_bf16_f32 v173, v186, v187\n"
"v_cvt_pk_bf16_f32 v174, v188, v189\n"
"v_cvt_pk_bf16_f32 v175, v190, v191\n"
"v_accvgpr_write_b32 a12, v172\n"
"v_accvgpr_write_b32 a13, v173\n"
"v_accvgpr_write_b32 a14, v174\n"
"v_accvgpr_write_b32 a15, v175\n"
"v_mfma_f32_16x16x32_bf16 v[192:195], a[96:99], a[0:3], 0\n"
"v_mfma_f32_16x16x32_bf16 v[192:195], a[100:103], a[4:7], v[192:195]\n"
"v_mfma_f32_16x16x32_bf16 v[192:195], a[104:107], a[8:11], v[192:195]\n"
"v_mfma_f32_16x16x32_bf16 v[192:195], a[108:111], a[12:15], v[192:195]\n"
"v_mfma_f32_16x16x32_bf16 v[196:199], a[112:115], a[12:15], 0\n"
"v_mfma_f32_16x16x32_bf16 v[196:199], a[116:119], a[8:11], v[196:199]\n"
"v_mfma_f32_16x16x32_bf16 v[196:199], a[120:123], a[4:7], v[196:199]\n"
"v_mfma_f32_16x16x32_bf16 v[196:199], a[124:127], a[0:3], v[196:199]\n"
"v_mfma_f32_16x16x32_bf16 v[200:203], a[128:131], a[0:3], 0\n"
"v_mfma_f32_16x16x32_bf16 v[200:203], a[132:135], a[4:7], v[200:203]\n"
"v_mfma_f32_16x16x32_bf16 v[200:203], a[136:139], a[8:11], v[200:203]\n"
"v_mfma_f32_16x16x32_bf16 v[200:203], a[140:143], a[12:15], v[200:203]\n"
"v_mfma_f32_16x16x32_bf16 v[204:207], a[144:147], a[12:15], 0\n"
"v_mfma_f32_16x16x32_bf16 v[204:207], a[148:151], a[8:11], v[204:207]\n"
"v_mfma_f32_16x16x32_bf16 v[204:207], a[152:155], a[4:7], v[204:207]\n"
"v_mfma_f32_16x16x32_bf16 v[204:207], a[156:159], a[0:3], v[204:207]\n"
"v_max3_f32 v253, v192, v193, v220\n"
"v_max3_f32 v254, v194, v195, v196\n"
"v_max3_f32 v253, v253, v197, v198\n"
"s_nop 0\n"
"v_max3_f32 v254, v254, v199, v200\n"
"v_max3_f32 v253, v253, v201, v202\n"
"s_nop 1\n"
"v_max3_f32 v254, v254, v203, v204\n"
"v_max3_f32 v253, v253, v205, v206\n"
"v_max3_f32 v253, v253, v254, v207\n"
"v_mov_b32 v254, v253\n"
"s_nop 1\n"
"v_permlane32_swap_b32 v253, v254\n"
"v_max_f32 v253, v253, v254\n"
"v_mov_b32 v254, v253\n"
"s_nop 1\n"
"v_permlane16_swap_b32 v253, v254\n"
"v_max_f32 v253, v253, v254\n"
"v_sub_f32 v192, v192, v253\n"
"v_sub_f32 v193, v193, v253\n"
"v_sub_f32 v194, v194, v253\n"
"v_sub_f32 v195, v195, v253\n"
"v_sub_f32 v196, v196, v253\n"
"v_sub_f32 v197, v197, v253\n"
"v_sub_f32 v198, v198, v253\n"
"v_sub_f32 v199, v199, v253\n"
"v_sub_f32 v200, v200, v253\n"
"v_sub_f32 v201, v201, v253\n"
"v_sub_f32 v202, v202, v253\n"
"v_sub_f32 v203, v203, v253\n"
"v_sub_f32 v204, v204, v253\n"
"v_sub_f32 v205, v205, v253\n"
"v_sub_f32 v206, v206, v253\n"
"v_sub_f32 v207, v207, v253\n"
"v_sub_f32 v220, 0, v253\n"
"v_sub_f32 v221, 0, v253\n"
"v_sub_f32 v222, 0, v253\n"
"v_sub_f32 v223, 0, v253\n"
"v_exp_f32 v192, v192\n"
"v_exp_f32 v193, v193\n"
"v_exp_f32 v194, v194\n"
"v_exp_f32 v195, v195\n"
"v_cvt_pk_bf16_f32 v208, v192, v193\n"
"v_exp_f32 v196, v196\n"
"v_exp_f32 v197, v197\n"
"v_cvt_pk_bf16_f32 v209, v194, v195\n"
"v_exp_f32 v198, v198\n"
"v_exp_f32 v199, v199\n"
"v_cvt_pk_bf16_f32 v210, v196, v197\n"
"v_exp_f32 v200, v200\n"
"v_cvt_pk_bf16_f32 v211, v198, v199\n"
"v_exp_f32 v201, v201\n"
"v_exp_f32 v202, v202\n"
"v_exp_f32 v203, v203\n"
"s_waitcnt vmcnt(4)\n"
"s_add_u32 m0, s53, 98304\n"
"s_nop 0\n"
"buffer_load_dwordx4 v136, s[44:47], s54 offen nt lds\n"
"buffer_load_dwordx4 v137, s[44:47], s54 offen offset:1024 nt lds\n"
"buffer_load_dwordx4 v138, s[44:47], s54 offen offset:2048 nt lds\n"
"buffer_load_dwordx4 v139, s[44:47], s54 offen offset:3072 nt lds\n"
"s_add_u32 s54, s54, 4096\n"
"ds_read_b128 v[160:163], v134 offset:16384\n"
"ds_read_b128 v[164:167], v135 offset:16384\n"
"ds_read_b128 v[168:171], v134 offset:17408\n"
"ds_read_b128 v[172:175], v135 offset:17408\n"
"s_waitcnt lgkmcnt(3)\n"
"v_lshlrev_b32 v176, 16, v160\n"
"v_and_b32 v177, 0xffff0000, v160\n"
"v_lshlrev_b32 v178, 16, v161\n"
"v_and_b32 v179, 0xffff0000, v161\n"
"v_lshlrev_b32 v180, 16, v162\n"
"v_and_b32 v181, 0xffff0000, v162\n"
"v_lshlrev_b32 v182, 16, v163\n"
"v_and_b32 v183, 0xffff0000, v163\n"
"v_pk_mul_f32 v[176:177], v[176:177], s[56:57]\n"
"v_pk_mul_f32 v[178:179], v[178:179], s[56:57]\n"
"v_pk_mul_f32 v[180:181], v[180:181], s[56:57]\n"
"v_pk_mul_f32 v[182:183], v[182:183], s[56:57]\n"
"v_cvt_pk_bf16_f32 v160, v176, v177\n"
"v_cvt_pk_bf16_f32 v161, v178, v179\n"
"v_cvt_pk_bf16_f32 v162, v180, v181\n"
"v_cvt_pk_bf16_f32 v163, v182, v183\n"
"v_accvgpr_write_b32 a16, v160\n"
"v_accvgpr_write_b32 a17, v161\n"
"v_accvgpr_write_b32 a18, v162\n"
"v_accvgpr_write_b32 a19, v163\n"
"s_waitcnt lgkmcnt(2)\n"
"v_lshlrev_b32 v184, 16, v164\n"
"v_and_b32 v185, 0xffff0000, v164\n"
"v_lshlrev_b32 v186, 16, v165\n"
"v_and_b32 v187, 0xffff0000, v165\n"
"v_lshlrev_b32 v188, 16, v166\n"
"v_and_b32 v189, 0xffff0000, v166\n"
"v_lshlrev_b32 v190, 16, v167\n"
"v_and_b32 v191, 0xffff0000, v167\n"
"v_pk_mul_f32 v[184:185], v[184:185], s[56:57]\n"
"v_pk_mul_f32 v[186:187], v[186:187], s[56:57]\n"
"v_pk_mul_f32 v[188:189], v[188:189], s[56:57]\n"
"v_pk_mul_f32 v[190:191], v[190:191], s[56:57]\n"
"v_cvt_pk_bf16_f32 v164, v184, v185\n"
"v_cvt_pk_bf16_f32 v165, v186, v187\n"
"v_cvt_pk_bf16_f32 v166, v188, v189\n"
"v_cvt_pk_bf16_f32 v167, v190, v191\n"
"v_accvgpr_write_b32 a20, v164\n"
"v_accvgpr_write_b32 a21, v165\n"
"v_accvgpr_write_b32 a22, v166\n"
"v_accvgpr_write_b32 a23, v167\n"
"s_waitcnt lgkmcnt(1)\n"
"v_lshlrev_b32 v176, 16, v168\n"
"v_and_b32 v177, 0xffff0000, v168\n"
"v_lshlrev_b32 v178, 16, v169\n"
"v_and_b32 v179, 0xffff0000, v169\n"
"v_lshlrev_b32 v180, 16, v170\n"
"v_and_b32 v181, 0xffff0000, v170\n"
"v_lshlrev_b32 v182, 16, v171\n"
"v_and_b32 v183, 0xffff0000, v171\n"
"v_pk_mul_f32 v[176:177], v[176:177], s[56:57]\n"
"v_pk_mul_f32 v[178:179], v[178:179], s[56:57]\n"
"v_pk_mul_f32 v[180:181], v[180:181], s[56:57]\n"
"v_pk_mul_f32 v[182:183], v[182:183], s[56:57]\n"
"v_cvt_pk_bf16_f32 v168, v176, v177\n"
"v_cvt_pk_bf16_f32 v169, v178, v179\n"
"v_cvt_pk_bf16_f32 v170, v180, v181\n"
"v_cvt_pk_bf16_f32 v171, v182, v183\n"
"v_accvgpr_write_b32 a24, v168\n"
"v_accvgpr_write_b32 a25, v169\n"
"v_accvgpr_write_b32 a26, v170\n"
"v_accvgpr_write_b32 a27, v171\n"
"s_waitcnt lgkmcnt(0)\n"
"v_lshlrev_b32 v184, 16, v172\n"
"v_and_b32 v185, 0xffff0000, v172\n"
"v_lshlrev_b32 v186, 16, v173\n"
"v_and_b32 v187, 0xffff0000, v173\n"
"v_lshlrev_b32 v188, 16, v174\n"
"v_and_b32 v189, 0xffff0000, v174\n"
"v_lshlrev_b32 v190, 16, v175\n"
"v_and_b32 v191, 0xffff0000, v175\n"
"v_pk_mul_f32 v[184:185], v[184:185], s[56:57]\n"
"v_pk_mul_f32 v[186:187], v[186:187], s[56:57]\n"
"v_pk_mul_f32 v[188:189], v[188:189], s[56:57]\n"
"v_pk_mul_f32 v[190:191], v[190:191], s[56:57]\n"
"v_cvt_pk_bf16_f32 v172, v184, v185\n"
"v_cvt_pk_bf16_f32 v173, v186, v187\n"
"v_cvt_pk_bf16_f32 v174, v188, v189\n"
"v_cvt_pk_bf16_f32 v175, v190, v191\n"
"v_accvgpr_write_b32 a28, v172\n"
"v_accvgpr_write_b32 a29, v173\n"
"v_accvgpr_write_b32 a30, v174\n"
"v_accvgpr_write_b32 a31, v175\n"
"v_mfma_f32_16x16x32_bf16 v[192:195], a[96:99], a[16:19], 0\n"
"v_exp_f32 v204, v204\n"
"v_mfma_f32_16x16x32_bf16 v[192:195], a[100:103], a[20:23], v[192:195]\n"
"v_cvt_pk_bf16_f32 v212, v200, v201\n"
"v_cvt_pk_bf16_f32 v213, v202, v203\n"
"v_mfma_f32_16x16x32_bf16 v[192:195], a[104:107], a[24:27], v[192:195]\n"
"v_exp_f32 v205, v205\n"
"v_mfma_f32_16x16x32_bf16 v[192:195], a[108:111], a[28:31], v[192:195]\n"
"v_cvt_pk_bf16_f32 v214, v204, v205\n"
"v_mfma_f32_16x16x32_bf16 v[196:199], a[112:115], a[28:31], 0\n"
"v_exp_f32 v206, v206\n"
"v_mfma_f32_16x16x32_bf16 v[196:199], a[116:119], a[24:27], v[196:199]\n"
"v_mfma_f32_16x16x32_bf16 v[196:199], a[120:123], a[20:23], v[196:199]\n"
"v_exp_f32 v207, v207\n"
"v_mfma_f32_16x16x32_bf16 v[196:199], a[124:127], a[16:19], v[196:199]\n"
"v_cvt_pk_bf16_f32 v215, v206, v207\n"
"s_waitcnt vmcnt(4)\n"
"s_barrier\n"
"ds_read_b64_tr_b16 a[160:161], v246 offset:2048\n"
"ds_read_b64_tr_b16 a[162:163], v246 offset:10240\n"
"ds_read_b64_tr_b16 a[164:165], v246 offset:18432\n"
"ds_read_b64_tr_b16 a[166:167], v246 offset:26624\n"
"ds_read_b64_tr_b16 a[168:169], v247 offset:18432\n"
"ds_read_b64_tr_b16 a[170:171], v247 offset:26624\n"
"ds_read_b64_tr_b16 a[172:173], v247 offset:2048\n"
"ds_read_b64_tr_b16 a[174:175], v247 offset:10240\n"
"ds_read_b64_tr_b16 a[176:177], v246 offset:2560\n"
"ds_read_b64_tr_b16 a[178:179], v246 offset:10752\n"
"ds_read_b64_tr_b16 a[180:181], v246 offset:18944\n"
"ds_read_b64_tr_b16 a[182:183], v246 offset:27136\n"
"ds_read_b64_tr_b16 a[184:185], v247 offset:18944\n"
"ds_read_b64_tr_b16 a[186:187], v247 offset:27136\n"
"ds_read_b64_tr_b16 a[188:189], v247 offset:2560\n"
"ds_read_b64_tr_b16 a[190:191], v247 offset:10752\n"
"ds_read_b64_tr_b16 a[192:193], v246 offset:3072\n"
"ds_read_b64_tr_b16 a[194:195], v246 offset:11264\n"
"ds_read_b64_tr_b16 a[196:197], v246 offset:19456\n"
"ds_read_b64_tr_b16 a[198:199], v246 offset:27648\n"
"ds_read_b64_tr_b16 a[200:201], v247 offset:19456\n"
"ds_read_b64_tr_b16 a[202:203], v247 offset:27648\n"
"ds_read_b64_tr_b16 a[204:205], v247 offset:3072\n"
"ds_read_b64_tr_b16 a[206:207], v247 offset:11264\n"
"ds_read_b64_tr_b16 a[208:209], v246 offset:3584\n"
"ds_read_b64_tr_b16 a[210:211], v246 offset:11776\n"
"ds_read_b64_tr_b16 a[212:213], v246 offset:19968\n"
"ds_read_b64_tr_b16 a[214:215], v246 offset:28160\n"
"ds_read_b64_tr_b16 a[216:217], v247 offset:19968\n"
"ds_read_b64_tr_b16 a[218:219], v247 offset:28160\n"
"ds_read_b64_tr_b16 a[220:221], v247 offset:3584\n"
"ds_read_b64_tr_b16 a[222:223], v247 offset:11776\n"
"s_add_u32 m0, s53, 114688\n"
"s_nop 0\n"
"buffer_load_dwordx4 v136, s[44:47], s54 offen nt lds\n"
"buffer_load_dwordx4 v137, s[44:47], s54 offen offset:1024 nt lds\n"
"buffer_load_dwordx4 v138, s[44:47], s54 offen offset:2048 nt lds\n"
"buffer_load_dwordx4 v139, s[44:47], s54 offen offset:3072 nt lds\n"
"s_add_u32 s54, s54, 4096\n"
"v_mfma_f32_16x16x32_bf16 v[200:203], a[128:131], a[16:19], 0\n"
"v_mfma_f32_16x16x32_bf16 v[200:203], a[132:135], a[20:23], v[200:203]\n"
"v_mfma_f32_16x16x32_bf16 v[200:203], a[136:139], a[24:27], v[200:203]\n"
"v_mfma_f32_16x16x32_bf16 v[200:203], a[140:143], a[28:31], v[200:203]\n"
"v_mfma_f32_16x16x32_bf16 v[204:207], a[144:147], a[28:31], 0\n"
"v_mfma_f32_16x16x32_bf16 v[204:207], a[148:151], a[24:27], v[204:207]\n"
"v_mfma_f32_16x16x32_bf16 v[204:207], a[152:155], a[20:23], v[204:207]\n"
"v_max3_f32 v253, v192, v193, v224\n"
"v_max3_f32 v254, v194, v195, v196\n"
"v_mfma_f32_16x16x32_bf16 v[204:207], a[156:159], a[16:19], v[204:207]\n"
"v_max3_f32 v253, v253, v197, v198\n"
"s_waitcnt lgkmcnt(15)\n"
"v_smfmac_f32_16x16x64_bf16 a[224:227], a[248:251], v[208:215], v252\n"
"v_max3_f32 v254, v254, v199, v200\n"
"v_mfma_f32_16x16x32_bf16 v[0:3], v[208:211], a[160:163], 0\n"
"v_max3_f32 v253, v253, v201, v202\n"
"v_mfma_f32_16x16x32_bf16 v[0:3], v[212:215], a[164:167], v[0:3]\n"
"v_mfma_f32_16x16x32_bf16 v[4:7], v[212:215], a[168:171], 0\n"
"v_max3_f32 v254, v254, v203, v204\n"
"v_max3_f32 v253, v253, v205, v206\n"
"v_max3_f32 v253, v253, v254, v207\n"
"v_mov_b32 v254, v253\n"
"v_mfma_f32_16x16x32_bf16 v[4:7], v[208:211], a[172:175], v[4:7]\n"
"s_nop 0\n"
"v_permlane32_swap_b32 v253, v254\n"
"v_max_f32 v253, v253, v254\n"
"v_mov_b32 v254, v253\n"
"v_mfma_f32_16x16x32_bf16 v[8:11], v[208:211], a[176:179], 0\n"
"s_nop 0\n"
"v_permlane16_swap_b32 v253, v254\n"
"v_max_f32 v253, v253, v254\n"
"v_sub_f32 v192, v192, v253\n"
"v_mfma_f32_16x16x32_bf16 v[8:11], v[212:215], a[180:183], v[8:11]\n"
"v_sub_f32 v193, v193, v253\n"
"v_sub_f32 v194, v194, v253\n"
"v_sub_f32 v195, v195, v253\n"
"v_mfma_f32_16x16x32_bf16 v[12:15], v[212:215], a[184:187], 0\n"
"v_sub_f32 v196, v196, v253\n"
"v_sub_f32 v197, v197, v253\n"
"v_sub_f32 v198, v198, v253\n"
"v_sub_f32 v199, v199, v253\n"
"s_waitcnt lgkmcnt(8)\n"
"v_mfma_f32_16x16x32_bf16 v[12:15], v[208:211], a[188:191], v[12:15]\n"
"v_sub_f32 v200, v200, v253\n"
"v_sub_f32 v201, v201, v253\n"
"v_sub_f32 v202, v202, v253\n"
"v_mfma_f32_16x16x32_bf16 v[16:19], v[208:211], a[192:195], 0\n"
"v_sub_f32 v203, v203, v253\n"
"v_sub_f32 v204, v204, v253\n"
"v_sub_f32 v205, v205, v253\n"
"v_mfma_f32_16x16x32_bf16 v[16:19], v[212:215], a[196:199], v[16:19]\n"
"v_sub_f32 v206, v206, v253\n"
"v_sub_f32 v207, v207, v253\n"
"v_sub_f32 v224, 0, v253\n"
"v_mfma_f32_16x16x32_bf16 v[20:23], v[212:215], a[200:203], 0\n"
"v_sub_f32 v225, 0, v253\n"
"v_sub_f32 v226, 0, v253\n"
"v_sub_f32 v227, 0, v253\n"
"s_waitcnt lgkmcnt(0)\n"
"v_mfma_f32_16x16x32_bf16 v[20:23], v[208:211], a[204:207], v[20:23]\n"
"v_exp_f32 v192, v192\n"
"v_exp_f32 v193, v193\n"
"v_exp_f32 v194, v194\n"
"v_exp_f32 v195, v195\n"
"v_mfma_f32_16x16x32_bf16 v[24:27], v[208:211], a[208:211], 0\n"
"v_cvt_pk_bf16_f32 v216, v192, v193\n"
"v_exp_f32 v196, v196\n"
"v_exp_f32 v197, v197\n"
"v_mfma_f32_16x16x32_bf16 v[24:27], v[212:215], a[212:215], v[24:27]\n"
"v_cvt_pk_bf16_f32 v217, v194, v195\n"
"v_exp_f32 v198, v198\n"
"v_exp_f32 v199, v199\n"
"v_mfma_f32_16x16x32_bf16 v[28:31], v[212:215], a[216:219], 0\n"
"v_cvt_pk_bf16_f32 v218, v196, v197\n"
"v_exp_f32 v200, v200\n"
"v_cvt_pk_bf16_f32 v219, v198, v199\n"
"v_mfma_f32_16x16x32_bf16 v[28:31], v[208:211], a[220:223], v[28:31]\n"
"v_exp_f32 v201, v201\n"
"v_exp_f32 v202, v202\n"
"v_exp_f32 v203, v203\n"
"s_waitcnt vmcnt(4)\n"
"s_add_u32 m0, s53, 131072\n"
"s_nop 0\n"
"buffer_load_dwordx4 v136, s[44:47], s54 offen nt lds\n"
"buffer_load_dwordx4 v137, s[44:47], s54 offen offset:1024 nt lds\n"
"buffer_load_dwordx4 v138, s[44:47], s54 offen offset:2048 nt lds\n"
"buffer_load_dwordx4 v139, s[44:47], s54 offen offset:3072 nt lds\n"
"s_add_u32 s54, s54, 4096\n"
"ds_read_b128 v[160:163], v134 offset:32768\n"
"ds_read_b128 v[164:167], v135 offset:32768\n"
"ds_read_b128 v[168:171], v134 offset:33792\n"
"ds_read_b128 v[172:175], v135 offset:33792\n"
"s_waitcnt lgkmcnt(3)\n"
"v_lshlrev_b32 v176, 16, v160\n"
"v_and_b32 v177, 0xffff0000, v160\n"
"v_lshlrev_b32 v178, 16, v161\n"
"v_and_b32 v179, 0xffff0000, v161\n"
"v_lshlrev_b32 v180, 16, v162\n"
"v_and_b32 v181, 0xffff0000, v162\n"
"v_lshlrev_b32 v182, 16, v163\n"
"v_and_b32 v183, 0xffff0000, v163\n"
"v_pk_mul_f32 v[176:177], v[176:177], s[56:57]\n"
"v_pk_mul_f32 v[178:179], v[178:179], s[56:57]\n"
"v_pk_mul_f32 v[180:181], v[180:181], s[56:57]\n"
"v_pk_mul_f32 v[182:183], v[182:183], s[56:57]\n"
"v_cvt_pk_bf16_f32 v160, v176, v177\n"
"v_cvt_pk_bf16_f32 v161, v178, v179\n"
"v_cvt_pk_bf16_f32 v162, v180, v181\n"
"v_cvt_pk_bf16_f32 v163, v182, v183\n"
"v_accvgpr_write_b32 a32, v160\n"
"v_accvgpr_write_b32 a33, v161\n"
"v_accvgpr_write_b32 a34, v162\n"
"v_accvgpr_write_b32 a35, v163\n"
"s_waitcnt lgkmcnt(2)\n"
"v_lshlrev_b32 v184, 16, v164\n"
"v_and_b32 v185, 0xffff0000, v164\n"
"v_lshlrev_b32 v186, 16, v165\n"
"v_and_b32 v187, 0xffff0000, v165\n"
"v_lshlrev_b32 v188, 16, v166\n"
"v_and_b32 v189, 0xffff0000, v166\n"
"v_lshlrev_b32 v190, 16, v167\n"
"v_and_b32 v191, 0xffff0000, v167\n"
"v_pk_mul_f32 v[184:185], v[184:185], s[56:57]\n"
"v_pk_mul_f32 v[186:187], v[186:187], s[56:57]\n"
"v_pk_mul_f32 v[188:189], v[188:189], s[56:57]\n"
"v_pk_mul_f32 v[190:191], v[190:191], s[56:57]\n"
"v_cvt_pk_bf16_f32 v164, v184, v185\n"
"v_cvt_pk_bf16_f32 v165, v186, v187\n"
"v_cvt_pk_bf16_f32 v166, v188, v189\n"
"v_cvt_pk_bf16_f32 v167, v190, v191\n"
"v_accvgpr_write_b32 a36, v164\n"
"v_accvgpr_write_b32 a37, v165\n"
"v_accvgpr_write_b32 a38, v166\n"
"v_accvgpr_write_b32 a39, v167\n"
"s_waitcnt lgkmcnt(1)\n"
"v_lshlrev_b32 v176, 16, v168\n"
"v_and_b32 v177, 0xffff0000, v168\n"
"v_lshlrev_b32 v178, 16, v169\n"
"v_and_b32 v179, 0xffff0000, v169\n"
"v_lshlrev_b32 v180, 16, v170\n"
"v_and_b32 v181, 0xffff0000, v170\n"
"v_lshlrev_b32 v182, 16, v171\n"
"v_and_b32 v183, 0xffff0000, v171\n"
"v_pk_mul_f32 v[176:177], v[176:177], s[56:57]\n"
"v_pk_mul_f32 v[178:179], v[178:179], s[56:57]\n"
"v_pk_mul_f32 v[180:181], v[180:181], s[56:57]\n"
"v_pk_mul_f32 v[182:183], v[182:183], s[56:57]\n"
"v_cvt_pk_bf16_f32 v168, v176, v177\n"
"v_cvt_pk_bf16_f32 v169, v178, v179\n"
"v_cvt_pk_bf16_f32 v170, v180, v181\n"
"v_cvt_pk_bf16_f32 v171, v182, v183\n"
"v_accvgpr_write_b32 a40, v168\n"
"v_accvgpr_write_b32 a41, v169\n"
"v_accvgpr_write_b32 a42, v170\n"
"v_accvgpr_write_b32 a43, v171\n"
"s_waitcnt lgkmcnt(0)\n"
"v_lshlrev_b32 v184, 16, v172\n"
"v_and_b32 v185, 0xffff0000, v172\n"
"v_lshlrev_b32 v186, 16, v173\n"
"v_and_b32 v187, 0xffff0000, v173\n"
"v_lshlrev_b32 v188, 16, v174\n"
"v_and_b32 v189, 0xffff0000, v174\n"
"v_lshlrev_b32 v190, 16, v175\n"
"v_and_b32 v191, 0xffff0000, v175\n"
"v_pk_mul_f32 v[184:185], v[184:185], s[56:57]\n"
"v_pk_mul_f32 v[186:187], v[186:187], s[56:57]\n"
"v_pk_mul_f32 v[188:189], v[188:189], s[56:57]\n"
"v_pk_mul_f32 v[190:191], v[190:191], s[56:57]\n"
"v_cvt_pk_bf16_f32 v172, v184, v185\n"
"v_cvt_pk_bf16_f32 v173, v186, v187\n"
"v_cvt_pk_bf16_f32 v174, v188, v189\n"
"v_cvt_pk_bf16_f32 v175, v190, v191\n"
"v_accvgpr_write_b32 a44, v172\n"
"v_accvgpr_write_b32 a45, v173\n"
"v_accvgpr_write_b32 a46, v174\n"
"v_accvgpr_write_b32 a47, v175\n"
"v_mfma_f32_16x16x32_bf16 v[192:195], a[96:99], a[32:35], 0\n"
"v_exp_f32 v204, v204\n"
"v_mfma_f32_16x16x32_bf16 v[192:195], a[100:103], a[36:39], v[192:195]\n"
"v_cvt_pk_bf16_f32 v212, v200, v201\n"
"v_cvt_pk_bf16_f32 v213, v202, v203\n"
"v_mfma_f32_16x16x32_bf16 v[192:195], a[104:107], a[40:43], v[192:195]\n"
"v_exp_f32 v205, v205\n"
"v_mfma_f32_16x16x32_bf16 v[192:195], a[108:111], a[44:47], v[192:195]\n"
"v_cvt_pk_bf16_f32 v214, v204, v205\n"
"v_mfma_f32_16x16x32_bf16 v[196:199], a[112:115], a[44:47], 0\n"
"v_exp_f32 v206, v206\n"
"v_mfma_f32_16x16x32_bf16 v[196:199], a[116:119], a[40:43], v[196:199]\n"
"v_mfma_f32_16x16x32_bf16 v[196:199], a[120:123], a[36:39], v[196:199]\n"
"v_exp_f32 v207, v207\n"
"v_mfma_f32_16x16x32_bf16 v[196:199], a[124:127], a[32:35], v[196:199]\n"
"v_cvt_pk_bf16_f32 v215, v206, v207\n"
"v_mfma_f32_16x16x32_bf16 v[200:203], a[128:131], a[32:35], 0\n"
"v_mfma_f32_16x16x32_bf16 v[200:203], a[132:135], a[36:39], v[200:203]\n"
"v_mfma_f32_16x16x32_bf16 v[200:203], a[136:139], a[40:43], v[200:203]\n"
"v_mfma_f32_16x16x32_bf16 v[200:203], a[140:143], a[44:47], v[200:203]\n"
"v_mfma_f32_16x16x32_bf16 v[204:207], a[144:147], a[44:47], 0\n"
"v_mfma_f32_16x16x32_bf16 v[204:207], a[148:151], a[40:43], v[204:207]\n"
"v_mfma_f32_16x16x32_bf16 v[204:207], a[152:155], a[36:39], v[204:207]\n"
"v_max3_f32 v253, v192, v193, v228\n"
"v_max3_f32 v254, v194, v195, v196\n"
"v_mfma_f32_16x16x32_bf16 v[204:207], a[156:159], a[32:35], v[204:207]\n"
"v_max3_f32 v253, v253, v197, v198\n"
"v_smfmac_f32_16x16x64_bf16 a[228:231], a[248:251], v[212:219], v252\n"
"v_max3_f32 v254, v254, v199, v200\n"
"v_mfma_f32_16x16x32_bf16 v[32:35], v[216:219], a[160:163], 0\n"
"v_max3_f32 v253, v253, v201, v202\n"
"v_mfma_f32_16x16x32_bf16 v[32:35], v[212:215], a[164:167], v[32:35]\n"
"v_mfma_f32_16x16x32_bf16 v[36:39], v[212:215], a[168:171], 0\n"
"s_nop 0\n"
"v_max3_f32 v254, v254, v203, v204\n"
"v_max3_f32 v253, v253, v205, v206\n"
"v_max3_f32 v253, v253, v254, v207\n"
"v_mov_b32 v254, v253\n"
"v_mfma_f32_16x16x32_bf16 v[36:39], v[216:219], a[172:175], v[36:39]\n"
"s_nop 0\n"
"v_permlane32_swap_b32 v253, v254\n"
"v_max_f32 v253, v253, v254\n"
"v_mov_b32 v254, v253\n"
"v_mfma_f32_16x16x32_bf16 v[40:43], v[216:219], a[176:179], 0\n"
"s_nop 0\n"
"v_permlane16_swap_b32 v253, v254\n"
"v_max_f32 v253, v253, v254\n"
"v_sub_f32 v192, v192, v253\n"
"v_mfma_f32_16x16x32_bf16 v[40:43], v[212:215], a[180:183], v[40:43]\n"
"v_sub_f32 v193, v193, v253\n"
"v_sub_f32 v194, v194, v253\n"
"v_sub_f32 v195, v195, v253\n"
"v_mfma_f32_16x16x32_bf16 v[44:47], v[212:215], a[184:187], 0\n"
"v_sub_f32 v196, v196, v253\n"
"v_sub_f32 v197, v197, v253\n"
"v_sub_f32 v198, v198, v253\n"
"v_sub_f32 v199, v199, v253\n"
"v_mfma_f32_16x16x32_bf16 v[44:47], v[216:219], a[188:191], v[44:47]\n"
"v_sub_f32 v200, v200, v253\n"
"v_sub_f32 v201, v201, v253\n"
"v_sub_f32 v202, v202, v253\n"
"v_mfma_f32_16x16x32_bf16 v[48:51], v[216:219], a[192:195], 0\n"
"v_sub_f32 v203, v203, v253\n"
"v_sub_f32 v204, v204, v253\n"
"v_sub_f32 v205, v205, v253\n"
"v_mfma_f32_16x16x32_bf16 v[48:51], v[212:215], a[196:199], v[48:51]\n"
"v_sub_f32 v206, v206, v253\n"
"v_sub_f32 v207, v207, v253\n"
"v_sub_f32 v228, 0, v253\n"
"v_mfma_f32_16x16x32_bf16 v[52:55], v[212:215], a[200:203], 0\n"
"v_sub_f32 v229, 0, v253\n"
"v_sub_f32 v230, 0, v253\n"
"v_sub_f32 v231, 0, v253\n"
"v_mfma_f32_16x16x32_bf16 v[52:55], v[216:219], a[204:207], v[52:55]\n"
"v_exp_f32 v192, v192\n"
"v_exp_f32 v193, v193\n"
"v_exp_f32 v194, v194\n"
"v_exp_f32 v195, v195\n"
"v_mfma_f32_16x16x32_bf16 v[56:59], v[216:219], a[208:211], 0\n"
"v_cvt_pk_bf16_f32 v208, v192, v193\n"
"v_exp_f32 v196, v196\n"
"v_exp_f32 v197, v197\n"
"v_mfma_f32_16x16x32_bf16 v[56:59], v[212:215], a[212:215], v[56:59]\n"
"v_cvt_pk_bf16_f32 v209, v194, v195\n"
"v_exp_f32 v198, v198\n"
"v_exp_f32 v199, v199\n"
"v_mfma_f32_16x16x32_bf16 v[60:63], v[212:215], a[216:219], 0\n"
"v_cvt_pk_bf16_f32 v210, v196, v197\n"
"v_exp_f32 v200, v200\n"
"v_cvt_pk_bf16_f32 v211, v198, v199\n"
"v_mfma_f32_16x16x32_bf16 v[60:63], v[216:219], a[220:223], v[60:63]\n"
"v_exp_f32 v201, v201\n"
"v_exp_f32 v202, v202\n"
"v_exp_f32 v203, v203\n"
"s_waitcnt vmcnt(4)\n"
"s_add_u32 m0, s53, 147456\n"
"s_nop 0\n"
"buffer_load_dwordx4 v136, s[44:47], s54 offen nt lds\n"
"buffer_load_dwordx4 v137, s[44:47], s54 offen offset:1024 nt lds\n"
"buffer_load_dwordx4 v138, s[44:47], s54 offen offset:2048 nt lds\n"
"buffer_load_dwordx4 v139, s[44:47], s54 offen offset:3072 nt lds\n"
"s_add_u32 s54, s54, 4096\n"
"ds_read_b128 v[160:163], v134 offset:49152\n"
"ds_read_b128 v[164:167], v135 offset:49152\n"
"ds_read_b128 v[168:171], v134 offset:50176\n"
"ds_read_b128 v[172:175], v135 offset:50176\n"
"s_waitcnt lgkmcnt(3)\n"
"v_lshlrev_b32 v176, 16, v160\n"
"v_and_b32 v177, 0xffff0000, v160\n"
"v_lshlrev_b32 v178, 16, v161\n"
"v_and_b32 v179, 0xffff0000, v161\n"
"v_lshlrev_b32 v180, 16, v162\n"
"v_and_b32 v181, 0xffff0000, v162\n"
"v_lshlrev_b32 v182, 16, v163\n"
"v_and_b32 v183, 0xffff0000, v163\n"
"v_pk_mul_f32 v[176:177], v[176:177], s[56:57]\n"
"v_pk_mul_f32 v[178:179], v[178:179], s[56:57]\n"
"v_pk_mul_f32 v[180:181], v[180:181], s[56:57]\n"
"v_pk_mul_f32 v[182:183], v[182:183], s[56:57]\n"
"v_cvt_pk_bf16_f32 v160, v176, v177\n"
"v_cvt_pk_bf16_f32 v161, v178, v179\n"
"v_cvt_pk_bf16_f32 v162, v180, v181\n"
"v_cvt_pk_bf16_f32 v163, v182, v183\n"
"v_accvgpr_write_b32 a48, v160\n"
"v_accvgpr_write_b32 a49, v161\n"
"v_accvgpr_write_b32 a50, v162\n"
"v_accvgpr_write_b32 a51, v163\n"
"s_waitcnt lgkmcnt(2)\n"
"v_lshlrev_b32 v184, 16, v164\n"
"v_and_b32 v185, 0xffff0000, v164\n"
"v_lshlrev_b32 v186, 16, v165\n"
"v_and_b32 v187, 0xffff0000, v165\n"
"v_lshlrev_b32 v188, 16, v166\n"
"v_and_b32 v189, 0xffff0000, v166\n"
"v_lshlrev_b32 v190, 16, v167\n"
"v_and_b32 v191, 0xffff0000, v167\n"
"v_pk_mul_f32 v[184:185], v[184:185], s[56:57]\n"
"v_pk_mul_f32 v[186:187], v[186:187], s[56:57]\n"
"v_pk_mul_f32 v[188:189], v[188:189], s[56:57]\n"
"v_pk_mul_f32 v[190:191], v[190:191], s[56:57]\n"
"v_cvt_pk_bf16_f32 v164, v184, v185\n"
"v_cvt_pk_bf16_f32 v165, v186, v187\n"
"v_cvt_pk_bf16_f32 v166, v188, v189\n"
"v_cvt_pk_bf16_f32 v167, v190, v191\n"
"v_accvgpr_write_b32 a52, v164\n"
"v_accvgpr_write_b32 a53, v165\n"
"v_accvgpr_write_b32 a54, v166\n"
"v_accvgpr_write_b32 a55, v167\n"
"s_waitcnt lgkmcnt(1)\n"
"v_lshlrev_b32 v176, 16, v168\n"
"v_and_b32 v177, 0xffff0000, v168\n"
"v_lshlrev_b32 v178, 16, v169\n"
"v_and_b32 v179, 0xffff0000, v169\n"
"v_lshlrev_b32 v180, 16, v170\n"
"v_and_b32 v181, 0xffff0000, v170\n"
"v_lshlrev_b32 v182, 16, v171\n"
"v_and_b32 v183, 0xffff0000, v171\n"
"v_pk_mul_f32 v[176:177], v[176:177], s[56:57]\n"
"v_pk_mul_f32 v[178:179], v[178:179], s[56:57]\n"
"v_pk_mul_f32 v[180:181], v[180:181], s[56:57]\n"
"v_pk_mul_f32 v[182:183], v[182:183], s[56:57]\n"
"v_cvt_pk_bf16_f32 v168, v176, v177\n"
"v_cvt_pk_bf16_f32 v169, v178, v179\n"
"v_cvt_pk_bf16_f32 v170, v180, v181\n"
"v_cvt_pk_bf16_f32 v171, v182, v183\n"
"v_accvgpr_write_b32 a56, v168\n"
"v_accvgpr_write_b32 a57, v169\n"
"v_accvgpr_write_b32 a58, v170\n"
"v_accvgpr_write_b32 a59, v171\n"
"s_waitcnt lgkmcnt(0)\n"
"v_lshlrev_b32 v184, 16, v172\n"
"v_and_b32 v185, 0xffff0000, v172\n"
"v_lshlrev_b32 v186, 16, v173\n"
"v_and_b32 v187, 0xffff0000, v173\n"
"v_lshlrev_b32 v188, 16, v174\n"
"v_and_b32 v189, 0xffff0000, v174\n"
"v_lshlrev_b32 v190, 16, v175\n"
"v_and_b32 v191, 0xffff0000, v175\n"
"v_pk_mul_f32 v[184:185], v[184:185], s[56:57]\n"
"v_pk_mul_f32 v[186:187], v[186:187], s[56:57]\n"
"v_pk_mul_f32 v[188:189], v[188:189], s[56:57]\n"
"v_pk_mul_f32 v[190:191], v[190:191], s[56:57]\n"
"v_cvt_pk_bf16_f32 v172, v184, v185\n"
"v_cvt_pk_bf16_f32 v173, v186, v187\n"
"v_cvt_pk_bf16_f32 v174, v188, v189\n"
"v_cvt_pk_bf16_f32 v175, v190, v191\n"
"v_accvgpr_write_b32 a60, v172\n"
"v_accvgpr_write_b32 a61, v173\n"
"v_accvgpr_write_b32 a62, v174\n"
"v_accvgpr_write_b32 a63, v175\n"
"v_mfma_f32_16x16x32_bf16 v[192:195], a[96:99], a[48:51], 0\n"
"v_exp_f32 v204, v204\n"
"v_mfma_f32_16x16x32_bf16 v[192:195], a[100:103], a[52:55], v[192:195]\n"
"v_cvt_pk_bf16_f32 v212, v200, v201\n"
"v_cvt_pk_bf16_f32 v213, v202, v203\n"
"v_mfma_f32_16x16x32_bf16 v[192:195], a[104:107], a[56:59], v[192:195]\n"
"v_exp_f32 v205, v205\n"
"v_mfma_f32_16x16x32_bf16 v[192:195], a[108:111], a[60:63], v[192:195]\n"
"v_cvt_pk_bf16_f32 v214, v204, v205\n"
"v_mfma_f32_16x16x32_bf16 v[196:199], a[112:115], a[60:63], 0\n"
"v_exp_f32 v206, v206\n"
"v_mfma_f32_16x16x32_bf16 v[196:199], a[116:119], a[56:59], v[196:199]\n"
"v_mfma_f32_16x16x32_bf16 v[196:199], a[120:123], a[52:55], v[196:199]\n"
"v_exp_f32 v207, v207\n"
"v_mfma_f32_16x16x32_bf16 v[196:199], a[124:127], a[48:51], v[196:199]\n"
"v_cvt_pk_bf16_f32 v215, v206, v207\n"
"v_mfma_f32_16x16x32_bf16 v[200:203], a[128:131], a[48:51], 0\n"
"v_mfma_f32_16x16x32_bf16 v[200:203], a[132:135], a[52:55], v[200:203]\n"
"v_mfma_f32_16x16x32_bf16 v[200:203], a[136:139], a[56:59], v[200:203]\n"
"v_mfma_f32_16x16x32_bf16 v[200:203], a[140:143], a[60:63], v[200:203]\n"
"v_mfma_f32_16x16x32_bf16 v[204:207], a[144:147], a[60:63], 0\n"
"v_mfma_f32_16x16x32_bf16 v[204:207], a[148:151], a[56:59], v[204:207]\n"
"v_mfma_f32_16x16x32_bf16 v[204:207], a[152:155], a[52:55], v[204:207]\n"
"v_max3_f32 v253, v192, v193, v232\n"
"v_max3_f32 v254, v194, v195, v196\n"
"v_mfma_f32_16x16x32_bf16 v[204:207], a[156:159], a[48:51], v[204:207]\n"
"v_max3_f32 v253, v253, v197, v198\n"
"v_smfmac_f32_16x16x64_bf16 a[232:235], a[248:251], v[208:215], v252\n"
"v_max3_f32 v254, v254, v199, v200\n"
"v_mfma_f32_16x16x32_bf16 v[64:67], v[208:211], a[160:163], 0\n"
"v_max3_f32 v253, v253, v201, v202\n"
"v_mfma_f32_16x16x32_bf16 v[64:67], v[212:215], a[164:167], v[64:67]\n"
"v_mfma_f32_16x16x32_bf16 v[68:71], v[212:215], a[168:171], 0\n"
"s_nop 0\n"
"v_max3_f32 v254, v254, v203, v204\n"
"v_max3_f32 v253, v253, v205, v206\n"
"v_max3_f32 v253, v253, v254, v207\n"
"v_mov_b32 v254, v253\n"
"v_mfma_f32_16x16x32_bf16 v[68:71], v[208:211], a[172:175], v[68:71]\n"
"s_nop 0\n"
"v_permlane32_swap_b32 v253, v254\n"
"v_max_f32 v253, v253, v254\n"
"v_mov_b32 v254, v253\n"
"v_mfma_f32_16x16x32_bf16 v[72:75], v[208:211], a[176:179], 0\n"
"s_nop 0\n"
"v_permlane16_swap_b32 v253, v254\n"
"v_max_f32 v253, v253, v254\n"
"v_sub_f32 v192, v192, v253\n"
"v_mfma_f32_16x16x32_bf16 v[72:75], v[212:215], a[180:183], v[72:75]\n"
"v_sub_f32 v193, v193, v253\n"
"v_sub_f32 v194, v194, v253\n"
"v_sub_f32 v195, v195, v253\n"
"v_mfma_f32_16x16x32_bf16 v[76:79], v[212:215], a[184:187], 0\n"
"v_sub_f32 v196, v196, v253\n"
"v_sub_f32 v197, v197, v253\n"
"v_sub_f32 v198, v198, v253\n"
"v_sub_f32 v199, v199, v253\n"
"v_mfma_f32_16x16x32_bf16 v[76:79], v[208:211], a[188:191], v[76:79]\n"
"v_sub_f32 v200, v200, v253\n"
"v_sub_f32 v201, v201, v253\n"
"v_sub_f32 v202, v202, v253\n"
"v_mfma_f32_16x16x32_bf16 v[80:83], v[208:211], a[192:195], 0\n"
"v_sub_f32 v203, v203, v253\n"
"v_sub_f32 v204, v204, v253\n"
"v_sub_f32 v205, v205, v253\n"
"v_mfma_f32_16x16x32_bf16 v[80:83], v[212:215], a[196:199], v[80:83]\n"
"v_sub_f32 v206, v206, v253\n"
"v_sub_f32 v207, v207, v253\n"
"v_sub_f32 v232, 0, v253\n"
"v_mfma_f32_16x16x32_bf16 v[84:87], v[212:215], a[200:203], 0\n"
"v_sub_f32 v233, 0, v253\n"
"v_sub_f32 v234, 0, v253\n"
"v_sub_f32 v235, 0, v253\n"
"v_mfma_f32_16x16x32_bf16 v[84:87], v[208:211], a[204:207], v[84:87]\n"
"v_exp_f32 v192, v192\n"
"v_exp_f32 v193, v193\n"
"v_exp_f32 v194, v194\n"
"v_exp_f32 v195, v195\n"
"v_mfma_f32_16x16x32_bf16 v[88:91], v[208:211], a[208:211], 0\n"
"v_cvt_pk_bf16_f32 v216, v192, v193\n"
"v_exp_f32 v196, v196\n"
"v_exp_f32 v197, v197\n"
"v_mfma_f32_16x16x32_bf16 v[88:91], v[212:215], a[212:215], v[88:91]\n"
"v_cvt_pk_bf16_f32 v217, v194, v195\n"
"v_exp_f32 v198, v198\n"
"v_exp_f32 v199, v199\n"
"v_mfma_f32_16x16x32_bf16 v[92:95], v[212:215], a[216:219], 0\n"
"v_cvt_pk_bf16_f32 v218, v196, v197\n"
"v_exp_f32 v200, v200\n"
"v_cvt_pk_bf16_f32 v219, v198, v199\n"
"v_mfma_f32_16x16x32_bf16 v[92:95], v[208:211], a[220:223], v[92:95]\n"
"v_exp_f32 v201, v201\n"
"v_exp_f32 v202, v202\n"
"v_exp_f32 v203, v203\n"
"s_waitcnt vmcnt(4)\n"
"s_add_u32 m0, s53, 32768\n"
"s_add_u32 s24, s52, 16384\n"
"buffer_load_dwordx4 v248, s[36:39], s24 offen nt lds\n"
"buffer_load_dwordx4 v249, s[36:39], s24 offen offset:1024 nt lds\n"
"s_add_u32 m0, s53, 49152\n"
"s_add_u32 s24, s52, 24576\n"
"buffer_load_dwordx4 v248, s[36:39], s24 offen nt lds\n"
"buffer_load_dwordx4 v249, s[36:39], s24 offen offset:1024 nt lds\n"
"v_add_u32 v134, 65536, v134\n"
"v_add_u32 v135, 65536, v135\n"
"ds_read_b128 v[160:163], v134 offset:0\n"
"ds_read_b128 v[164:167], v135 offset:0\n"
"ds_read_b128 v[168:171], v134 offset:1024\n"
"ds_read_b128 v[172:175], v135 offset:1024\n"
"s_waitcnt lgkmcnt(3)\n"
"v_lshlrev_b32 v176, 16, v160\n"
"v_and_b32 v177, 0xffff0000, v160\n"
"v_lshlrev_b32 v178, 16, v161\n"
"v_and_b32 v179, 0xffff0000, v161\n"
"v_lshlrev_b32 v180, 16, v162\n"
"v_and_b32 v181, 0xffff0000, v162\n"
"v_lshlrev_b32 v182, 16, v163\n"
"v_and_b32 v183, 0xffff0000, v163\n"
"v_pk_mul_f32 v[176:177], v[176:177], s[56:57]\n"
"v_pk_mul_f32 v[178:179], v[178:179], s[56:57]\n"
"v_pk_mul_f32 v[180:181], v[180:181], s[56:57]\n"
"v_pk_mul_f32 v[182:183], v[182:183], s[56:57]\n"
"v_cvt_pk_bf16_f32 v160, v176, v177\n"
"v_cvt_pk_bf16_f32 v161, v178, v179\n"
"v_cvt_pk_bf16_f32 v162, v180, v181\n"
"v_cvt_pk_bf16_f32 v163, v182, v183\n"
"v_accvgpr_write_b32 a64, v160\n"
"v_accvgpr_write_b32 a65, v161\n"
"v_accvgpr_write_b32 a66, v162\n"
"v_accvgpr_write_b32 a67, v163\n"
"s_waitcnt lgkmcnt(2)\n"
"v_lshlrev_b32 v184, 16, v164\n"
"v_and_b32 v185, 0xffff0000, v164\n"
"v_lshlrev_b32 v186, 16, v165\n"
"v_and_b32 v187, 0xffff0000, v165\n"
"v_lshlrev_b32 v188, 16, v166\n"
"v_and_b32 v189, 0xffff0000, v166\n"
"v_lshlrev_b32 v190, 16, v167\n"
"v_and_b32 v191, 0xffff0000, v167\n"
"v_pk_mul_f32 v[184:185], v[184:185], s[56:57]\n"
"v_pk_mul_f32 v[186:187], v[186:187], s[56:57]\n"
"v_pk_mul_f32 v[188:189], v[188:189], s[56:57]\n"
"v_pk_mul_f32 v[190:191], v[190:191], s[56:57]\n"
"v_cvt_pk_bf16_f32 v164, v184, v185\n"
"v_cvt_pk_bf16_f32 v165, v186, v187\n"
"v_cvt_pk_bf16_f32 v166, v188, v189\n"
"v_cvt_pk_bf16_f32 v167, v190, v191\n"
"v_accvgpr_write_b32 a68, v164\n"
"v_accvgpr_write_b32 a69, v165\n"
"v_accvgpr_write_b32 a70, v166\n"
"v_accvgpr_write_b32 a71, v167\n"
"s_waitcnt lgkmcnt(1)\n"
"v_lshlrev_b32 v176, 16, v168\n"
"v_and_b32 v177, 0xffff0000, v168\n"
"v_lshlrev_b32 v178, 16, v169\n"
"v_and_b32 v179, 0xffff0000, v169\n"
"v_lshlrev_b32 v180, 16, v170\n"
"v_and_b32 v181, 0xffff0000, v170\n"
"v_lshlrev_b32 v182, 16, v171\n"
"v_and_b32 v183, 0xffff0000, v171\n"
"v_pk_mul_f32 v[176:177], v[176:177], s[56:57]\n"
"v_pk_mul_f32 v[178:179], v[178:179], s[56:57]\n"
"v_pk_mul_f32 v[180:181], v[180:181], s[56:57]\n"
"v_pk_mul_f32 v[182:183], v[182:183], s[56:57]\n"
"v_cvt_pk_bf16_f32 v168, v176, v177\n"
"v_cvt_pk_bf16_f32 v169, v178, v179\n"
"v_cvt_pk_bf16_f32 v170, v180, v181\n"
"v_cvt_pk_bf16_f32 v171, v182, v183\n"
"v_accvgpr_write_b32 a72, v168\n"
"v_accvgpr_write_b32 a73, v169\n"
"v_accvgpr_write_b32 a74, v170\n"
"v_accvgpr_write_b32 a75, v171\n"
"s_waitcnt lgkmcnt(0)\n"
"v_lshlrev_b32 v184, 16, v172\n"
"v_and_b32 v185, 0xffff0000, v172\n"
"v_lshlrev_b32 v186, 16, v173\n"
"v_and_b32 v187, 0xffff0000, v173\n"
"v_lshlrev_b32 v188, 16, v174\n"
"v_and_b32 v189, 0xffff0000, v174\n"
"v_lshlrev_b32 v190, 16, v175\n"
"v_and_b32 v191, 0xffff0000, v175\n"
"v_pk_mul_f32 v[184:185], v[184:185], s[56:57]\n"
"v_pk_mul_f32 v[186:187], v[186:187], s[56:57]\n"
"v_pk_mul_f32 v[188:189], v[188:189], s[56:57]\n"
"v_pk_mul_f32 v[190:191], v[190:191], s[56:57]\n"
"v_cvt_pk_bf16_f32 v172, v184, v185\n"
"v_cvt_pk_bf16_f32 v173, v186, v187\n"
"v_cvt_pk_bf16_f32 v174, v188, v189\n"
"v_cvt_pk_bf16_f32 v175, v190, v191\n"
"v_accvgpr_write_b32 a76, v172\n"
"v_accvgpr_write_b32 a77, v173\n"
"v_accvgpr_write_b32 a78, v174\n"
"v_accvgpr_write_b32 a79, v175\n"
"v_mfma_f32_16x16x32_bf16 v[192:195], a[96:99], a[64:67], 0\n"
"v_exp_f32 v204, v204\n"
"v_mfma_f32_16x16x32_bf16 v[192:195], a[100:103], a[68:71], v[192:195]\n"
"v_cvt_pk_bf16_f32 v212, v200, v201\n"
"v_cvt_pk_bf16_f32 v213, v202, v203\n"
"v_mfma_f32_16x16x32_bf16 v[192:195], a[104:107], a[72:75], v[192:195]\n"
"v_exp_f32 v205, v205\n"
"v_mfma_f32_16x16x32_bf16 v[192:195], a[108:111], a[76:79], v[192:195]\n"
"v_cvt_pk_bf16_f32 v214, v204, v205\n"
"v_mfma_f32_16x16x32_bf16 v[196:199], a[112:115], a[76:79], 0\n"
"v_exp_f32 v206, v206\n"
"v_mfma_f32_16x16x32_bf16 v[196:199], a[116:119], a[72:75], v[196:199]\n"
"v_mfma_f32_16x16x32_bf16 v[196:199], a[120:123], a[68:71], v[196:199]\n"
"v_exp_f32 v207, v207\n"
"v_mfma_f32_16x16x32_bf16 v[196:199], a[124:127], a[64:67], v[196:199]\n"
"v_cvt_pk_bf16_f32 v215, v206, v207\n"
"v_mfma_f32_16x16x32_bf16 v[200:203], a[128:131], a[64:67], 0\n"
"v_mfma_f32_16x16x32_bf16 v[200:203], a[132:135], a[68:71], v[200:203]\n"
"v_mfma_f32_16x16x32_bf16 v[200:203], a[136:139], a[72:75], v[200:203]\n"
"v_mfma_f32_16x16x32_bf16 v[200:203], a[140:143], a[76:79], v[200:203]\n"
"v_mfma_f32_16x16x32_bf16 v[204:207], a[144:147], a[76:79], 0\n"
"v_mfma_f32_16x16x32_bf16 v[204:207], a[148:151], a[72:75], v[204:207]\n"
"v_mfma_f32_16x16x32_bf16 v[204:207], a[152:155], a[68:71], v[204:207]\n"
"v_max3_f32 v253, v192, v193, v236\n"
"v_max3_f32 v254, v194, v195, v196\n"
"v_mfma_f32_16x16x32_bf16 v[204:207], a[156:159], a[64:67], v[204:207]\n"
"v_max3_f32 v253, v253, v197, v198\n"
"v_smfmac_f32_16x16x64_bf16 a[236:239], a[248:251], v[212:219], v252\n"
"v_max3_f32 v254, v254, v199, v200\n"
"v_mfma_f32_16x16x32_bf16 v[96:99], v[216:219], a[160:163], 0\n"
"v_max3_f32 v253, v253, v201, v202\n"
"v_mfma_f32_16x16x32_bf16 v[96:99], v[212:215], a[164:167], v[96:99]\n"
"v_mfma_f32_16x16x32_bf16 v[100:103], v[212:215], a[168:171], 0\n"
"s_nop 0\n"
"v_max3_f32 v254, v254, v203, v204\n"
"v_max3_f32 v253, v253, v205, v206\n"
"v_max3_f32 v253, v253, v254, v207\n"
"v_mov_b32 v254, v253\n"
"v_mfma_f32_16x16x32_bf16 v[100:103], v[216:219], a[172:175], v[100:103]\n"
"s_nop 0\n"
"v_permlane32_swap_b32 v253, v254\n"
"v_max_f32 v253, v253, v254\n"
"v_mov_b32 v254, v253\n"
"v_mfma_f32_16x16x32_bf16 v[104:107], v[216:219], a[176:179], 0\n"
"s_nop 0\n"
"v_permlane16_swap_b32 v253, v254\n"
"v_max_f32 v253, v253, v254\n"
"v_sub_f32 v192, v192, v253\n"
"v_mfma_f32_16x16x32_bf16 v[104:107], v[212:215], a[180:183], v[104:107]\n"
"v_sub_f32 v193, v193, v253\n"
"v_sub_f32 v194, v194, v253\n"
"v_sub_f32 v195, v195, v253\n"
"v_mfma_f32_16x16x32_bf16 v[108:111], v[212:215], a[184:187], 0\n"
"v_sub_f32 v196, v196, v253\n"
"v_sub_f32 v197, v197, v253\n"
"v_sub_f32 v198, v198, v253\n"
"v_sub_f32 v199, v199, v253\n"
"v_mfma_f32_16x16x32_bf16 v[108:111], v[216:219], a[188:191], v[108:111]\n"
"v_sub_f32 v200, v200, v253\n"
"v_sub_f32 v201, v201, v253\n"
"v_sub_f32 v202, v202, v253\n"
"v_mfma_f32_16x16x32_bf16 v[112:115], v[216:219], a[192:195], 0\n"
"v_sub_f32 v203, v203, v253\n"
"v_sub_f32 v204, v204, v253\n"
"v_sub_f32 v205, v205, v253\n"
"v_mfma_f32_16x16x32_bf16 v[112:115], v[212:215], a[196:199], v[112:115]\n"
"v_sub_f32 v206, v206, v253\n"
"v_sub_f32 v207, v207, v253\n"
"v_sub_f32 v236, 0, v253\n"
"v_mfma_f32_16x16x32_bf16 v[116:119], v[212:215], a[200:203], 0\n"
"v_sub_f32 v237, 0, v253\n"
"v_sub_f32 v238, 0, v253\n"
"v_sub_f32 v239, 0, v253\n"
"v_mfma_f32_16x16x32_bf16 v[116:119], v[216:219], a[204:207], v[116:119]\n"
"v_exp_f32 v192, v192\n"
"v_exp_f32 v193, v193\n"
"v_exp_f32 v194, v194\n"
"v_exp_f32 v195, v195\n"
"v_mfma_f32_16x16x32_bf16 v[120:123], v[216:219], a[208:211], 0\n"
"v_cvt_pk_bf16_f32 v208, v192, v193\n"
"v_exp_f32 v196, v196\n"
"v_exp_f32 v197, v197\n"
"v_mfma_f32_16x16x32_bf16 v[120:123], v[212:215], a[212:215], v[120:123]\n"
"v_cvt_pk_bf16_f32 v209, v194, v195\n"
"v_exp_f32 v198, v198\n"
"v_exp_f32 v199, v199\n"
"v_mfma_f32_16x16x32_bf16 v[124:127], v[212:215], a[216:219], 0\n"
"v_cvt_pk_bf16_f32 v210, v196, v197\n"
"v_exp_f32 v200, v200\n"
"v_cvt_pk_bf16_f32 v211, v198, v199\n"
"v_mfma_f32_16x16x32_bf16 v[124:127], v[216:219], a[220:223], v[124:127]\n"
"v_exp_f32 v201, v201\n"
"v_exp_f32 v202, v202\n"
"v_exp_f32 v203, v203\n"
"s_waitcnt vmcnt(4)\n"
"s_add_u32 m0, s53, 32768\n"
"s_add_u32 s24, s52, 16384\n"
"buffer_load_dwordx4 v250, s[40:43], s24 offen offset:2048 nt lds\n"
"buffer_load_dwordx4 v251, s[40:43], s24 offen offset:3072 nt lds\n"
"s_add_u32 m0, s53, 49152\n"
"s_add_u32 s24, s52, 24576\n"
"buffer_load_dwordx4 v250, s[40:43], s24 offen offset:2048 nt lds\n"
"buffer_load_dwordx4 v251, s[40:43], s24 offen offset:3072 nt lds\n"
"ds_read_b128 v[160:163], v134 offset:16384\n"
"ds_read_b128 v[164:167], v135 offset:16384\n"
"ds_read_b128 v[168:171], v134 offset:17408\n"
"ds_read_b128 v[172:175], v135 offset:17408\n"
"s_waitcnt lgkmcnt(3)\n"
"v_lshlrev_b32 v176, 16, v160\n"
"v_and_b32 v177, 0xffff0000, v160\n"
"v_lshlrev_b32 v178, 16, v161\n"
"v_and_b32 v179, 0xffff0000, v161\n"
"v_lshlrev_b32 v180, 16, v162\n"
"v_and_b32 v181, 0xffff0000, v162\n"
"v_lshlrev_b32 v182, 16, v163\n"
"v_and_b32 v183, 0xffff0000, v163\n"
"v_pk_mul_f32 v[176:177], v[176:177], s[56:57]\n"
"v_pk_mul_f32 v[178:179], v[178:179], s[56:57]\n"
"v_pk_mul_f32 v[180:181], v[180:181], s[56:57]\n"
"v_pk_mul_f32 v[182:183], v[182:183], s[56:57]\n"
"v_cvt_pk_bf16_f32 v160, v176, v177\n"
"v_cvt_pk_bf16_f32 v161, v178, v179\n"
"v_cvt_pk_bf16_f32 v162, v180, v181\n"
"v_cvt_pk_bf16_f32 v163, v182, v183\n"
"v_accvgpr_write_b32 a80, v160\n"
"v_accvgpr_write_b32 a81, v161\n"
"v_accvgpr_write_b32 a82, v162\n"
"v_accvgpr_write_b32 a83, v163\n"
"s_waitcnt lgkmcnt(2)\n"
"v_lshlrev_b32 v184, 16, v164\n"
"v_and_b32 v185, 0xffff0000, v164\n"
"v_lshlrev_b32 v186, 16, v165\n"
"v_and_b32 v187, 0xffff0000, v165\n"
"v_lshlrev_b32 v188, 16, v166\n"
"v_and_b32 v189, 0xffff0000, v166\n"
"v_lshlrev_b32 v190, 16, v167\n"
"v_and_b32 v191, 0xffff0000, v167\n"
"v_pk_mul_f32 v[184:185], v[184:185], s[56:57]\n"
"v_pk_mul_f32 v[186:187], v[186:187], s[56:57]\n"
"v_pk_mul_f32 v[188:189], v[188:189], s[56:57]\n"
"v_pk_mul_f32 v[190:191], v[190:191], s[56:57]\n"
"v_cvt_pk_bf16_f32 v164, v184, v185\n"
"v_cvt_pk_bf16_f32 v165, v186, v187\n"
"v_cvt_pk_bf16_f32 v166, v188, v189\n"
"v_cvt_pk_bf16_f32 v167, v190, v191\n"
"v_accvgpr_write_b32 a84, v164\n"
"v_accvgpr_write_b32 a85, v165\n"
"v_accvgpr_write_b32 a86, v166\n"
"v_accvgpr_write_b32 a87, v167\n"
"s_waitcnt lgkmcnt(1)\n"
"v_lshlrev_b32 v176, 16, v168\n"
"v_and_b32 v177, 0xffff0000, v168\n"
"v_lshlrev_b32 v178, 16, v169\n"
"v_and_b32 v179, 0xffff0000, v169\n"
"v_lshlrev_b32 v180, 16, v170\n"
"v_and_b32 v181, 0xffff0000, v170\n"
"v_lshlrev_b32 v182, 16, v171\n"
"v_and_b32 v183, 0xffff0000, v171\n"
"v_pk_mul_f32 v[176:177], v[176:177], s[56:57]\n"
"v_pk_mul_f32 v[178:179], v[178:179], s[56:57]\n"
"v_pk_mul_f32 v[180:181], v[180:181], s[56:57]\n"
"v_pk_mul_f32 v[182:183], v[182:183], s[56:57]\n"
"v_cvt_pk_bf16_f32 v168, v176, v177\n"
"v_cvt_pk_bf16_f32 v169, v178, v179\n"
"v_cvt_pk_bf16_f32 v170, v180, v181\n"
"v_cvt_pk_bf16_f32 v171, v182, v183\n"
"v_accvgpr_write_b32 a88, v168\n"
"v_accvgpr_write_b32 a89, v169\n"
"v_accvgpr_write_b32 a90, v170\n"
"v_accvgpr_write_b32 a91, v171\n"
"s_waitcnt lgkmcnt(0)\n"
"v_lshlrev_b32 v184, 16, v172\n"
"v_and_b32 v185, 0xffff0000, v172\n"
"v_lshlrev_b32 v186, 16, v173\n"
"v_and_b32 v187, 0xffff0000, v173\n"
"v_lshlrev_b32 v188, 16, v174\n"
"v_and_b32 v189, 0xffff0000, v174\n"
"v_lshlrev_b32 v190, 16, v175\n"
"v_and_b32 v191, 0xffff0000, v175\n"
"v_pk_mul_f32 v[184:185], v[184:185], s[56:57]\n"
"v_pk_mul_f32 v[186:187], v[186:187], s[56:57]\n"
"v_pk_mul_f32 v[188:189], v[188:189], s[56:57]\n"
"v_pk_mul_f32 v[190:191], v[190:191], s[56:57]\n"
"v_cvt_pk_bf16_f32 v172, v184, v185\n"
"v_cvt_pk_bf16_f32 v173, v186, v187\n"
"v_cvt_pk_bf16_f32 v174, v188, v189\n"
"v_cvt_pk_bf16_f32 v175, v190, v191\n"
"v_accvgpr_write_b32 a92, v172\n"
"v_accvgpr_write_b32 a93, v173\n"
"v_accvgpr_write_b32 a94, v174\n"
"v_accvgpr_write_b32 a95, v175\n"
"v_mfma_f32_16x16x32_bf16 v[192:195], a[96:99], a[80:83], 0\n"
"v_exp_f32 v204, v204\n"
"v_mfma_f32_16x16x32_bf16 v[192:195], a[100:103], a[84:87], v[192:195]\n"
"v_cvt_pk_bf16_f32 v212, v200, v201\n"
"v_cvt_pk_bf16_f32 v213, v202, v203\n"
"v_mfma_f32_16x16x32_bf16 v[192:195], a[104:107], a[88:91], v[192:195]\n"
"v_exp_f32 v205, v205\n"
"v_mfma_f32_16x16x32_bf16 v[192:195], a[108:111], a[92:95], v[192:195]\n"
"v_cvt_pk_bf16_f32 v214, v204, v205\n"
"v_mfma_f32_16x16x32_bf16 v[196:199], a[112:115], a[92:95], 0\n"
"v_exp_f32 v206, v206\n"
"v_mfma_f32_16x16x32_bf16 v[196:199], a[116:119], a[88:91], v[196:199]\n"
"v_mfma_f32_16x16x32_bf16 v[196:199], a[120:123], a[84:87], v[196:199]\n"
"v_exp_f32 v207, v207\n"
"v_mfma_f32_16x16x32_bf16 v[196:199], a[124:127], a[80:83], v[196:199]\n"
"v_cvt_pk_bf16_f32 v215, v206, v207\n"
"v_mfma_f32_16x16x32_bf16 v[200:203], a[128:131], a[80:83], 0\n"
"v_mfma_f32_16x16x32_bf16 v[200:203], a[132:135], a[84:87], v[200:203]\n"
"v_mfma_f32_16x16x32_bf16 v[200:203], a[136:139], a[88:91], v[200:203]\n"
"v_mfma_f32_16x16x32_bf16 v[200:203], a[140:143], a[92:95], v[200:203]\n"
"v_mfma_f32_16x16x32_bf16 v[204:207], a[144:147], a[92:95], 0\n"
"v_mfma_f32_16x16x32_bf16 v[204:207], a[148:151], a[88:91], v[204:207]\n"
"v_mfma_f32_16x16x32_bf16 v[204:207], a[152:155], a[84:87], v[204:207]\n"
"v_max3_f32 v253, v192, v193, v240\n"
"v_max3_f32 v254, v194, v195, v196\n"
"v_mfma_f32_16x16x32_bf16 v[204:207], a[156:159], a[80:83], v[204:207]\n"
"v_max3_f32 v253, v253, v197, v198\n"
"v_smfmac_f32_16x16x64_bf16 a[240:243], a[248:251], v[208:215], v252\n"
"v_max3_f32 v254, v254, v199, v200\n"
"v_mfma_f32_16x16x32_bf16 v[128:131], v[208:211], a[160:163], 0\n"
"v_max3_f32 v253, v253, v201, v202\n"
"v_mfma_f32_16x16x32_bf16 v[128:131], v[212:215], a[164:167], v[128:131]\n"
"v_mfma_f32_16x16x32_bf16 v[132:135], v[212:215], a[168:171], 0\n"
"s_nop 0\n"
"v_max3_f32 v254, v254, v203, v204\n"
"v_max3_f32 v253, v253, v205, v206\n"
"v_max3_f32 v253, v253, v254, v207\n"
"v_mov_b32 v254, v253\n"
"v_mfma_f32_16x16x32_bf16 v[132:135], v[208:211], a[172:175], v[132:135]\n"
"s_nop 0\n"
"v_permlane32_swap_b32 v253, v254\n"
"v_max_f32 v253, v253, v254\n"
"v_mov_b32 v254, v253\n"
"v_mfma_f32_16x16x32_bf16 v[136:139], v[208:211], a[176:179], 0\n"
"s_nop 0\n"
"v_permlane16_swap_b32 v253, v254\n"
"v_max_f32 v253, v253, v254\n"
"v_sub_f32 v192, v192, v253\n"
"v_mfma_f32_16x16x32_bf16 v[136:139], v[212:215], a[180:183], v[136:139]\n"
"v_sub_f32 v193, v193, v253\n"
"v_sub_f32 v194, v194, v253\n"
"v_sub_f32 v195, v195, v253\n"
"v_mfma_f32_16x16x32_bf16 v[140:143], v[212:215], a[184:187], 0\n"
"v_sub_f32 v196, v196, v253\n"
"v_sub_f32 v197, v197, v253\n"
"v_sub_f32 v198, v198, v253\n"
"v_sub_f32 v199, v199, v253\n"
"v_mfma_f32_16x16x32_bf16 v[140:143], v[208:211], a[188:191], v[140:143]\n"
"v_sub_f32 v200, v200, v253\n"
"v_sub_f32 v201, v201, v253\n"
"v_sub_f32 v202, v202, v253\n"
"v_mfma_f32_16x16x32_bf16 v[144:147], v[208:211], a[192:195], 0\n"
"v_sub_f32 v203, v203, v253\n"
"v_sub_f32 v204, v204, v253\n"
"v_sub_f32 v205, v205, v253\n"
"v_mfma_f32_16x16x32_bf16 v[144:147], v[212:215], a[196:199], v[144:147]\n"
"v_sub_f32 v206, v206, v253\n"
"v_sub_f32 v207, v207, v253\n"
"v_sub_f32 v240, 0, v253\n"
"v_mfma_f32_16x16x32_bf16 v[148:151], v[212:215], a[200:203], 0\n"
"v_sub_f32 v241, 0, v253\n"
"v_sub_f32 v242, 0, v253\n"
"v_sub_f32 v243, 0, v253\n"
"v_mfma_f32_16x16x32_bf16 v[148:151], v[208:211], a[204:207], v[148:151]\n"
"v_exp_f32 v192, v192\n"
"v_exp_f32 v193, v193\n"
"v_exp_f32 v194, v194\n"
"v_exp_f32 v195, v195\n"
"v_mfma_f32_16x16x32_bf16 v[152:155], v[208:211], a[208:211], 0\n"
"v_cvt_pk_bf16_f32 v216, v192, v193\n"
"v_exp_f32 v196, v196\n"
"v_exp_f32 v197, v197\n"
"v_mfma_f32_16x16x32_bf16 v[152:155], v[212:215], a[212:215], v[152:155]\n"
"v_cvt_pk_bf16_f32 v217, v194, v195\n"
"v_exp_f32 v198, v198\n"
"v_exp_f32 v199, v199\n"
"v_mfma_f32_16x16x32_bf16 v[156:159], v[212:215], a[216:219], 0\n"
"v_cvt_pk_bf16_f32 v218, v196, v197\n"
"v_exp_f32 v200, v200\n"
"v_cvt_pk_bf16_f32 v219, v198, v199\n"
"v_mfma_f32_16x16x32_bf16 v[156:159], v[208:211], a[220:223], v[156:159]\n"
"v_exp_f32 v201, v201\n"
"v_exp_f32 v202, v202\n"
"v_exp_f32 v203, v203\n"
"s_waitcnt vmcnt(4)\n"
"s_barrier\n"
"ds_read_b128 a[96:99], v244 offset:32768\n"
"ds_read_b128 a[100:103], v245 offset:32768\n"
"ds_read_b128 a[104:107], v244 offset:33792\n"
"ds_read_b128 a[108:111], v245 offset:33792\n"
"ds_read_b128 a[112:115], v245 offset:41984\n"
"ds_read_b128 a[116:119], v244 offset:41984\n"
"ds_read_b128 a[120:123], v245 offset:40960\n"
"ds_read_b128 a[124:127], v244 offset:40960\n"
"ds_read_b128 a[128:131], v244 offset:49152\n"
"ds_read_b128 a[132:135], v245 offset:49152\n"
"ds_read_b128 a[136:139], v244 offset:50176\n"
"ds_read_b128 a[140:143], v245 offset:50176\n"
"ds_read_b128 a[144:147], v245 offset:58368\n"
"ds_read_b128 a[148:151], v244 offset:58368\n"
"ds_read_b128 a[152:155], v245 offset:57344\n"
"ds_read_b128 a[156:159], v244 offset:57344\n"
"s_add_u32 m0, s53, 65536\n"
"s_add_u32 s24, s52, 32768\n"
"buffer_load_dwordx4 v248, s[36:39], s24 offen nt lds\n"
"buffer_load_dwordx4 v249, s[36:39], s24 offen offset:1024 nt lds\n"
"buffer_load_dwordx4 v250, s[40:43], s24 offen offset:2048 nt lds\n"
"buffer_load_dwordx4 v251, s[40:43], s24 offen offset:3072 nt lds\n"
"s_waitcnt lgkmcnt(8)\n"
"s_lshl_b32 s55, %[wid], 2\n"
"s_add_u32 s55, s55, 161792\n"
"v_mov_b32 v253, s55\n"
"v_mbcnt_lo_u32_b32 v254, -1, 0\n"
"v_and_b32 v254, 3, v254\n"
"v_lshlrev_b32 v254, 2, v254\n"
"v_add_u32 v254, 161792, v254\n"
"s_xor_b32 s25, s53, 0x10000\n"
"s_mov_b32 s26, 3\n"
"v_mfma_f32_16x16x32_bf16 v[192:195], a[96:99], a[0:3], v[220:223]\n"
"v_exp_f32 v204, v204\n"
"v_mfma_f32_16x16x32_bf16 v[192:195], a[100:103], a[4:7], v[192:195]\n"
"v_cvt_pk_bf16_f32 v212, v200, v201\n"
"v_cvt_pk_bf16_f32 v213, v202, v203\n"
"v_mfma_f32_16x16x32_bf16 v[192:195], a[104:107], a[8:11], v[192:195]\n"
"v_exp_f32 v205, v205\n"
"v_mfma_f32_16x16x32_bf16 v[192:195], a[108:111], a[12:15], v[192:195]\n"
"v_cvt_pk_bf16_f32 v214, v204, v205\n"
"v_mfma_f32_16x16x32_bf16 v[196:199], a[112:115], a[12:15], v[220:223]\n"
"v_exp_f32 v206, v206\n"
"v_mfma_f32_16x16x32_bf16 v[196:199], a[116:119], a[8:11], v[196:199]\n"
"v_mfma_f32_16x16x32_bf16 v[196:199], a[120:123], a[4:7], v[196:199]\n"
"v_exp_f32 v207, v207\n"
"v_mfma_f32_16x16x32_bf16 v[196:199], a[124:127], a[0:3], v[196:199]\n"
"v_cvt_pk_bf16_f32 v215, v206, v207\n"
"s_waitcnt lgkmcnt(0)\n"
"v_mfma_f32_16x16x32_bf16 v[200:203], a[128:131], a[0:3], v[220:223]\n"
"v_exp_f32 v192, v192\n"
"v_mfma_f32_16x16x32_bf16 v[200:203], a[132:135], a[4:7], v[200:203]\n"
"v_mfma_f32_16x16x32_bf16 v[200:203], a[136:139], a[8:11], v[200:203]\n"
"v_exp_f32 v193, v193\n"
"v_mfma_f32_16x16x32_bf16 v[200:203], a[140:143], a[12:15], v[200:203]\n"
"v_cvt_pk_bf16_f32 v208, v192, v193\n"
"v_mfma_f32_16x16x32_bf16 v[204:207], a[144:147], a[12:15], v[220:223]\n"
"v_exp_f32 v194, v194\n"
"v_mfma_f32_16x16x32_bf16 v[204:207], a[148:151], a[8:11], v[204:207]\n"
"v_mfma_f32_16x16x32_bf16 v[204:207], a[152:155], a[4:7], v[204:207]\n"
"v_exp_f32 v195, v195\n"
"v_mfma_f32_16x16x32_bf16 v[204:207], a[156:159], a[0:3], v[204:207]\n"
"v_cvt_pk_bf16_f32 v209, v194, v195\n"
"s_waitcnt vmcnt(4)\n"
"s_barrier\n"
"v_smfmac_f32_16x16x64_bf16 a[244:247], a[248:251], v[212:219], v252\n"
"v_exp_f32 v196, v196\n"
"v_mfma_f32_16x16x32_bf16 v[160:163], v[216:219], a[160:163], 0\n"
"ds_read_b64_tr_b16 a[160:161], v246 offset:34816\n"
"ds_read_b64_tr_b16 a[162:163], v246 offset:43008\n"
"v_mfma_f32_16x16x32_bf16 v[160:163], v[212:215], a[164:167], v[160:163]\n"
"v_exp_f32 v197, v197\n"
"v_mfma_f32_16x16x32_bf16 v[164:167], v[212:215], a[168:171], 0\n"
"ds_read_b64_tr_b16 a[164:165], v246 offset:51200\n"
"v_cvt_pk_bf16_f32 v210, v196, v197\n"
"v_mfma_f32_16x16x32_bf16 v[164:167], v[216:219], a[172:175], v[164:167]\n"
"v_exp_f32 v198, v198\n"
"v_mfma_f32_16x16x32_bf16 v[168:171], v[216:219], a[176:179], 0\n"
"ds_read_b64_tr_b16 a[166:167], v246 offset:59392\n"
"ds_read_b64_tr_b16 a[168:169], v247 offset:51200\n"
"v_mfma_f32_16x16x32_bf16 v[168:171], v[212:215], a[180:183], v[168:171]\n"
"v_exp_f32 v199, v199\n"
"v_mfma_f32_16x16x32_bf16 v[172:175], v[212:215], a[184:187], 0\n"
"ds_read_b64_tr_b16 a[170:171], v247 offset:59392\n"
"v_cvt_pk_bf16_f32 v211, v198, v199\n"
"v_mfma_f32_16x16x32_bf16 v[172:175], v[216:219], a[188:191], v[172:175]\n"
"v_exp_f32 v200, v200\n"
"v_mfma_f32_16x16x32_bf16 v[176:179], v[216:219], a[192:195], 0\n"
"ds_read_b64_tr_b16 a[172:173], v247 offset:34816\n"
"ds_read_b64_tr_b16 a[174:175], v247 offset:43008\n"
"v_mfma_f32_16x16x32_bf16 v[176:179], v[212:215], a[196:199], v[176:179]\n"
"v_exp_f32 v201, v201\n"
"v_mfma_f32_16x16x32_bf16 v[180:183], v[212:215], a[200:203], 0\n"
"ds_read_b64_tr_b16 a[176:177], v246 offset:35328\n"
"ds_read_b64_tr_b16 a[178:179], v246 offset:43520\n"
"v_mfma_f32_16x16x32_bf16 v[180:183], v[216:219], a[204:207], v[180:183]\n"
"v_exp_f32 v202, v202\n"
"v_mfma_f32_16x16x32_bf16 v[184:187], v[216:219], a[208:211], 0\n"
"ds_read_b64_tr_b16 a[180:181], v246 offset:51712\n"
"ds_read_b64_tr_b16 a[182:183], v246 offset:59904\n"
"v_mfma_f32_16x16x32_bf16 v[184:187], v[212:215], a[212:215], v[184:187]\n"
"v_exp_f32 v203, v203\n"
"v_mfma_f32_16x16x32_bf16 v[188:191], v[212:215], a[216:219], 0\n"
"ds_read_b64_tr_b16 a[184:185], v247 offset:51712\n"
"ds_read_b64_tr_b16 a[186:187], v247 offset:59904\n"
"v_mfma_f32_16x16x32_bf16 v[188:191], v[216:219], a[220:223], v[188:191]\n"
"ds_read_b64_tr_b16 a[188:189], v247 offset:35328\n"
"ds_read_b64_tr_b16 a[190:191], v247 offset:43520\n"
"v_mfma_f32_16x16x32_bf16 v[192:195], a[96:99], a[16:19], v[224:227]\n"
"v_exp_f32 v204, v204\n"
"v_mfma_f32_16x16x32_bf16 v[192:195], a[100:103], a[20:23], v[192:195]\n"
"v_cvt_pk_bf16_f32 v212, v200, v201\n"
"v_cvt_pk_bf16_f32 v213, v202, v203\n"
"v_mfma_f32_16x16x32_bf16 v[192:195], a[104:107], a[24:27], v[192:195]\n"
"v_exp_f32 v205, v205\n"
"v_mfma_f32_16x16x32_bf16 v[192:195], a[108:111], a[28:31], v[192:195]\n"
"ds_read_b64_tr_b16 a[192:193], v246 offset:35840\n"
"v_cvt_pk_bf16_f32 v214, v204, v205\n"
"v_mfma_f32_16x16x32_bf16 v[196:199], a[112:115], a[28:31], v[224:227]\n"
"v_exp_f32 v206, v206\n"
"v_mfma_f32_16x16x32_bf16 v[196:199], a[116:119], a[24:27], v[196:199]\n"
"ds_read_b64_tr_b16 a[194:195], v246 offset:44032\n"
"ds_read_b64_tr_b16 a[196:197], v246 offset:52224\n"
"v_mfma_f32_16x16x32_bf16 v[196:199], a[120:123], a[20:23], v[196:199]\n"
"v_exp_f32 v207, v207\n"
"v_mfma_f32_16x16x32_bf16 v[196:199], a[124:127], a[16:19], v[196:199]\n"
"ds_read_b64_tr_b16 a[198:199], v246 offset:60416\n"
"v_cvt_pk_bf16_f32 v215, v206, v207\n"
"v_mfma_f32_16x16x32_bf16 v[200:203], a[128:131], a[16:19], v[224:227]\n"
"v_exp_f32 v192, v192\n"
"v_mfma_f32_16x16x32_bf16 v[200:203], a[132:135], a[20:23], v[200:203]\n"
"ds_read_b64_tr_b16 a[200:201], v247 offset:52224\n"
"ds_read_b64_tr_b16 a[202:203], v247 offset:60416\n"
"v_mfma_f32_16x16x32_bf16 v[200:203], a[136:139], a[24:27], v[200:203]\n"
"v_exp_f32 v193, v193\n"
"v_mfma_f32_16x16x32_bf16 v[200:203], a[140:143], a[28:31], v[200:203]\n"
"ds_read_b64_tr_b16 a[204:205], v247 offset:35840\n"
"v_cvt_pk_bf16_f32 v216, v192, v193\n"
"v_mfma_f32_16x16x32_bf16 v[204:207], a[144:147], a[28:31], v[224:227]\n"
"v_exp_f32 v194, v194\n"
"v_mfma_f32_16x16x32_bf16 v[204:207], a[148:151], a[24:27], v[204:207]\n"
"ds_read_b64_tr_b16 a[206:207], v247 offset:44032\n"
"ds_read_b64_tr_b16 a[208:209], v246 offset:36352\n"
"v_mfma_f32_16x16x32_bf16 v[204:207], a[152:155], a[20:23], v[204:207]\n"
"v_exp_f32 v195, v195\n"
"v_mfma_f32_16x16x32_bf16 v[204:207], a[156:159], a[16:19], v[204:207]\n"
"v_cvt_pk_bf16_f32 v217, v194, v195\n"
"s_waitcnt lgkmcnt(15)\n"
"v_smfmac_f32_16x16x64_bf16 a[224:227], a[248:251], v[208:215], v252\n"
"v_exp_f32 v196, v196\n"
"v_mfma_f32_16x16x32_bf16 v[0:3], v[208:211], a[160:163], v[0:3]\n"
"ds_read_b64_tr_b16 a[210:211], v246 offset:44544\n"
"ds_read_b64_tr_b16 a[212:213], v246 offset:52736\n"
"ds_read_b64_tr_b16 a[214:215], v246 offset:60928\n"
"v_mfma_f32_16x16x32_bf16 v[0:3], v[212:215], a[164:167], v[0:3]\n"
"ds_read_b64_tr_b16 a[216:217], v247 offset:52736\n"
"v_exp_f32 v197, v197\n"
"v_mfma_f32_16x16x32_bf16 v[4:7], v[212:215], a[168:171], v[4:7]\n"
"v_cvt_pk_bf16_f32 v218, v196, v197\n"
"s_waitcnt lgkmcnt(13)\n"
"ds_read_b64_tr_b16 a[218:219], v247 offset:60928\n"
"v_mfma_f32_16x16x32_bf16 v[4:7], v[208:211], a[172:175], v[4:7]\n"
"ds_read_b64_tr_b16 a[220:221], v247 offset:36352\n"
"v_exp_f32 v198, v198\n"
"v_mfma_f32_16x16x32_bf16 v[8:11], v[208:211], a[176:179], v[8:11]\n"
"ds_read_b64_tr_b16 a[222:223], v247 offset:44544\n"
"s_add_u32 m0, s25, 16384\n"
"s_add_u32 s24, s24, 8192\n"
"v_mfma_f32_16x16x32_bf16 v[8:11], v[212:215], a[180:183], v[8:11]\n"
"v_exp_f32 v199, v199\n"
"v_mfma_f32_16x16x32_bf16 v[12:15], v[212:215], a[184:187], v[12:15]\n"
"v_cvt_pk_bf16_f32 v219, v198, v199\n"
"s_waitcnt lgkmcnt(8)\n"
"v_mfma_f32_16x16x32_bf16 v[12:15], v[208:211], a[188:191], v[12:15]\n"
"v_exp_f32 v200, v200\n"
"v_mfma_f32_16x16x32_bf16 v[16:19], v[208:211], a[192:195], v[16:19]\n"
"buffer_load_dwordx4 v248, s[36:39], s24 offen nt lds\n"
"v_mfma_f32_16x16x32_bf16 v[16:19], v[212:215], a[196:199], v[16:19]\n"
"v_exp_f32 v201, v201\n"
"v_mfma_f32_16x16x32_bf16 v[20:23], v[212:215], a[200:203], v[20:23]\n"
"s_waitcnt lgkmcnt(0)\n"
"v_mfma_f32_16x16x32_bf16 v[20:23], v[208:211], a[204:207], v[20:23]\n"
"v_exp_f32 v202, v202\n"
"v_mfma_f32_16x16x32_bf16 v[24:27], v[208:211], a[208:211], v[24:27]\n"
"buffer_load_dwordx4 v249, s[36:39], s24 offen offset:1024 nt lds\n"
"v_mfma_f32_16x16x32_bf16 v[24:27], v[212:215], a[212:215], v[24:27]\n"
"v_exp_f32 v203, v203\n"
"v_mfma_f32_16x16x32_bf16 v[28:31], v[212:215], a[216:219], v[28:31]\n"
"v_mfma_f32_16x16x32_bf16 v[28:31], v[208:211], a[220:223], v[28:31]\n"
"v_mfma_f32_16x16x32_bf16 v[192:195], a[96:99], a[32:35], v[228:231]\n"
"v_exp_f32 v204, v204\n"
"v_mfma_f32_16x16x32_bf16 v[192:195], a[100:103], a[36:39], v[192:195]\n"
"v_cvt_pk_bf16_f32 v212, v200, v201\n"
"v_cvt_pk_bf16_f32 v213, v202, v203\n"
"v_mfma_f32_16x16x32_bf16 v[192:195], a[104:107], a[40:43], v[192:195]\n"
"v_exp_f32 v205, v205\n"
"v_mfma_f32_16x16x32_bf16 v[192:195], a[108:111], a[44:47], v[192:195]\n"
"v_cvt_pk_bf16_f32 v214, v204, v205\n"
"v_mfma_f32_16x16x32_bf16 v[196:199], a[112:115], a[44:47], v[228:231]\n"
"v_exp_f32 v206, v206\n"
"v_mfma_f32_16x16x32_bf16 v[196:199], a[116:119], a[40:43], v[196:199]\n"
"buffer_load_dwordx4 v250, s[40:43], s24 offen offset:2048 nt lds\n"
"v_mfma_f32_16x16x32_bf16 v[196:199], a[120:123], a[36:39], v[196:199]\n"
"v_exp_f32 v207, v207\n"
"v_mfma_f32_16x16x32_bf16 v[196:199], a[124:127], a[32:35], v[196:199]\n"
"v_cvt_pk_bf16_f32 v215, v206, v207\n"
"v_mfma_f32_16x16x32_bf16 v[200:203], a[128:131], a[32:35], v[228:231]\n"
"v_exp_f32 v192, v192\n"
"v_mfma_f32_16x16x32_bf16 v[200:203], a[132:135], a[36:39], v[200:203]\n"
"buffer_load_dwordx4 v251, s[40:43], s24 offen offset:3072 nt lds\n"
"v_mfma_f32_16x16x32_bf16 v[200:203], a[136:139], a[40:43], v[200:203]\n"
"v_exp_f32 v193, v193\n"
"v_mfma_f32_16x16x32_bf16 v[200:203], a[140:143], a[44:47], v[200:203]\n"
"v_cvt_pk_bf16_f32 v208, v192, v193\n"
"s_add_u32 m0, s25, 32768\n"
"s_add_u32 s24, s24, 8192\n"
"v_mfma_f32_16x16x32_bf16 v[204:207], a[144:147], a[44:47], v[228:231]\n"
"v_exp_f32 v194, v194\n"
"v_mfma_f32_16x16x32_bf16 v[204:207], a[148:151], a[40:43], v[204:207]\n"
"buffer_load_dwordx4 v248, s[36:39], s24 offen nt lds\n"
"v_mfma_f32_16x16x32_bf16 v[204:207], a[152:155], a[36:39], v[204:207]\n"
"v_exp_f32 v195, v195\n"
"v_mfma_f32_16x16x32_bf16 v[204:207], a[156:159], a[32:35], v[204:207]\n"
"v_cvt_pk_bf16_f32 v209, v194, v195\n"
"v_smfmac_f32_16x16x64_bf16 a[228:231], a[248:251], v[212:219], v252\n"
"v_exp_f32 v196, v196\n"
"v_mfma_f32_16x16x32_bf16 v[32:35], v[216:219], a[160:163], v[32:35]\n"
"buffer_load_dwordx4 v249, s[36:39], s24 offen offset:1024 nt lds\n"
"v_mfma_f32_16x16x32_bf16 v[32:35], v[212:215], a[164:167], v[32:35]\n"
"v_exp_f32 v197, v197\n"
"v_mfma_f32_16x16x32_bf16 v[36:39], v[212:215], a[168:171], v[36:39]\n"
"v_cvt_pk_bf16_f32 v210, v196, v197\n"
"v_mfma_f32_16x16x32_bf16 v[36:39], v[216:219], a[172:175], v[36:39]\n"
"v_exp_f32 v198, v198\n"
"v_mfma_f32_16x16x32_bf16 v[40:43], v[216:219], a[176:179], v[40:43]\n"
"v_mfma_f32_16x16x32_bf16 v[40:43], v[212:215], a[180:183], v[40:43]\n"
"v_exp_f32 v199, v199\n"
"v_mfma_f32_16x16x32_bf16 v[44:47], v[212:215], a[184:187], v[44:47]\n"
"v_cvt_pk_bf16_f32 v211, v198, v199\n"
"v_mfma_f32_16x16x32_bf16 v[44:47], v[216:219], a[188:191], v[44:47]\n"
"v_exp_f32 v200, v200\n"
"v_mfma_f32_16x16x32_bf16 v[48:51], v[216:219], a[192:195], v[48:51]\n"
"buffer_load_dwordx4 v250, s[40:43], s24 offen offset:2048 nt lds\n"
"v_mfma_f32_16x16x32_bf16 v[48:51], v[212:215], a[196:199], v[48:51]\n"
"v_exp_f32 v201, v201\n"
"v_mfma_f32_16x16x32_bf16 v[52:55], v[212:215], a[200:203], v[52:55]\n"
"v_mfma_f32_16x16x32_bf16 v[52:55], v[216:219], a[204:207], v[52:55]\n"
"v_exp_f32 v202, v202\n"
"v_mfma_f32_16x16x32_bf16 v[56:59], v[216:219], a[208:211], v[56:59]\n"
"v_mfma_f32_16x16x32_bf16 v[56:59], v[212:215], a[212:215], v[56:59]\n"
"v_exp_f32 v203, v203\n"
"v_mfma_f32_16x16x32_bf16 v[60:63], v[212:215], a[216:219], v[60:63]\n"
"v_mfma_f32_16x16x32_bf16 v[60:63], v[216:219], a[220:223], v[60:63]\n"
"v_mfma_f32_16x16x32_bf16 v[192:195], a[96:99], a[48:51], v[232:235]\n"
"v_exp_f32 v204, v204\n"
"v_mfma_f32_16x16x32_bf16 v[192:195], a[100:103], a[52:55], v[192:195]\n"
"v_cvt_pk_bf16_f32 v212, v200, v201\n"
"v_cvt_pk_bf16_f32 v213, v202, v203\n"
"v_mfma_f32_16x16x32_bf16 v[192:195], a[104:107], a[56:59], v[192:195]\n"
"v_exp_f32 v205, v205\n"
"v_mfma_f32_16x16x32_bf16 v[192:195], a[108:111], a[60:63], v[192:195]\n"
"v_cvt_pk_bf16_f32 v214, v204, v205\n"
"v_mfma_f32_16x16x32_bf16 v[196:199], a[112:115], a[60:63], v[232:235]\n"
"v_exp_f32 v206, v206\n"
"v_mfma_f32_16x16x32_bf16 v[196:199], a[116:119], a[56:59], v[196:199]\n"
"v_mfma_f32_16x16x32_bf16 v[196:199], a[120:123], a[52:55], v[196:199]\n"
"v_exp_f32 v207, v207\n"
"v_mfma_f32_16x16x32_bf16 v[196:199], a[124:127], a[48:51], v[196:199]\n"
"v_cvt_pk_bf16_f32 v215, v206, v207\n"
"v_mfma_f32_16x16x32_bf16 v[200:203], a[128:131], a[48:51], v[232:235]\n"
"v_exp_f32 v192, v192\n"
"v_mfma_f32_16x16x32_bf16 v[200:203], a[132:135], a[52:55], v[200:203]\n"
"v_mfma_f32_16x16x32_bf16 v[200:203], a[136:139], a[56:59], v[200:203]\n"
"v_exp_f32 v193, v193\n"
"v_mfma_f32_16x16x32_bf16 v[200:203], a[140:143], a[60:63], v[200:203]\n"
"v_cvt_pk_bf16_f32 v216, v192, v193\n"
"v_mfma_f32_16x16x32_bf16 v[204:207], a[144:147], a[60:63], v[232:235]\n"
"v_exp_f32 v194, v194\n"
"v_mfma_f32_16x16x32_bf16 v[204:207], a[148:151], a[56:59], v[204:207]\n"
"buffer_load_dwordx4 v251, s[40:43], s24 offen offset:3072 nt lds\n"
"v_mfma_f32_16x16x32_bf16 v[204:207], a[152:155], a[52:55], v[204:207]\n"
"v_exp_f32 v195, v195\n"
"v_mfma_f32_16x16x32_bf16 v[204:207], a[156:159], a[48:51], v[204:207]\n"
"v_cvt_pk_bf16_f32 v217, v194, v195\n"
"v_smfmac_f32_16x16x64_bf16 a[232:235], a[248:251], v[208:215], v252\n"
"v_exp_f32 v196, v196\n"
"v_mfma_f32_16x16x32_bf16 v[64:67], v[208:211], a[160:163], v[64:67]\n"
"v_mfma_f32_16x16x32_bf16 v[64:67], v[212:215], a[164:167], v[64:67]\n"
"v_exp_f32 v197, v197\n"
"v_mfma_f32_16x16x32_bf16 v[68:71], v[212:215], a[168:171], v[68:71]\n"
"v_cvt_pk_bf16_f32 v218, v196, v197\n"
"v_mfma_f32_16x16x32_bf16 v[68:71], v[208:211], a[172:175], v[68:71]\n"
"v_exp_f32 v198, v198\n"
"v_mfma_f32_16x16x32_bf16 v[72:75], v[208:211], a[176:179], v[72:75]\n"
"v_mfma_f32_16x16x32_bf16 v[72:75], v[212:215], a[180:183], v[72:75]\n"
"v_exp_f32 v199, v199\n"
"v_mfma_f32_16x16x32_bf16 v[76:79], v[212:215], a[184:187], v[76:79]\n"
"v_cvt_pk_bf16_f32 v219, v198, v199\n"
"v_mfma_f32_16x16x32_bf16 v[76:79], v[208:211], a[188:191], v[76:79]\n"
"v_exp_f32 v200, v200\n"
"v_mfma_f32_16x16x32_bf16 v[80:83], v[208:211], a[192:195], v[80:83]\n"
"s_add_u32 s27, s27, 1\n"
"v_mov_b32 v255, s27\n"
"v_mfma_f32_16x16x32_bf16 v[80:83], v[212:215], a[196:199], v[80:83]\n"
"v_exp_f32 v201, v201\n"
"v_mfma_f32_16x16x32_bf16 v[84:87], v[212:215], a[200:203], v[84:87]\n"
"v_mfma_f32_16x16x32_bf16 v[84:87], v[208:211], a[204:207], v[84:87]\n"
"v_exp_f32 v202, v202\n"
"v_mfma_f32_16x16x32_bf16 v[88:91], v[208:211], a[208:211], v[88:91]\n"
"v_mfma_f32_16x16x32_bf16 v[88:91], v[212:215], a[212:215], v[88:91]\n"
"v_exp_f32 v203, v203\n"
"v_mfma_f32_16x16x32_bf16 v[92:95], v[212:215], a[216:219], v[92:95]\n"
"s_waitcnt vmcnt(4)\n"
"ds_write_b32 v253, v255\n"
"v_mfma_f32_16x16x32_bf16 v[92:95], v[208:211], a[220:223], v[92:95]\n"
"v_mfma_f32_16x16x32_bf16 v[192:195], a[96:99], a[64:67], v[236:239]\n"
"v_exp_f32 v204, v204\n"
"v_mfma_f32_16x16x32_bf16 v[192:195], a[100:103], a[68:71], v[192:195]\n"
"v_cvt_pk_bf16_f32 v212, v200, v201\n"
"v_cvt_pk_bf16_f32 v213, v202, v203\n"
"v_mfma_f32_16x16x32_bf16 v[192:195], a[104:107], a[72:75], v[192:195]\n"
"v_exp_f32 v205, v205\n"
"v_mfma_f32_16x16x32_bf16 v[192:195], a[108:111], a[76:79], v[192:195]\n"
"v_cvt_pk_bf16_f32 v214, v204, v205\n"
"s_add_u32 m0, s25, 49152\n"
"s_add_u32 s24, s24, 8192\n"
"v_mfma_f32_16x16x32_bf16 v[196:199], a[112:115], a[76:79], v[236:239]\n"
"v_exp_f32 v206, v206\n"
"v_mfma_f32_16x16x32_bf16 v[196:199], a[116:119], a[72:75], v[196:199]\n"
"buffer_load_dwordx4 v248, s[36:39], s24 offen nt lds\n"
"v_mfma_f32_16x16x32_bf16 v[196:199], a[120:123], a[68:71], v[196:199]\n"
"v_exp_f32 v207, v207\n"
"v_mfma_f32_16x16x32_bf16 v[196:199], a[124:127], a[64:67], v[196:199]\n"
"v_cvt_pk_bf16_f32 v215, v206, v207\n"
"v_mfma_f32_16x16x32_bf16 v[200:203], a[128:131], a[64:67], v[236:239]\n"
"v_exp_f32 v192, v192\n"
"v_mfma_f32_16x16x32_bf16 v[200:203], a[132:135], a[68:71], v[200:203]\n"
"v_xor_b32 v244, 0x10000, v244\n"
"v_xor_b32 v245, 0x10000, v245\n"
"v_mfma_f32_16x16x32_bf16 v[200:203], a[136:139], a[72:75], v[200:203]\n"
"v_exp_f32 v193, v193\n"
"v_mfma_f32_16x16x32_bf16 v[200:203], a[140:143], a[76:79], v[200:203]\n"
"v_cvt_pk_bf16_f32 v208, v192, v193\n"
"v_mfma_f32_16x16x32_bf16 v[204:207], a[144:147], a[76:79], v[236:239]\n"
"v_exp_f32 v194, v194\n"
"v_mfma_f32_16x16x32_bf16 v[204:207], a[148:151], a[72:75], v[204:207]\n"
"v_mfma_f32_16x16x32_bf16 v[204:207], a[152:155], a[68:71], v[204:207]\n"
"v_exp_f32 v195, v195\n"
"v_mfma_f32_16x16x32_bf16 v[204:207], a[156:159], a[64:67], v[204:207]\n"
"v_cvt_pk_bf16_f32 v209, v194, v195\n"
"v_smfmac_f32_16x16x64_bf16 a[236:239], a[248:251], v[212:219], v252\n"
"v_exp_f32 v196, v196\n"
"v_mfma_f32_16x16x32_bf16 v[96:99], v[216:219], a[160:163], v[96:99]\n"
"v_mfma_f32_16x16x32_bf16 v[96:99], v[212:215], a[164:167], v[96:99]\n"
"v_exp_f32 v197, v197\n"
"v_mfma_f32_16x16x32_bf16 v[100:103], v[212:215], a[168:171], v[100:103]\n"
"v_cvt_pk_bf16_f32 v210, v196, v197\n"
"v_mfma_f32_16x16x32_bf16 v[100:103], v[216:219], a[172:175], v[100:103]\n"
"v_exp_f32 v198, v198\n"
"v_mfma_f32_16x16x32_bf16 v[104:107], v[216:219], a[176:179], v[104:107]\n"
"ds_read_b32 v255, v254\n"
"v_mfma_f32_16x16x32_bf16 v[104:107], v[212:215], a[180:183], v[104:107]\n"
"v_exp_f32 v199, v199\n"
"v_mfma_f32_16x16x32_bf16 v[108:111], v[212:215], a[184:187], v[108:111]\n"
"v_cvt_pk_bf16_f32 v211, v198, v199\n"
"v_mfma_f32_16x16x32_bf16 v[108:111], v[216:219], a[188:191], v[108:111]\n"
"v_exp_f32 v200, v200\n"
"v_mfma_f32_16x16x32_bf16 v[112:115], v[216:219], a[192:195], v[112:115]\n"
"buffer_load_dwordx4 v249, s[36:39], s24 offen offset:1024 nt lds\n"
"v_mfma_f32_16x16x32_bf16 v[112:115], v[212:215], a[196:199], v[112:115]\n"
"v_exp_f32 v201, v201\n"
"v_mfma_f32_16x16x32_bf16 v[116:119], v[212:215], a[200:203], v[116:119]\n"
"v_mfma_f32_16x16x32_bf16 v[116:119], v[216:219], a[204:207], v[116:119]\n"
"v_exp_f32 v202, v202\n"
"v_mfma_f32_16x16x32_bf16 v[120:123], v[216:219], a[208:211], v[120:123]\n"
"v_mfma_f32_16x16x32_bf16 v[120:123], v[212:215], a[212:215], v[120:123]\n"
"v_exp_f32 v203, v203\n"
"v_mfma_f32_16x16x32_bf16 v[124:127], v[212:215], a[216:219], v[124:127]\n"
"s_waitcnt lgkmcnt(0)\n"
"v_cmp_gt_u32 vcc, s27, v255\n"
"v_mfma_f32_16x16x32_bf16 v[124:127], v[216:219], a[220:223], v[124:127]\n"
"s_cbranch_vccnz L_late6_2_%=\n"
"L_back6_2_%=:\n"
"v_mfma_f32_16x16x32_bf16 v[192:195], a[96:99], a[80:83], v[240:243]\n"
"v_exp_f32 v204, v204\n"
"v_mfma_f32_16x16x32_bf16 v[192:195], a[100:103], a[84:87], v[192:195]\n"
"v_cvt_pk_bf16_f32 v212, v200, v201\n"
"v_cvt_pk_bf16_f32 v213, v202, v203\n"
"v_mfma_f32_16x16x32_bf16 v[192:195], a[104:107], a[88:91], v[192:195]\n"
"v_exp_f32 v205, v205\n"
"v_mfma_f32_16x16x32_bf16 v[192:195], a[108:111], a[92:95], v[192:195]\n"
"ds_read_b128 a[96:99], v244 offset:0\n"
"v_cvt_pk_bf16_f32 v214, v204, v205\n"
"v_mfma_f32_16x16x32_bf16 v[196:199], a[112:115], a[92:95], v[240:243]\n"
"v_exp_f32 v206, v206\n"
"v_mfma_f32_16x16x32_bf16 v[196:199], a[116:119], a[88:91], v[196:199]\n"
"ds_read_b128 a[100:103], v245 offset:0\n"
"ds_read_b128 a[104:107], v244 offset:1024\n"
"v_mfma_f32_16x16x32_bf16 v[196:199], a[120:123], a[84:87], v[196:199]\n"
"v_exp_f32 v207, v207\n"
"v_mfma_f32_16x16x32_bf16 v[196:199], a[124:127], a[80:83], v[196:199]\n"
"ds_read_b128 a[108:111], v245 offset:1024\n"
"v_cvt_pk_bf16_f32 v215, v206, v207\n"
"v_mfma_f32_16x16x32_bf16 v[200:203], a[128:131], a[80:83], v[240:243]\n"
"v_exp_f32 v192, v192\n"
"v_mfma_f32_16x16x32_bf16 v[200:203], a[132:135], a[84:87], v[200:203]\n"
"ds_read_b128 a[112:115], v245 offset:9216\n"
"ds_read_b128 a[116:119], v244 offset:9216\n"
"v_mfma_f32_16x16x32_bf16 v[200:203], a[136:139], a[88:91], v[200:203]\n"
"v_exp_f32 v193, v193\n"
"v_mfma_f32_16x16x32_bf16 v[200:203], a[140:143], a[92:95], v[200:203]\n"
"ds_read_b128 a[120:123], v245 offset:8192\n"
"v_cvt_pk_bf16_f32 v216, v192, v193\n"
"v_mfma_f32_16x16x32_bf16 v[204:207], a[144:147], a[92:95], v[240:243]\n"
"v_exp_f32 v194, v194\n"
"v_mfma_f32_16x16x32_bf16 v[204:207], a[148:151], a[88:91], v[204:207]\n"
"ds_read_b128 a[124:127], v244 offset:8192\n"
"v_mfma_f32_16x16x32_bf16 v[204:207], a[152:155], a[84:87], v[204:207]\n"
"v_exp_f32 v195, v195\n"
"v_mfma_f32_16x16x32_bf16 v[204:207], a[156:159], a[80:83], v[204:207]\n"
"ds_read_b128 a[128:131], v244 offset:16384\n"
"v_cvt_pk_bf16_f32 v217, v194, v195\n"
"v_smfmac_f32_16x16x64_bf16 a[240:243], a[248:251], v[208:215], v252\n"
"v_exp_f32 v196, v196\n"
"v_mfma_f32_16x16x32_bf16 v[128:131], v[208:211], a[160:163], v[128:131]\n"
"ds_read_b128 a[132:135], v245 offset:16384\n"
"v_mfma_f32_16x16x32_bf16 v[128:131], v[212:215], a[164:167], v[128:131]\n"
"v_exp_f32 v197, v197\n"
"v_mfma_f32_16x16x32_bf16 v[132:135], v[212:215], a[168:171], v[132:135]\n"
"ds_read_b128 a[136:139], v244 offset:17408\n"
"v_cvt_pk_bf16_f32 v218, v196, v197\n"
"v_mfma_f32_16x16x32_bf16 v[132:135], v[208:211], a[172:175], v[132:135]\n"
"v_exp_f32 v198, v198\n"
"v_mfma_f32_16x16x32_bf16 v[136:139], v[208:211], a[176:179], v[136:139]\n"
"ds_read_b128 a[140:143], v245 offset:17408\n"
"ds_read_b128 a[144:147], v245 offset:25600\n"
"v_mfma_f32_16x16x32_bf16 v[136:139], v[212:215], a[180:183], v[136:139]\n"
"v_exp_f32 v199, v199\n"
"v_mfma_f32_16x16x32_bf16 v[140:143], v[212:215], a[184:187], v[140:143]\n"
"ds_read_b128 a[148:151], v244 offset:25600\n"
"v_cvt_pk_bf16_f32 v219, v198, v199\n"
"v_mfma_f32_16x16x32_bf16 v[140:143], v[208:211], a[188:191], v[140:143]\n"
"v_exp_f32 v200, v200\n"
"v_mfma_f32_16x16x32_bf16 v[144:147], v[208:211], a[192:195], v[144:147]\n"
"ds_read_b128 a[152:155], v245 offset:24576\n"
"v_mfma_f32_16x16x32_bf16 v[144:147], v[212:215], a[196:199], v[144:147]\n"
"v_exp_f32 v201, v201\n"
"v_mfma_f32_16x16x32_bf16 v[148:151], v[212:215], a[200:203], v[148:151]\n"
"buffer_load_dwordx4 v250, s[40:43], s24 offen offset:2048 nt lds\n"
"v_mfma_f32_16x16x32_bf16 v[148:151], v[208:211], a[204:207], v[148:151]\n"
"v_exp_f32 v202, v202\n"
"v_mfma_f32_16x16x32_bf16 v[152:155], v[208:211], a[208:211], v[152:155]\n"
"ds_read_b128 a[156:159], v244 offset:24576\n"
"v_mfma_f32_16x16x32_bf16 v[152:155], v[212:215], a[212:215], v[152:155]\n"
"v_exp_f32 v203, v203\n"
"v_mfma_f32_16x16x32_bf16 v[156:159], v[212:215], a[216:219], v[156:159]\n"
"v_mfma_f32_16x16x32_bf16 v[156:159], v[208:211], a[220:223], v[156:159]\n"
"s_waitcnt lgkmcnt(8)\n"
"v_mfma_f32_16x16x32_bf16 v[192:195], a[96:99], a[0:3], v[220:223]\n"
"v_exp_f32 v204, v204\n"
"v_mfma_f32_16x16x32_bf16 v[192:195], a[100:103], a[4:7], v[192:195]\n"
"v_cvt_pk_bf16_f32 v212, v200, v201\n"
"v_cvt_pk_bf16_f32 v213, v202, v203\n"
"v_mfma_f32_16x16x32_bf16 v[192:195], a[104:107], a[8:11], v[192:195]\n"
"v_exp_f32 v205, v205\n"
"v_mfma_f32_16x16x32_bf16 v[192:195], a[108:111], a[12:15], v[192:195]\n"
"v_cvt_pk_bf16_f32 v214, v204, v205\n"
"v_mfma_f32_16x16x32_bf16 v[196:199], a[112:115], a[12:15], v[220:223]\n"
"v_exp_f32 v206, v206\n"
"v_mfma_f32_16x16x32_bf16 v[196:199], a[116:119], a[8:11], v[196:199]\n"
"buffer_load_dwordx4 v251, s[40:43], s24 offen offset:3072 nt lds\n"
"v_mfma_f32_16x16x32_bf16 v[196:199], a[120:123], a[4:7], v[196:199]\n"
"v_exp_f32 v207, v207\n"
"v_mfma_f32_16x16x32_bf16 v[196:199], a[124:127], a[0:3], v[196:199]\n"
"v_cvt_pk_bf16_f32 v215, v206, v207\n"
"s_waitcnt lgkmcnt(0)\n"
"v_mfma_f32_16x16x32_bf16 v[200:203], a[128:131], a[0:3], v[220:223]\n"
"v_exp_f32 v192, v192\n"
"v_mfma_f32_16x16x32_bf16 v[200:203], a[132:135], a[4:7], v[200:203]\n"
"v_xor_b32 v246, 0x10000, v246\n"
"v_xor_b32 v247, 0x10000, v247\n"
"v_mfma_f32_16x16x32_bf16 v[200:203], a[136:139], a[8:11], v[200:203]\n"
"v_exp_f32 v193, v193\n"
"v_mfma_f32_16x16x32_bf16 v[200:203], a[140:143], a[12:15], v[200:203]\n"
"v_cvt_pk_bf16_f32 v208, v192, v193\n"
"v_mfma_f32_16x16x32_bf16 v[204:207], a[144:147], a[12:15], v[220:223]\n"
"v_exp_f32 v194, v194\n"
"v_mfma_f32_16x16x32_bf16 v[204:207], a[148:151], a[8:11], v[204:207]\n"
"v_mfma_f32_16x16x32_bf16 v[204:207], a[152:155], a[4:7], v[204:207]\n"
"v_exp_f32 v195, v195\n"
"v_mfma_f32_16x16x32_bf16 v[204:207], a[156:159], a[0:3], v[204:207]\n"
"v_cvt_pk_bf16_f32 v209, v194, v195\n"
"v_smfmac_f32_16x16x64_bf16 a[244:247], a[248:251], v[212:219], v252\n"
"v_exp_f32 v196, v196\n"
"v_mfma_f32_16x16x32_bf16 v[160:163], v[216:219], a[160:163], v[160:163]\n"
"ds_read_b64_tr_b16 a[160:161], v246 offset:2048\n"
"ds_read_b64_tr_b16 a[162:163], v246 offset:10240\n"
"v_mfma_f32_16x16x32_bf16 v[160:163], v[212:215], a[164:167], v[160:163]\n"
"v_exp_f32 v197, v197\n"
"v_mfma_f32_16x16x32_bf16 v[164:167], v[212:215], a[168:171], v[164:167]\n"
"ds_read_b64_tr_b16 a[164:165], v246 offset:18432\n"
"v_cvt_pk_bf16_f32 v210, v196, v197\n"
"v_mfma_f32_16x16x32_bf16 v[164:167], v[216:219], a[172:175], v[164:167]\n"
"v_exp_f32 v198, v198\n"
"v_mfma_f32_16x16x32_bf16 v[168:171], v[216:219], a[176:179], v[168:171]\n"
"ds_read_b64_tr_b16 a[166:167], v246 offset:26624\n"
"ds_read_b64_tr_b16 a[168:169], v247 offset:18432\n"
"v_mfma_f32_16x16x32_bf16 v[168:171], v[212:215], a[180:183], v[168:171]\n"
"v_exp_f32 v199, v199\n"
"v_mfma_f32_16x16x32_bf16 v[172:175], v[212:215], a[184:187], v[172:175]\n"
"ds_read_b64_tr_b16 a[170:171], v247 offset:26624\n"
"v_cvt_pk_bf16_f32 v211, v198, v199\n"
"v_mfma_f32_16x16x32_bf16 v[172:175], v[216:219], a[188:191], v[172:175]\n"
"v_exp_f32 v200, v200\n"
"v_mfma_f32_16x16x32_bf16 v[176:179], v[216:219], a[192:195], v[176:179]\n"
"ds_read_b64_tr_b16 a[172:173], v247 offset:2048\n"
"ds_read_b64_tr_b16 a[174:175], v247 offset:10240\n"
"v_mfma_f32_16x16x32_bf16 v[176:179], v[212:215], a[196:199], v[176:179]\n"
"v_exp_f32 v201, v201\n"
"v_mfma_f32_16x16x32_bf16 v[180:183], v[212:215], a[200:203], v[180:183]\n"
"ds_read_b64_tr_b16 a[176:177], v246 offset:2560\n"
"ds_read_b64_tr_b16 a[178:179], v246 offset:10752\n"
"v_mfma_f32_16x16x32_bf16 v[180:183], v[216:219], a[204:207], v[180:183]\n"
"v_exp_f32 v202, v202\n"
"v_mfma_f32_16x16x32_bf16 v[184:187], v[216:219], a[208:211], v[184:187]\n"
"ds_read_b64_tr_b16 a[180:181], v246 offset:18944\n"
"ds_read_b64_tr_b16 a[182:183], v246 offset:27136\n"
"v_mfma_f32_16x16x32_bf16 v[184:187], v[212:215], a[212:215], v[184:187]\n"
"v_exp_f32 v203, v203\n"
"v_mfma_f32_16x16x32_bf16 v[188:191], v[212:215], a[216:219], v[188:191]\n"
"ds_read_b64_tr_b16 a[184:185], v247 offset:18944\n"
"ds_read_b64_tr_b16 a[186:187], v247 offset:27136\n"
"v_mfma_f32_16x16x32_bf16 v[188:191], v[216:219], a[220:223], v[188:191]\n"
"ds_read_b64_tr_b16 a[188:189], v247 offset:2560\n"
"ds_read_b64_tr_b16 a[190:191], v247 offset:10752\n"
"v_mfma_f32_16x16x32_bf16 v[192:195], a[96:99], a[16:19], v[224:227]\n"
"v_exp_f32 v204, v204\n"
"v_mfma_f32_16x16x32_bf16 v[192:195], a[100:103], a[20:23], v[192:195]\n"
"v_cvt_pk_bf16_f32 v212, v200, v201\n"
"v_cvt_pk_bf16_f32 v213, v202, v203\n"
"v_mfma_f32_16x16x32_bf16 v[192:195], a[104:107], a[24:27], v[192:195]\n"
"v_exp_f32 v205, v205\n"
"v_mfma_f32_16x16x32_bf16 v[192:195], a[108:111], a[28:31], v[192:195]\n"
"ds_read_b64_tr_b16 a[192:193], v246 offset:3072\n"
"v_cvt_pk_bf16_f32 v214, v204, v205\n"
"v_mfma_f32_16x16x32_bf16 v[196:199], a[112:115], a[28:31], v[224:227]\n"
"v_exp_f32 v206, v206\n"
"v_mfma_f32_16x16x32_bf16 v[196:199], a[116:119], a[24:27], v[196:199]\n"
"ds_read_b64_tr_b16 a[194:195], v246 offset:11264\n"
"ds_read_b64_tr_b16 a[196:197], v246 offset:19456\n"
"v_mfma_f32_16x16x32_bf16 v[196:199], a[120:123], a[20:23], v[196:199]\n"
"v_exp_f32 v207, v207\n"
"v_mfma_f32_16x16x32_bf16 v[196:199], a[124:127], a[16:19], v[196:199]\n"
"ds_read_b64_tr_b16 a[198:199], v246 offset:27648\n"
"v_cvt_pk_bf16_f32 v215, v206, v207\n"
"v_mfma_f32_16x16x32_bf16 v[200:203], a[128:131], a[16:19], v[224:227]\n"
"v_exp_f32 v192, v192\n"
"v_mfma_f32_16x16x32_bf16 v[200:203], a[132:135], a[20:23], v[200:203]\n"
"ds_read_b64_tr_b16 a[200:201], v247 offset:19456\n"
"ds_read_b64_tr_b16 a[202:203], v247 offset:27648\n"
"v_mfma_f32_16x16x32_bf16 v[200:203], a[136:139], a[24:27], v[200:203]\n"
"v_exp_f32 v193, v193\n"
"v_mfma_f32_16x16x32_bf16 v[200:203], a[140:143], a[28:31], v[200:203]\n"
"ds_read_b64_tr_b16 a[204:205], v247 offset:3072\n"
"v_cvt_pk_bf16_f32 v216, v192, v193\n"
"v_mfma_f32_16x16x32_bf16 v[204:207], a[144:147], a[28:31], v[224:227]\n"
"v_exp_f32 v194, v194\n"
"v_mfma_f32_16x16x32_bf16 v[204:207], a[148:151], a[24:27], v[204:207]\n"
"ds_read_b64_tr_b16 a[206:207], v247 offset:11264\n"
"ds_read_b64_tr_b16 a[208:209], v246 offset:3584\n"
"v_mfma_f32_16x16x32_bf16 v[204:207], a[152:155], a[20:23], v[204:207]\n"
"v_exp_f32 v195, v195\n"
"v_mfma_f32_16x16x32_bf16 v[204:207], a[156:159], a[16:19], v[204:207]\n"
"v_cvt_pk_bf16_f32 v217, v194, v195\n"
"s_waitcnt lgkmcnt(15)\n"
"v_smfmac_f32_16x16x64_bf16 a[224:227], a[248:251], v[208:215], v252\n"
"v_exp_f32 v196, v196\n"
"v_mfma_f32_16x16x32_bf16 v[0:3], v[208:211], a[160:163], v[0:3]\n"
"ds_read_b64_tr_b16 a[210:211], v246 offset:11776\n"
"ds_read_b64_tr_b16 a[212:213], v246 offset:19968\n"
"ds_read_b64_tr_b16 a[214:215], v246 offset:28160\n"
"v_mfma_f32_16x16x32_bf16 v[0:3], v[212:215], a[164:167], v[0:3]\n"
"ds_read_b64_tr_b16 a[216:217], v247 offset:19968\n"
"v_exp_f32 v197, v197\n"
"v_mfma_f32_16x16x32_bf16 v[4:7], v[212:215], a[168:171], v[4:7]\n"
"v_cvt_pk_bf16_f32 v218, v196, v197\n"
"s_waitcnt lgkmcnt(13)\n"
"ds_read_b64_tr_b16 a[218:219], v247 offset:28160\n"
"v_mfma_f32_16x16x32_bf16 v[4:7], v[208:211], a[172:175], v[4:7]\n"
"ds_read_b64_tr_b16 a[220:221], v247 offset:3584\n"
"v_exp_f32 v198, v198\n"
"v_mfma_f32_16x16x32_bf16 v[8:11], v[208:211], a[176:179], v[8:11]\n"
"ds_read_b64_tr_b16 a[222:223], v247 offset:11776\n"
"s_xor_b32 s25, s25, 0x10000\n"
"s_mov_b32 m0, s25\n"
"v_mfma_f32_16x16x32_bf16 v[8:11], v[212:215], a[180:183], v[8:11]\n"
"v_exp_f32 v199, v199\n"
"v_mfma_f32_16x16x32_bf16 v[12:15], v[212:215], a[184:187], v[12:15]\n"
"v_cvt_pk_bf16_f32 v219, v198, v199\n"
"s_add_u32 s24, s24, 8192\n"
"s_waitcnt lgkmcnt(8)\n"
"v_mfma_f32_16x16x32_bf16 v[12:15], v[208:211], a[188:191], v[12:15]\n"
"v_exp_f32 v200, v200\n"
"v_mfma_f32_16x16x32_bf16 v[16:19], v[208:211], a[192:195], v[16:19]\n"
"buffer_load_dwordx4 v248, s[36:39], s24 offen nt lds\n"
"v_mfma_f32_16x16x32_bf16 v[16:19], v[212:215], a[196:199], v[16:19]\n"
"v_exp_f32 v201, v201\n"
"v_mfma_f32_16x16x32_bf16 v[20:23], v[212:215], a[200:203], v[20:23]\n"
"s_waitcnt lgkmcnt(0)\n"
"v_mfma_f32_16x16x32_bf16 v[20:23], v[208:211], a[204:207], v[20:23]\n"
"v_exp_f32 v202, v202\n"
"v_mfma_f32_16x16x32_bf16 v[24:27], v[208:211], a[208:211], v[24:27]\n"
"buffer_load_dwordx4 v249, s[36:39], s24 offen offset:1024 nt lds\n"
"v_mfma_f32_16x16x32_bf16 v[24:27], v[212:215], a[212:215], v[24:27]\n"
"v_exp_f32 v203, v203\n"
"v_mfma_f32_16x16x32_bf16 v[28:31], v[212:215], a[216:219], v[28:31]\n"
"v_mfma_f32_16x16x32_bf16 v[28:31], v[208:211], a[220:223], v[28:31]\n"
"v_mfma_f32_16x16x32_bf16 v[192:195], a[96:99], a[32:35], v[228:231]\n"
"v_exp_f32 v204, v204\n"
"v_mfma_f32_16x16x32_bf16 v[192:195], a[100:103], a[36:39], v[192:195]\n"
"v_cvt_pk_bf16_f32 v212, v200, v201\n"
"v_cvt_pk_bf16_f32 v213, v202, v203\n"
"v_mfma_f32_16x16x32_bf16 v[192:195], a[104:107], a[40:43], v[192:195]\n"
"v_exp_f32 v205, v205\n"
"v_mfma_f32_16x16x32_bf16 v[192:195], a[108:111], a[44:47], v[192:195]\n"
"v_cvt_pk_bf16_f32 v214, v204, v205\n"
"v_mfma_f32_16x16x32_bf16 v[196:199], a[112:115], a[44:47], v[228:231]\n"
"v_exp_f32 v206, v206\n"
"v_mfma_f32_16x16x32_bf16 v[196:199], a[116:119], a[40:43], v[196:199]\n"
"buffer_load_dwordx4 v250, s[40:43], s24 offen offset:2048 nt lds\n"
"v_mfma_f32_16x16x32_bf16 v[196:199], a[120:123], a[36:39], v[196:199]\n"
"v_exp_f32 v207, v207\n"
"v_mfma_f32_16x16x32_bf16 v[196:199], a[124:127], a[32:35], v[196:199]\n"
"v_cvt_pk_bf16_f32 v215, v206, v207\n"
"v_mfma_f32_16x16x32_bf16 v[200:203], a[128:131], a[32:35], v[228:231]\n"
"v_exp_f32 v192, v192\n"
"v_mfma_f32_16x16x32_bf16 v[200:203], a[132:135], a[36:39], v[200:203]\n"
"v_mfma_f32_16x16x32_bf16 v[200:203], a[136:139], a[40:43], v[200:203]\n"
"v_exp_f32 v193, v193\n"
"v_mfma_f32_16x16x32_bf16 v[200:203], a[140:143], a[44:47], v[200:203]\n"
"v_cvt_pk_bf16_f32 v208, v192, v193\n"
"v_mfma_f32_16x16x32_bf16 v[204:207], a[144:147], a[44:47], v[228:231]\n"
"v_exp_f32 v194, v194\n"
"v_mfma_f32_16x16x32_bf16 v[204:207], a[148:151], a[40:43], v[204:207]\n"
"v_mfma_f32_16x16x32_bf16 v[204:207], a[152:155], a[36:39], v[204:207]\n"
"v_exp_f32 v195, v195\n"
"v_mfma_f32_16x16x32_bf16 v[204:207], a[156:159], a[32:35], v[204:207]\n"
"v_cvt_pk_bf16_f32 v209, v194, v195\n"
"v_smfmac_f32_16x16x64_bf16 a[228:231], a[248:251], v[212:219], v252\n"
"v_exp_f32 v196, v196\n"
"v_mfma_f32_16x16x32_bf16 v[32:35], v[216:219], a[160:163], v[32:35]\n"
"v_mfma_f32_16x16x32_bf16 v[32:35], v[212:215], a[164:167], v[32:35]\n"
"v_exp_f32 v197, v197\n"
"v_mfma_f32_16x16x32_bf16 v[36:39], v[212:215], a[168:171], v[36:39]\n"
"v_cvt_pk_bf16_f32 v210, v196, v197\n"
"v_mfma_f32_16x16x32_bf16 v[36:39], v[216:219], a[172:175], v[36:39]\n"
"v_exp_f32 v198, v198\n"
"v_mfma_f32_16x16x32_bf16 v[40:43], v[216:219], a[176:179], v[40:43]\n"
"v_mfma_f32_16x16x32_bf16 v[40:43], v[212:215], a[180:183], v[40:43]\n"
"v_exp_f32 v199, v199\n"
"v_mfma_f32_16x16x32_bf16 v[44:47], v[212:215], a[184:187], v[44:47]\n"
"v_cvt_pk_bf16_f32 v211, v198, v199\n"
"v_mfma_f32_16x16x32_bf16 v[44:47], v[216:219], a[188:191], v[44:47]\n"
"v_exp_f32 v200, v200\n"
"v_mfma_f32_16x16x32_bf16 v[48:51], v[216:219], a[192:195], v[48:51]\n"
"buffer_load_dwordx4 v251, s[40:43], s24 offen offset:3072 nt lds\n"
"v_mfma_f32_16x16x32_bf16 v[48:51], v[212:215], a[196:199], v[48:51]\n"
"v_exp_f32 v201, v201\n"
"v_mfma_f32_16x16x32_bf16 v[52:55], v[212:215], a[200:203], v[52:55]\n"
"v_mfma_f32_16x16x32_bf16 v[52:55], v[216:219], a[204:207], v[52:55]\n"
"v_exp_f32 v202, v202\n"
"v_mfma_f32_16x16x32_bf16 v[56:59], v[216:219], a[208:211], v[56:59]\n"
"v_mfma_f32_16x16x32_bf16 v[56:59], v[212:215], a[212:215], v[56:59]\n"
"v_exp_f32 v203, v203\n"
"v_mfma_f32_16x16x32_bf16 v[60:63], v[212:215], a[216:219], v[60:63]\n"
"v_mfma_f32_16x16x32_bf16 v[60:63], v[216:219], a[220:223], v[60:63]\n"
"v_mfma_f32_16x16x32_bf16 v[192:195], a[96:99], a[48:51], v[232:235]\n"
"v_exp_f32 v204, v204\n"
"v_mfma_f32_16x16x32_bf16 v[192:195], a[100:103], a[52:55], v[192:195]\n"
"v_cvt_pk_bf16_f32 v212, v200, v201\n"
"v_cvt_pk_bf16_f32 v213, v202, v203\n"
"v_mfma_f32_16x16x32_bf16 v[192:195], a[104:107], a[56:59], v[192:195]\n"
"v_exp_f32 v205, v205\n"
"v_mfma_f32_16x16x32_bf16 v[192:195], a[108:111], a[60:63], v[192:195]\n"
"v_cvt_pk_bf16_f32 v214, v204, v205\n"
"v_mfma_f32_16x16x32_bf16 v[196:199], a[112:115], a[60:63], v[232:235]\n"
"v_exp_f32 v206, v206\n"
"v_mfma_f32_16x16x32_bf16 v[196:199], a[116:119], a[56:59], v[196:199]\n"
"v_mfma_f32_16x16x32_bf16 v[196:199], a[120:123], a[52:55], v[196:199]\n"
"v_exp_f32 v207, v207\n"
"v_mfma_f32_16x16x32_bf16 v[196:199], a[124:127], a[48:51], v[196:199]\n"
"v_cvt_pk_bf16_f32 v215, v206, v207\n"
"v_mfma_f32_16x16x32_bf16 v[200:203], a[128:131], a[48:51], v[232:235]\n"
"v_exp_f32 v192, v192\n"
"v_mfma_f32_16x16x32_bf16 v[200:203], a[132:135], a[52:55], v[200:203]\n"
"v_mfma_f32_16x16x32_bf16 v[200:203], a[136:139], a[56:59], v[200:203]\n"
"v_exp_f32 v193, v193\n"
"v_mfma_f32_16x16x32_bf16 v[200:203], a[140:143], a[60:63], v[200:203]\n"
"v_cvt_pk_bf16_f32 v216, v192, v193\n"
"v_mfma_f32_16x16x32_bf16 v[204:207], a[144:147], a[60:63], v[232:235]\n"
"v_exp_f32 v194, v194\n"
"v_mfma_f32_16x16x32_bf16 v[204:207], a[148:151], a[56:59], v[204:207]\n"
"s_add_u32 m0, s25, 16384\n"
"s_add_u32 s24, s24, 8192\n"
"v_mfma_f32_16x16x32_bf16 v[204:207], a[152:155], a[52:55], v[204:207]\n"
"v_exp_f32 v195, v195\n"
"v_mfma_f32_16x16x32_bf16 v[204:207], a[156:159], a[48:51], v[204:207]\n"
"v_cvt_pk_bf16_f32 v217, v194, v195\n"
"v_smfmac_f32_16x16x64_bf16 a[232:235], a[248:251], v[208:215], v252\n"
"v_exp_f32 v196, v196\n"
"v_mfma_f32_16x16x32_bf16 v[64:67], v[208:211], a[160:163], v[64:67]\n"
"buffer_load_dwordx4 v248, s[36:39], s24 offen nt lds\n"
"v_mfma_f32_16x16x32_bf16 v[64:67], v[212:215], a[164:167], v[64:67]\n"
"v_exp_f32 v197, v197\n"
"v_mfma_f32_16x16x32_bf16 v[68:71], v[212:215], a[168:171], v[68:71]\n"
"v_cvt_pk_bf16_f32 v218, v196, v197\n"
"v_mfma_f32_16x16x32_bf16 v[68:71], v[208:211], a[172:175], v[68:71]\n"
"v_exp_f32 v198, v198\n"
"v_mfma_f32_16x16x32_bf16 v[72:75], v[208:211], a[176:179], v[72:75]\n"
"v_mfma_f32_16x16x32_bf16 v[72:75], v[212:215], a[180:183], v[72:75]\n"
"v_exp_f32 v199, v199\n"
"v_mfma_f32_16x16x32_bf16 v[76:79], v[212:215], a[184:187], v[76:79]\n"
"v_cvt_pk_bf16_f32 v219, v198, v199\n"
"v_mfma_f32_16x16x32_bf16 v[76:79], v[208:211], a[188:191], v[76:79]\n"
"v_exp_f32 v200, v200\n"
"v_mfma_f32_16x16x32_bf16 v[80:83], v[208:211], a[192:195], v[80:83]\n"
"s_add_u32 s27, s27, 1\n"
"v_mov_b32 v255, s27\n"
"v_mfma_f32_16x16x32_bf16 v[80:83], v[212:215], a[196:199], v[80:83]\n"
"v_exp_f32 v201, v201\n"
"v_mfma_f32_16x16x32_bf16 v[84:87], v[212:215], a[200:203], v[84:87]\n"
"v_mfma_f32_16x16x32_bf16 v[84:87], v[208:211], a[204:207], v[84:87]\n"
"v_exp_f32 v202, v202\n"
"v_mfma_f32_16x16x32_bf16 v[88:91], v[208:211], a[208:211], v[88:91]\n"
"v_mfma_f32_16x16x32_bf16 v[88:91], v[212:215], a[212:215], v[88:91]\n"
"v_exp_f32 v203, v203\n"
"v_mfma_f32_16x16x32_bf16 v[92:95], v[212:215], a[216:219], v[92:95]\n"
"s_waitcnt vmcnt(5)\n"
"ds_write_b32 v253, v255\n"
"v_mfma_f32_16x16x32_bf16 v[92:95], v[208:211], a[220:223], v[92:95]\n"
"v_mfma_f32_16x16x32_bf16 v[192:195], a[96:99], a[64:67], v[236:239]\n"
"v_exp_f32 v204, v204\n"
"v_mfma_f32_16x16x32_bf16 v[192:195], a[100:103], a[68:71], v[192:195]\n"
"v_cvt_pk_bf16_f32 v212, v200, v201\n"
"v_cvt_pk_bf16_f32 v213, v202, v203\n"
"v_mfma_f32_16x16x32_bf16 v[192:195], a[104:107], a[72:75], v[192:195]\n"
"v_exp_f32 v205, v205\n"
"v_mfma_f32_16x16x32_bf16 v[192:195], a[108:111], a[76:79], v[192:195]\n"
"v_cvt_pk_bf16_f32 v214, v204, v205\n"
"v_mfma_f32_16x16x32_bf16 v[196:199], a[112:115], a[76:79], v[236:239]\n"
"v_exp_f32 v206, v206\n"
"v_mfma_f32_16x16x32_bf16 v[196:199], a[116:119], a[72:75], v[196:199]\n"
"buffer_load_dwordx4 v249, s[36:39], s24 offen offset:1024 nt lds\n"
"v_mfma_f32_16x16x32_bf16 v[196:199], a[120:123], a[68:71], v[196:199]\n"
"v_exp_f32 v207, v207\n"
"v_mfma_f32_16x16x32_bf16 v[196:199], a[124:127], a[64:67], v[196:199]\n"
"v_cvt_pk_bf16_f32 v215, v206, v207\n"
"v_mfma_f32_16x16x32_bf16 v[200:203], a[128:131], a[64:67], v[236:239]\n"
"v_exp_f32 v192, v192\n"
"v_mfma_f32_16x16x32_bf16 v[200:203], a[132:135], a[68:71], v[200:203]\n"
"v_mfma_f32_16x16x32_bf16 v[200:203], a[136:139], a[72:75], v[200:203]\n"
"v_exp_f32 v193, v193\n"
"v_mfma_f32_16x16x32_bf16 v[200:203], a[140:143], a[76:79], v[200:203]\n"
"v_cvt_pk_bf16_f32 v208, v192, v193\n"
"v_mfma_f32_16x16x32_bf16 v[204:207], a[144:147], a[76:79], v[236:239]\n"
"v_exp_f32 v194, v194\n"
"v_mfma_f32_16x16x32_bf16 v[204:207], a[148:151], a[72:75], v[204:207]\n"
"v_mfma_f32_16x16x32_bf16 v[204:207], a[152:155], a[68:71], v[204:207]\n"
"v_exp_f32 v195, v195\n"
"v_mfma_f32_16x16x32_bf16 v[204:207], a[156:159], a[64:67], v[204:207]\n"
"v_cvt_pk_bf16_f32 v209, v194, v195\n"
"v_smfmac_f32_16x16x64_bf16 a[236:239], a[248:251], v[212:219], v252\n"
"v_exp_f32 v196, v196\n"
"v_mfma_f32_16x16x32_bf16 v[96:99], v[216:219], a[160:163], v[96:99]\n"
"v_mfma_f32_16x16x32_bf16 v[96:99], v[212:215], a[164:167], v[96:99]\n"
"v_exp_f32 v197, v197\n"
"v_mfma_f32_16x16x32_bf16 v[100:103], v[212:215], a[168:171], v[100:103]\n"
"v_cvt_pk_bf16_f32 v210, v196, v197\n"
"v_mfma_f32_16x16x32_bf16 v[100:103], v[216:219], a[172:175], v[100:103]\n"
"v_exp_f32 v198, v198\n"
"v_mfma_f32_16x16x32_bf16 v[104:107], v[216:219], a[176:179], v[104:107]\n"
"ds_read_b32 v255, v254\n"
"v_mfma_f32_16x16x32_bf16 v[104:107], v[212:215], a[180:183], v[104:107]\n"
"v_exp_f32 v199, v199\n"
"v_mfma_f32_16x16x32_bf16 v[108:111], v[212:215], a[184:187], v[108:111]\n"
"v_cvt_pk_bf16_f32 v211, v198, v199\n"
"v_mfma_f32_16x16x32_bf16 v[108:111], v[216:219], a[188:191], v[108:111]\n"
"v_exp_f32 v200, v200\n"
"v_mfma_f32_16x16x32_bf16 v[112:115], v[216:219], a[192:195], v[112:115]\n"
"buffer_load_dwordx4 v250, s[40:43], s24 offen offset:2048 nt lds\n"
"v_mfma_f32_16x16x32_bf16 v[112:115], v[212:215], a[196:199], v[112:115]\n"
"v_exp_f32 v201, v201\n"
"v_mfma_f32_16x16x32_bf16 v[116:119], v[212:215], a[200:203], v[116:119]\n"
"v_mfma_f32_16x16x32_bf16 v[116:119], v[216:219], a[204:207], v[116:119]\n"
"v_exp_f32 v202, v202\n"
"v_mfma_f32_16x16x32_bf16 v[120:123], v[216:219], a[208:211], v[120:123]\n"
"v_mfma_f32_16x16x32_bf16 v[120:123], v[212:215], a[212:215], v[120:123]\n"
"v_exp_f32 v203, v203\n"
"v_mfma_f32_16x16x32_bf16 v[124:127], v[212:215], a[216:219], v[124:127]\n"
"s_waitcnt lgkmcnt(0)\n"
"v_cmp_gt_u32 vcc, s27, v255\n"
"v_mfma_f32_16x16x32_bf16 v[124:127], v[216:219], a[220:223], v[124:127]\n"
"s_cbranch_vccnz L_late6_3_%=\n"
"L_back6_3_%=:\n"
"v_mfma_f32_16x16x32_bf16 v[192:195], a[96:99], a[80:83], v[240:243]\n"
"v_exp_f32 v204, v204\n"
"v_mfma_f32_16x16x32_bf16 v[192:195], a[100:103], a[84:87], v[192:195]\n"
"v_cvt_pk_bf16_f32 v212, v200, v201\n"
"v_cvt_pk_bf16_f32 v213, v202, v203\n"
"v_mfma_f32_16x16x32_bf16 v[192:195], a[104:107], a[88:91], v[192:195]\n"
"v_exp_f32 v205, v205\n"
"v_mfma_f32_16x16x32_bf16 v[192:195], a[108:111], a[92:95], v[192:195]\n"
"ds_read_b128 a[96:99], v244 offset:32768\n"
"v_cvt_pk_bf16_f32 v214, v204, v205\n"
"v_mfma_f32_16x16x32_bf16 v[196:199], a[112:115], a[92:95], v[240:243]\n"
"v_exp_f32 v206, v206\n"
"v_mfma_f32_16x16x32_bf16 v[196:199], a[116:119], a[88:91], v[196:199]\n"
"ds_read_b128 a[100:103], v245 offset:32768\n"
"ds_read_b128 a[104:107], v244 offset:33792\n"
"v_mfma_f32_16x16x32_bf16 v[196:199], a[120:123], a[84:87], v[196:199]\n"
"v_exp_f32 v207, v207\n"
"v_mfma_f32_16x16x32_bf16 v[196:199], a[124:127], a[80:83], v[196:199]\n"
"ds_read_b128 a[108:111], v245 offset:33792\n"
"v_cvt_pk_bf16_f32 v215, v206, v207\n"
"v_mfma_f32_16x16x32_bf16 v[200:203], a[128:131], a[80:83], v[240:243]\n"
"v_exp_f32 v192, v192\n"
"v_mfma_f32_16x16x32_bf16 v[200:203], a[132:135], a[84:87], v[200:203]\n"
"ds_read_b128 a[112:115], v245 offset:41984\n"
"ds_read_b128 a[116:119], v244 offset:41984\n"
"v_mfma_f32_16x16x32_bf16 v[200:203], a[136:139], a[88:91], v[200:203]\n"
"v_exp_f32 v193, v193\n"
"v_mfma_f32_16x16x32_bf16 v[200:203], a[140:143], a[92:95], v[200:203]\n"
"ds_read_b128 a[120:123], v245 offset:40960\n"
"v_cvt_pk_bf16_f32 v216, v192, v193\n"
"v_mfma_f32_16x16x32_bf16 v[204:207], a[144:147], a[92:95], v[240:243]\n"
"v_exp_f32 v194, v194\n"
"v_mfma_f32_16x16x32_bf16 v[204:207], a[148:151], a[88:91], v[204:207]\n"
"ds_read_b128 a[124:127], v244 offset:40960\n"
"v_mfma_f32_16x16x32_bf16 v[204:207], a[152:155], a[84:87], v[204:207]\n"
"v_exp_f32 v195, v195\n"
"v_mfma_f32_16x16x32_bf16 v[204:207], a[156:159], a[80:83], v[204:207]\n"
"ds_read_b128 a[128:131], v244 offset:49152\n"
"v_cvt_pk_bf16_f32 v217, v194, v195\n"
"v_smfmac_f32_16x16x64_bf16 a[240:243], a[248:251], v[208:215], v252\n"
"v_exp_f32 v196, v196\n"
"v_mfma_f32_16x16x32_bf16 v[128:131], v[208:211], a[160:163], v[128:131]\n"
"ds_read_b128 a[132:135], v245 offset:49152\n"
"v_mfma_f32_16x16x32_bf16 v[128:131], v[212:215], a[164:167], v[128:131]\n"
"v_exp_f32 v197, v197\n"
"v_mfma_f32_16x16x32_bf16 v[132:135], v[212:215], a[168:171], v[132:135]\n"
"ds_read_b128 a[136:139], v244 offset:50176\n"
"v_cvt_pk_bf16_f32 v218, v196, v197\n"
"v_mfma_f32_16x16x32_bf16 v[132:135], v[208:211], a[172:175], v[132:135]\n"
"v_exp_f32 v198, v198\n"
"v_mfma_f32_16x16x32_bf16 v[136:139], v[208:211], a[176:179], v[136:139]\n"
"ds_read_b128 a[140:143], v245 offset:50176\n"
"ds_read_b128 a[144:147], v245 offset:58368\n"
"v_mfma_f32_16x16x32_bf16 v[136:139], v[212:215], a[180:183], v[136:139]\n"
"v_exp_f32 v199, v199\n"
"v_mfma_f32_16x16x32_bf16 v[140:143], v[212:215], a[184:187], v[140:143]\n"
"ds_read_b128 a[148:151], v244 offset:58368\n"
"v_cvt_pk_bf16_f32 v219, v198, v199\n"
"v_mfma_f32_16x16x32_bf16 v[140:143], v[208:211], a[188:191], v[140:143]\n"
"v_exp_f32 v200, v200\n"
"v_mfma_f32_16x16x32_bf16 v[144:147], v[208:211], a[192:195], v[144:147]\n"
"ds_read_b128 a[152:155], v245 offset:57344\n"
"v_mfma_f32_16x16x32_bf16 v[144:147], v[212:215], a[196:199], v[144:147]\n"
"v_exp_f32 v201, v201\n"
"v_mfma_f32_16x16x32_bf16 v[148:151], v[212:215], a[200:203], v[148:151]\n"
"buffer_load_dwordx4 v251, s[40:43], s24 offen offset:3072 nt lds\n"
"v_mfma_f32_16x16x32_bf16 v[148:151], v[208:211], a[204:207], v[148:151]\n"
"v_exp_f32 v202, v202\n"
"v_mfma_f32_16x16x32_bf16 v[152:155], v[208:211], a[208:211], v[152:155]\n"
"ds_read_b128 a[156:159], v244 offset:57344\n"
"v_mfma_f32_16x16x32_bf16 v[152:155], v[212:215], a[212:215], v[152:155]\n"
"v_exp_f32 v203, v203\n"
"v_mfma_f32_16x16x32_bf16 v[156:159], v[212:215], a[216:219], v[156:159]\n"
"v_mfma_f32_16x16x32_bf16 v[156:159], v[208:211], a[220:223], v[156:159]\n"
"s_waitcnt lgkmcnt(8)\n"
"v_mfma_f32_16x16x32_bf16 v[192:195], a[96:99], a[0:3], v[220:223]\n"
"v_exp_f32 v204, v204\n"
"v_mfma_f32_16x16x32_bf16 v[192:195], a[100:103], a[4:7], v[192:195]\n"
"v_cvt_pk_bf16_f32 v212, v200, v201\n"
"v_cvt_pk_bf16_f32 v213, v202, v203\n"
"v_mfma_f32_16x16x32_bf16 v[192:195], a[104:107], a[8:11], v[192:195]\n"
"v_exp_f32 v205, v205\n"
"v_mfma_f32_16x16x32_bf16 v[192:195], a[108:111], a[12:15], v[192:195]\n"
"v_cvt_pk_bf16_f32 v214, v204, v205\n"
"s_add_u32 m0, s25, 32768\n"
"s_add_u32 s24, s24, 8192\n"
"v_mfma_f32_16x16x32_bf16 v[196:199], a[112:115], a[12:15], v[220:223]\n"
"v_exp_f32 v206, v206\n"
"v_mfma_f32_16x16x32_bf16 v[196:199], a[116:119], a[8:11], v[196:199]\n"
"buffer_load_dwordx4 v248, s[36:39], s24 offen nt lds\n"
"v_mfma_f32_16x16x32_bf16 v[196:199], a[120:123], a[4:7], v[196:199]\n"
"v_exp_f32 v207, v207\n"
"v_mfma_f32_16x16x32_bf16 v[196:199], a[124:127], a[0:3], v[196:199]\n"
"v_cvt_pk_bf16_f32 v215, v206, v207\n"
"s_waitcnt lgkmcnt(0)\n"
"v_mfma_f32_16x16x32_bf16 v[200:203], a[128:131], a[0:3], v[220:223]\n"
"v_exp_f32 v192, v192\n"
"v_mfma_f32_16x16x32_bf16 v[200:203], a[132:135], a[4:7], v[200:203]\n"
"v_mfma_f32_16x16x32_bf16 v[200:203], a[136:139], a[8:11], v[200:203]\n"
"v_exp_f32 v193, v193\n"
"v_mfma_f32_16x16x32_bf16 v[200:203], a[140:143], a[12:15], v[200:203]\n"
"v_cvt_pk_bf16_f32 v208, v192, v193\n"
"v_mfma_f32_16x16x32_bf16 v[204:207], a[144:147], a[12:15], v[220:223]\n"
"v_exp_f32 v194, v194\n"
"v_mfma_f32_16x16x32_bf16 v[204:207], a[148:151], a[8:11], v[204:207]\n"
"v_mfma_f32_16x16x32_bf16 v[204:207], a[152:155], a[4:7], v[204:207]\n"
"v_exp_f32 v195, v195\n"
"v_mfma_f32_16x16x32_bf16 v[204:207], a[156:159], a[0:3], v[204:207]\n"
"v_cvt_pk_bf16_f32 v209, v194, v195\n"
"v_smfmac_f32_16x16x64_bf16 a[244:247], a[248:251], v[212:219], v252\n"
"v_exp_f32 v196, v196\n"
"v_mfma_f32_16x16x32_bf16 v[160:163], v[216:219], a[160:163], v[160:163]\n"
"ds_read_b64_tr_b16 a[160:161], v246 offset:34816\n"
"ds_read_b64_tr_b16 a[162:163], v246 offset:43008\n"
"v_mfma_f32_16x16x32_bf16 v[160:163], v[212:215], a[164:167], v[160:163]\n"
"v_exp_f32 v197, v197\n"
"v_mfma_f32_16x16x32_bf16 v[164:167], v[212:215], a[168:171], v[164:167]\n"
"ds_read_b64_tr_b16 a[164:165], v246 offset:51200\n"
"v_cvt_pk_bf16_f32 v210, v196, v197\n"
"v_mfma_f32_16x16x32_bf16 v[164:167], v[216:219], a[172:175], v[164:167]\n"
"v_exp_f32 v198, v198\n"
"v_mfma_f32_16x16x32_bf16 v[168:171], v[216:219], a[176:179], v[168:171]\n"
"ds_read_b64_tr_b16 a[166:167], v246 offset:59392\n"
"ds_read_b64_tr_b16 a[168:169], v247 offset:51200\n"
"v_mfma_f32_16x16x32_bf16 v[168:171], v[212:215], a[180:183], v[168:171]\n"
"v_exp_f32 v199, v199\n"
"v_mfma_f32_16x16x32_bf16 v[172:175], v[212:215], a[184:187], v[172:175]\n"
"ds_read_b64_tr_b16 a[170:171], v247 offset:59392\n"
"v_cvt_pk_bf16_f32 v211, v198, v199\n"
"v_mfma_f32_16x16x32_bf16 v[172:175], v[216:219], a[188:191], v[172:175]\n"
"v_exp_f32 v200, v200\n"
"v_mfma_f32_16x16x32_bf16 v[176:179], v[216:219], a[192:195], v[176:179]\n"
"ds_read_b64_tr_b16 a[172:173], v247 offset:34816\n"
"ds_read_b64_tr_b16 a[174:175], v247 offset:43008\n"
"v_mfma_f32_16x16x32_bf16 v[176:179], v[212:215], a[196:199], v[176:179]\n"
"v_exp_f32 v201, v201\n"
"v_mfma_f32_16x16x32_bf16 v[180:183], v[212:215], a[200:203], v[180:183]\n"
"ds_read_b64_tr_b16 a[176:177], v246 offset:35328\n"
"ds_read_b64_tr_b16 a[178:179], v246 offset:43520\n"
"v_mfma_f32_16x16x32_bf16 v[180:183], v[216:219], a[204:207], v[180:183]\n"
"v_exp_f32 v202, v202\n"
"v_mfma_f32_16x16x32_bf16 v[184:187], v[216:219], a[208:211], v[184:187]\n"
"ds_read_b64_tr_b16 a[180:181], v246 offset:51712\n"
"ds_read_b64_tr_b16 a[182:183], v246 offset:59904\n"
"v_mfma_f32_16x16x32_bf16 v[184:187], v[212:215], a[212:215], v[184:187]\n"
"v_exp_f32 v203, v203\n"
"v_mfma_f32_16x16x32_bf16 v[188:191], v[212:215], a[216:219], v[188:191]\n"
"ds_read_b64_tr_b16 a[184:185], v247 offset:51712\n"
"ds_read_b64_tr_b16 a[186:187], v247 offset:59904\n"
"v_mfma_f32_16x16x32_bf16 v[188:191], v[216:219], a[220:223], v[188:191]\n"
"ds_read_b64_tr_b16 a[188:189], v247 offset:35328\n"
"ds_read_b64_tr_b16 a[190:191], v247 offset:43520\n"
"v_mfma_f32_16x16x32_bf16 v[192:195], a[96:99], a[16:19], v[224:227]\n"
"v_exp_f32 v204, v204\n"
"v_mfma_f32_16x16x32_bf16 v[192:195], a[100:103], a[20:23], v[192:195]\n"
"v_cvt_pk_bf16_f32 v212, v200, v201\n"
"v_cvt_pk_bf16_f32 v213, v202, v203\n"
"v_mfma_f32_16x16x32_bf16 v[192:195], a[104:107], a[24:27], v[192:195]\n"
"v_exp_f32 v205, v205\n"
"v_mfma_f32_16x16x32_bf16 v[192:195], a[108:111], a[28:31], v[192:195]\n"
"ds_read_b64_tr_b16 a[192:193], v246 offset:35840\n"
"v_cvt_pk_bf16_f32 v214, v204, v205\n"
"v_mfma_f32_16x16x32_bf16 v[196:199], a[112:115], a[28:31], v[224:227]\n"
"v_exp_f32 v206, v206\n"
"v_mfma_f32_16x16x32_bf16 v[196:199], a[116:119], a[24:27], v[196:199]\n"
"ds_read_b64_tr_b16 a[194:195], v246 offset:44032\n"
"ds_read_b64_tr_b16 a[196:197], v246 offset:52224\n"
"v_mfma_f32_16x16x32_bf16 v[196:199], a[120:123], a[20:23], v[196:199]\n"
"v_exp_f32 v207, v207\n"
"v_mfma_f32_16x16x32_bf16 v[196:199], a[124:127], a[16:19], v[196:199]\n"
"ds_read_b64_tr_b16 a[198:199], v246 offset:60416\n"
"v_cvt_pk_bf16_f32 v215, v206, v207\n"
"v_mfma_f32_16x16x32_bf16 v[200:203], a[128:131], a[16:19], v[224:227]\n"
"v_exp_f32 v192, v192\n"
"v_mfma_f32_16x16x32_bf16 v[200:203], a[132:135], a[20:23], v[200:203]\n"
"ds_read_b64_tr_b16 a[200:201], v247 offset:52224\n"
"ds_read_b64_tr_b16 a[202:203], v247 offset:60416\n"
"v_mfma_f32_16x16x32_bf16 v[200:203], a[136:139], a[24:27], v[200:203]\n"
"v_exp_f32 v193, v193\n"
"v_mfma_f32_16x16x32_bf16 v[200:203], a[140:143], a[28:31], v[200:203]\n"
"ds_read_b64_tr_b16 a[204:205], v247 offset:35840\n"
"v_cvt_pk_bf16_f32 v216, v192, v193\n"
"v_mfma_f32_16x16x32_bf16 v[204:207], a[144:147], a[28:31], v[224:227]\n"
"v_exp_f32 v194, v194\n"
"v_mfma_f32_16x16x32_bf16 v[204:207], a[148:151], a[24:27], v[204:207]\n"
"ds_read_b64_tr_b16 a[206:207], v247 offset:44032\n"
"ds_read_b64_tr_b16 a[208:209], v246 offset:36352\n"
"v_mfma_f32_16x16x32_bf16 v[204:207], a[152:155], a[20:23], v[204:207]\n"
"v_exp_f32 v195, v195\n"
"v_mfma_f32_16x16x32_bf16 v[204:207], a[156:159], a[16:19], v[204:207]\n"
"v_cvt_pk_bf16_f32 v217, v194, v195\n"
"s_waitcnt lgkmcnt(15)\n"
"v_smfmac_f32_16x16x64_bf16 a[224:227], a[248:251], v[208:215], v252\n"
"v_exp_f32 v196, v196\n"
"v_mfma_f32_16x16x32_bf16 v[0:3], v[208:211], a[160:163], v[0:3]\n"
"ds_read_b64_tr_b16 a[210:211], v246 offset:44544\n"
"ds_read_b64_tr_b16 a[212:213], v246 offset:52736\n"
"v_mfma_f32_16x16x32_bf16 v[0:3], v[212:215], a[164:167], v[0:3]\n"
"v_exp_f32 v197, v197\n"
"v_mfma_f32_16x16x32_bf16 v[4:7], v[212:215], a[168:171], v[4:7]\n"
"v_cvt_pk_bf16_f32 v218, v196, v197\n"
"s_waitcnt lgkmcnt(11)\n"
"v_mfma_f32_16x16x32_bf16 v[4:7], v[208:211], a[172:175], v[4:7]\n"
"v_exp_f32 v198, v198\n"
"v_mfma_f32_16x16x32_bf16 v[8:11], v[208:211], a[176:179], v[8:11]\n"
"ds_read_b64_tr_b16 a[214:215], v246 offset:60928\n"
"ds_read_b64_tr_b16 a[216:217], v247 offset:52736\n"
"ds_read_b64_tr_b16 a[218:219], v247 offset:60928\n"
"ds_read_b64_tr_b16 a[220:221], v247 offset:36352\n"
"ds_read_b64_tr_b16 a[222:223], v247 offset:44544\n"
"v_mfma_f32_16x16x32_bf16 v[8:11], v[212:215], a[180:183], v[8:11]\n"
"v_exp_f32 v199, v199\n"
"v_mfma_f32_16x16x32_bf16 v[12:15], v[212:215], a[184:187], v[12:15]\n"
"v_cvt_pk_bf16_f32 v219, v198, v199\n"
"s_waitcnt lgkmcnt(8)\n"
"v_mfma_f32_16x16x32_bf16 v[12:15], v[208:211], a[188:191], v[12:15]\n"
"v_exp_f32 v200, v200\n"
"v_mfma_f32_16x16x32_bf16 v[16:19], v[208:211], a[192:195], v[16:19]\n"
"buffer_load_dwordx4 v249, s[36:39], s24 offen offset:1024 nt lds\n"
"v_mfma_f32_16x16x32_bf16 v[16:19], v[212:215], a[196:199], v[16:19]\n"
"v_exp_f32 v201, v201\n"
"v_mfma_f32_16x16x32_bf16 v[20:23], v[212:215], a[200:203], v[20:23]\n"
"s_waitcnt lgkmcnt(0)\n"
"v_mfma_f32_16x16x32_bf16 v[20:23], v[208:211], a[204:207], v[20:23]\n"
"v_exp_f32 v202, v202\n"
"v_mfma_f32_16x16x32_bf16 v[24:27], v[208:211], a[208:211], v[24:27]\n"
"buffer_load_dwordx4 v250, s[40:43], s24 offen offset:2048 nt lds\n"
"v_mfma_f32_16x16x32_bf16 v[24:27], v[212:215], a[212:215], v[24:27]\n"
"v_exp_f32 v203, v203\n"
"v_mfma_f32_16x16x32_bf16 v[28:31], v[212:215], a[216:219], v[28:31]\n"
"v_mfma_f32_16x16x32_bf16 v[28:31], v[208:211], a[220:223], v[28:31]\n"
"v_mfma_f32_16x16x32_bf16 v[192:195], a[96:99], a[32:35], v[228:231]\n"
"v_exp_f32 v204, v204\n"
"v_mfma_f32_16x16x32_bf16 v[192:195], a[100:103], a[36:39], v[192:195]\n"
"v_cvt_pk_bf16_f32 v212, v200, v201\n"
"v_cvt_pk_bf16_f32 v213, v202, v203\n"
"v_mfma_f32_16x16x32_bf16 v[192:195], a[104:107], a[40:43], v[192:195]\n"
"v_exp_f32 v205, v205\n"
"v_mfma_f32_16x16x32_bf16 v[192:195], a[108:111], a[44:47], v[192:195]\n"
"v_cvt_pk_bf16_f32 v214, v204, v205\n"
"v_mfma_f32_16x16x32_bf16 v[196:199], a[112:115], a[44:47], v[228:231]\n"
"v_exp_f32 v206, v206\n"
"v_mfma_f32_16x16x32_bf16 v[196:199], a[116:119], a[40:43], v[196:199]\n"
"buffer_load_dwordx4 v251, s[40:43], s24 offen offset:3072 nt lds\n"
"v_mfma_f32_16x16x32_bf16 v[196:199], a[120:123], a[36:39], v[196:199]\n"
"v_exp_f32 v207, v207\n"
"v_mfma_f32_16x16x32_bf16 v[196:199], a[124:127], a[32:35], v[196:199]\n"
"v_cvt_pk_bf16_f32 v215, v206, v207\n"
"v_mfma_f32_16x16x32_bf16 v[200:203], a[128:131], a[32:35], v[228:231]\n"
"v_exp_f32 v192, v192\n"
"v_mfma_f32_16x16x32_bf16 v[200:203], a[132:135], a[36:39], v[200:203]\n"
"v_mfma_f32_16x16x32_bf16 v[200:203], a[136:139], a[40:43], v[200:203]\n"
"v_exp_f32 v193, v193\n"
"v_mfma_f32_16x16x32_bf16 v[200:203], a[140:143], a[44:47], v[200:203]\n"
"v_cvt_pk_bf16_f32 v208, v192, v193\n"
"v_mfma_f32_16x16x32_bf16 v[204:207], a[144:147], a[44:47], v[228:231]\n"
"v_exp_f32 v194, v194\n"
"v_mfma_f32_16x16x32_bf16 v[204:207], a[148:151], a[40:43], v[204:207]\n"
"v_mfma_f32_16x16x32_bf16 v[204:207], a[152:155], a[36:39], v[204:207]\n"
"v_exp_f32 v195, v195\n"
"v_mfma_f32_16x16x32_bf16 v[204:207], a[156:159], a[32:35], v[204:207]\n"
"v_cvt_pk_bf16_f32 v209, v194, v195\n"
"v_smfmac_f32_16x16x64_bf16 a[228:231], a[248:251], v[212:219], v252\n"
"v_exp_f32 v196, v196\n"
"v_mfma_f32_16x16x32_bf16 v[32:35], v[216:219], a[160:163], v[32:35]\n"
"v_mfma_f32_16x16x32_bf16 v[32:35], v[212:215], a[164:167], v[32:35]\n"
"v_exp_f32 v197, v197\n"
"v_mfma_f32_16x16x32_bf16 v[36:39], v[212:215], a[168:171], v[36:39]\n"
"v_cvt_pk_bf16_f32 v210, v196, v197\n"
"v_mfma_f32_16x16x32_bf16 v[36:39], v[216:219], a[172:175], v[36:39]\n"
"v_exp_f32 v198, v198\n"
"v_mfma_f32_16x16x32_bf16 v[40:43], v[216:219], a[176:179], v[40:43]\n"
"v_mfma_f32_16x16x32_bf16 v[40:43], v[212:215], a[180:183], v[40:43]\n"
"v_exp_f32 v199, v199\n"
"v_mfma_f32_16x16x32_bf16 v[44:47], v[212:215], a[184:187], v[44:47]\n"
"v_cvt_pk_bf16_f32 v211, v198, v199\n"
"v_mfma_f32_16x16x32_bf16 v[44:47], v[216:219], a[188:191], v[44:47]\n"
"v_exp_f32 v200, v200\n"
"v_mfma_f32_16x16x32_bf16 v[48:51], v[216:219], a[192:195], v[48:51]\n"
"s_add_u32 m0, s25, 49152\n"
"s_add_u32 s24, s24, 8192\n"
"v_mfma_f32_16x16x32_bf16 v[48:51], v[212:215], a[196:199], v[48:51]\n"
"v_exp_f32 v201, v201\n"
"v_mfma_f32_16x16x32_bf16 v[52:55], v[212:215], a[200:203], v[52:55]\n"
"v_mfma_f32_16x16x32_bf16 v[52:55], v[216:219], a[204:207], v[52:55]\n"
"v_exp_f32 v202, v202\n"
"v_mfma_f32_16x16x32_bf16 v[56:59], v[216:219], a[208:211], v[56:59]\n"
"buffer_load_dwordx4 v248, s[36:39], s24 offen nt lds\n"
"v_mfma_f32_16x16x32_bf16 v[56:59], v[212:215], a[212:215], v[56:59]\n"
"v_exp_f32 v203, v203\n"
"v_mfma_f32_16x16x32_bf16 v[60:63], v[212:215], a[216:219], v[60:63]\n"
"v_mfma_f32_16x16x32_bf16 v[60:63], v[216:219], a[220:223], v[60:63]\n"
"v_mfma_f32_16x16x32_bf16 v[192:195], a[96:99], a[48:51], v[232:235]\n"
"v_exp_f32 v204, v204\n"
"v_mfma_f32_16x16x32_bf16 v[192:195], a[100:103], a[52:55], v[192:195]\n"
"v_cvt_pk_bf16_f32 v212, v200, v201\n"
"v_cvt_pk_bf16_f32 v213, v202, v203\n"
"v_mfma_f32_16x16x32_bf16 v[192:195], a[104:107], a[56:59], v[192:195]\n"
"v_exp_f32 v205, v205\n"
"v_mfma_f32_16x16x32_bf16 v[192:195], a[108:111], a[60:63], v[192:195]\n"
"v_cvt_pk_bf16_f32 v214, v204, v205\n"
"v_mfma_f32_16x16x32_bf16 v[196:199], a[112:115], a[60:63], v[232:235]\n"
"v_exp_f32 v206, v206\n"
"v_mfma_f32_16x16x32_bf16 v[196:199], a[116:119], a[56:59], v[196:199]\n"
"v_mfma_f32_16x16x32_bf16 v[196:199], a[120:123], a[52:55], v[196:199]\n"
"v_exp_f32 v207, v207\n"
"v_mfma_f32_16x16x32_bf16 v[196:199], a[124:127], a[48:51], v[196:199]\n"
"v_cvt_pk_bf16_f32 v215, v206, v207\n"
"v_mfma_f32_16x16x32_bf16 v[200:203], a[128:131], a[48:51], v[232:235]\n"
"v_exp_f32 v192, v192\n"
"v_mfma_f32_16x16x32_bf16 v[200:203], a[132:135], a[52:55], v[200:203]\n"
"v_mfma_f32_16x16x32_bf16 v[200:203], a[136:139], a[56:59], v[200:203]\n"
"v_exp_f32 v193, v193\n"
"v_mfma_f32_16x16x32_bf16 v[200:203], a[140:143], a[60:63], v[200:203]\n"
"v_cvt_pk_bf16_f32 v216, v192, v193\n"
"v_mfma_f32_16x16x32_bf16 v[204:207], a[144:147], a[60:63], v[232:235]\n"
"v_exp_f32 v194, v194\n"
"v_mfma_f32_16x16x32_bf16 v[204:207], a[148:151], a[56:59], v[204:207]\n"
"v_mfma_f32_16x16x32_bf16 v[204:207], a[152:155], a[52:55], v[204:207]\n"
"v_exp_f32 v195, v195\n"
"v_mfma_f32_16x16x32_bf16 v[204:207], a[156:159], a[48:51], v[204:207]\n"
"v_cvt_pk_bf16_f32 v217, v194, v195\n"
"v_smfmac_f32_16x16x64_bf16 a[232:235], a[248:251], v[208:215], v252\n"
"v_exp_f32 v196, v196\n"
"v_mfma_f32_16x16x32_bf16 v[64:67], v[208:211], a[160:163], v[64:67]\n"
"buffer_load_dwordx4 v249, s[36:39], s24 offen offset:1024 nt lds\n"
"v_mfma_f32_16x16x32_bf16 v[64:67], v[212:215], a[164:167], v[64:67]\n"
"v_exp_f32 v197, v197\n"
"v_mfma_f32_16x16x32_bf16 v[68:71], v[212:215], a[168:171], v[68:71]\n"
"v_cvt_pk_bf16_f32 v218, v196, v197\n"
"v_mfma_f32_16x16x32_bf16 v[68:71], v[208:211], a[172:175], v[68:71]\n"
"v_exp_f32 v198, v198\n"
"v_mfma_f32_16x16x32_bf16 v[72:75], v[208:211], a[176:179], v[72:75]\n"
"v_mfma_f32_16x16x32_bf16 v[72:75], v[212:215], a[180:183], v[72:75]\n"
"v_exp_f32 v199, v199\n"
"v_mfma_f32_16x16x32_bf16 v[76:79], v[212:215], a[184:187], v[76:79]\n"
"v_cvt_pk_bf16_f32 v219, v198, v199\n"
"v_mfma_f32_16x16x32_bf16 v[76:79], v[208:211], a[188:191], v[76:79]\n"
"v_exp_f32 v200, v200\n"
"v_mfma_f32_16x16x32_bf16 v[80:83], v[208:211], a[192:195], v[80:83]\n"
"s_add_u32 s27, s27, 1\n"
"v_mov_b32 v255, s27\n"
"v_mfma_f32_16x16x32_bf16 v[80:83], v[212:215], a[196:199], v[80:83]\n"
"v_exp_f32 v201, v201\n"
"v_mfma_f32_16x16x32_bf16 v[84:87], v[212:215], a[200:203], v[84:87]\n"
"v_mfma_f32_16x16x32_bf16 v[84:87], v[208:211], a[204:207], v[84:87]\n"
"v_exp_f32 v202, v202\n"
"v_mfma_f32_16x16x32_bf16 v[88:91], v[208:211], a[208:211], v[88:91]\n"
"v_mfma_f32_16x16x32_bf16 v[88:91], v[212:215], a[212:215], v[88:91]\n"
"v_exp_f32 v203, v203\n"
"v_mfma_f32_16x16x32_bf16 v[92:95], v[212:215], a[216:219], v[92:95]\n"
"s_waitcnt vmcnt(6)\n"
"ds_write_b32 v253, v255\n"
"v_mfma_f32_16x16x32_bf16 v[92:95], v[208:211], a[220:223], v[92:95]\n"
"v_mfma_f32_16x16x32_bf16 v[192:195], a[96:99], a[64:67], v[236:239]\n"
"v_exp_f32 v204, v204\n"
"v_mfma_f32_16x16x32_bf16 v[192:195], a[100:103], a[68:71], v[192:195]\n"
"v_cvt_pk_bf16_f32 v212, v200, v201\n"
"v_cvt_pk_bf16_f32 v213, v202, v203\n"
"v_mfma_f32_16x16x32_bf16 v[192:195], a[104:107], a[72:75], v[192:195]\n"
"v_exp_f32 v205, v205\n"
"v_mfma_f32_16x16x32_bf16 v[192:195], a[108:111], a[76:79], v[192:195]\n"
"v_cvt_pk_bf16_f32 v214, v204, v205\n"
"v_mfma_f32_16x16x32_bf16 v[196:199], a[112:115], a[76:79], v[236:239]\n"
"v_exp_f32 v206, v206\n"
"v_mfma_f32_16x16x32_bf16 v[196:199], a[116:119], a[72:75], v[196:199]\n"
"buffer_load_dwordx4 v250, s[40:43], s24 offen offset:2048 nt lds\n"
"v_mfma_f32_16x16x32_bf16 v[196:199], a[120:123], a[68:71], v[196:199]\n"
"v_exp_f32 v207, v207\n"
"v_mfma_f32_16x16x32_bf16 v[196:199], a[124:127], a[64:67], v[196:199]\n"
"v_cvt_pk_bf16_f32 v215, v206, v207\n"
"v_mfma_f32_16x16x32_bf16 v[200:203], a[128:131], a[64:67], v[236:239]\n"
"v_exp_f32 v192, v192\n"
"v_mfma_f32_16x16x32_bf16 v[200:203], a[132:135], a[68:71], v[200:203]\n"
"v_xor_b32 v244, 0x10000, v244\n"
"v_xor_b32 v245, 0x10000, v245\n"
"v_mfma_f32_16x16x32_bf16 v[200:203], a[136:139], a[72:75], v[200:203]\n"
"v_exp_f32 v193, v193\n"
"v_mfma_f32_16x16x32_bf16 v[200:203], a[140:143], a[76:79], v[200:203]\n"
"v_cvt_pk_bf16_f32 v208, v192, v193\n"
"v_mfma_f32_16x16x32_bf16 v[204:207], a[144:147], a[76:79], v[236:239]\n"
"v_exp_f32 v194, v194\n"
"v_mfma_f32_16x16x32_bf16 v[204:207], a[148:151], a[72:75], v[204:207]\n"
"v_mfma_f32_16x16x32_bf16 v[204:207], a[152:155], a[68:71], v[204:207]\n"
"v_exp_f32 v195, v195\n"
"v_mfma_f32_16x16x32_bf16 v[204:207], a[156:159], a[64:67], v[204:207]\n"
"v_cvt_pk_bf16_f32 v209, v194, v195\n"
"v_smfmac_f32_16x16x64_bf16 a[236:239], a[248:251], v[212:219], v252\n"
"v_exp_f32 v196, v196\n"
"v_mfma_f32_16x16x32_bf16 v[96:99], v[216:219], a[160:163], v[96:99]\n"
"v_mfma_f32_16x16x32_bf16 v[96:99], v[212:215], a[164:167], v[96:99]\n"
"v_exp_f32 v197, v197\n"
"v_mfma_f32_16x16x32_bf16 v[100:103], v[212:215], a[168:171], v[100:103]\n"
"v_cvt_pk_bf16_f32 v210, v196, v197\n"
"v_mfma_f32_16x16x32_bf16 v[100:103], v[216:219], a[172:175], v[100:103]\n"
"v_exp_f32 v198, v198\n"
"v_mfma_f32_16x16x32_bf16 v[104:107], v[216:219], a[176:179], v[104:107]\n"
"ds_read_b32 v255, v254\n"
"v_mfma_f32_16x16x32_bf16 v[104:107], v[212:215], a[180:183], v[104:107]\n"
"v_exp_f32 v199, v199\n"
"v_mfma_f32_16x16x32_bf16 v[108:111], v[212:215], a[184:187], v[108:111]\n"
"v_cvt_pk_bf16_f32 v211, v198, v199\n"
"v_mfma_f32_16x16x32_bf16 v[108:111], v[216:219], a[188:191], v[108:111]\n"
"v_exp_f32 v200, v200\n"
"v_mfma_f32_16x16x32_bf16 v[112:115], v[216:219], a[192:195], v[112:115]\n"
"v_mfma_f32_16x16x32_bf16 v[112:115], v[212:215], a[196:199], v[112:115]\n"
"v_exp_f32 v201, v201\n"
"v_mfma_f32_16x16x32_bf16 v[116:119], v[212:215], a[200:203], v[116:119]\n"
"v_mfma_f32_16x16x32_bf16 v[116:119], v[216:219], a[204:207], v[116:119]\n"
"v_exp_f32 v202, v202\n"
"v_mfma_f32_16x16x32_bf16 v[120:123], v[216:219], a[208:211], v[120:123]\n"
"buffer_load_dwordx4 v251, s[40:43], s24 offen offset:3072 nt lds\n"
"v_mfma_f32_16x16x32_bf16 v[120:123], v[212:215], a[212:215], v[120:123]\n"
"v_exp_f32 v203, v203\n"
"v_mfma_f32_16x16x32_bf16 v[124:127], v[212:215], a[216:219], v[124:127]\n"
"s_waitcnt lgkmcnt(0)\n"
"v_cmp_gt_u32 vcc, s27, v255\n"
"v_mfma_f32_16x16x32_bf16 v[124:127], v[216:219], a[220:223], v[124:127]\n"
"s_cbranch_vccnz L_late6_4_%=\n"
"L_back6_4_%=:\n"
"v_mfma_f32_16x16x32_bf16 v[192:195], a[96:99], a[80:83], v[240:243]\n"
"v_exp_f32 v204, v204\n"
"v_mfma_f32_16x16x32_bf16 v[192:195], a[100:103], a[84:87], v[192:195]\n"
"v_cvt_pk_bf16_f32 v212, v200, v201\n"
"v_cvt_pk_bf16_f32 v213, v202, v203\n"
"v_mfma_f32_16x16x32_bf16 v[192:195], a[104:107], a[88:91], v[192:195]\n"
"v_exp_f32 v205, v205\n"
"v_mfma_f32_16x16x32_bf16 v[192:195], a[108:111], a[92:95], v[192:195]\n"
"ds_read_b128 a[96:99], v244 offset:0\n"
"v_cvt_pk_bf16_f32 v214, v204, v205\n"
"v_mfma_f32_16x16x32_bf16 v[196:199], a[112:115], a[92:95], v[240:243]\n"
"v_exp_f32 v206, v206\n"
"v_mfma_f32_16x16x32_bf16 v[196:199], a[116:119], a[88:91], v[196:199]\n"
"ds_read_b128 a[100:103], v245 offset:0\n"
"ds_read_b128 a[104:107], v244 offset:1024\n"
"v_mfma_f32_16x16x32_bf16 v[196:199], a[120:123], a[84:87], v[196:199]\n"
"v_exp_f32 v207, v207\n"
"v_mfma_f32_16x16x32_bf16 v[196:199], a[124:127], a[80:83], v[196:199]\n"
"ds_read_b128 a[108:111], v245 offset:1024\n"
"v_cvt_pk_bf16_f32 v215, v206, v207\n"
"v_mfma_f32_16x16x32_bf16 v[200:203], a[128:131], a[80:83], v[240:243]\n"
"v_exp_f32 v192, v192\n"
"v_mfma_f32_16x16x32_bf16 v[200:203], a[132:135], a[84:87], v[200:203]\n"
"ds_read_b128 a[112:115], v245 offset:9216\n"
"ds_read_b128 a[116:119], v244 offset:9216\n"
"v_mfma_f32_16x16x32_bf16 v[200:203], a[136:139], a[88:91], v[200:203]\n"
"v_exp_f32 v193, v193\n"
"v_mfma_f32_16x16x32_bf16 v[200:203], a[140:143], a[92:95], v[200:203]\n"
"ds_read_b128 a[120:123], v245 offset:8192\n"
"v_cvt_pk_bf16_f32 v216, v192, v193\n"
"v_mfma_f32_16x16x32_bf16 v[204:207], a[144:147], a[92:95], v[240:243]\n"
"v_exp_f32 v194, v194\n"
"v_mfma_f32_16x16x32_bf16 v[204:207], a[148:151], a[88:91], v[204:207]\n"
"ds_read_b128 a[124:127], v244 offset:8192\n"
"v_mfma_f32_16x16x32_bf16 v[204:207], a[152:155], a[84:87], v[204:207]\n"
"v_exp_f32 v195, v195\n"
"v_mfma_f32_16x16x32_bf16 v[204:207], a[156:159], a[80:83], v[204:207]\n"
"ds_read_b128 a[128:131], v244 offset:16384\n"
"v_cvt_pk_bf16_f32 v217, v194, v195\n"
"v_smfmac_f32_16x16x64_bf16 a[240:243], a[248:251], v[208:215], v252\n"
"v_exp_f32 v196, v196\n"
"v_mfma_f32_16x16x32_bf16 v[128:131], v[208:211], a[160:163], v[128:131]\n"
"ds_read_b128 a[132:135], v245 offset:16384\n"
"v_mfma_f32_16x16x32_bf16 v[128:131], v[212:215], a[164:167], v[128:131]\n"
"v_exp_f32 v197, v197\n"
"v_mfma_f32_16x16x32_bf16 v[132:135], v[212:215], a[168:171], v[132:135]\n"
"ds_read_b128 a[136:139], v244 offset:17408\n"
"v_cvt_pk_bf16_f32 v218, v196, v197\n"
"v_mfma_f32_16x16x32_bf16 v[132:135], v[208:211], a[172:175], v[132:135]\n"
"v_exp_f32 v198, v198\n"
"v_mfma_f32_16x16x32_bf16 v[136:139], v[208:211], a[176:179], v[136:139]\n"
"ds_read_b128 a[140:143], v245 offset:17408\n"
"ds_read_b128 a[144:147], v245 offset:25600\n"
"v_mfma_f32_16x16x32_bf16 v[136:139], v[212:215], a[180:183], v[136:139]\n"
"v_exp_f32 v199, v199\n"
"v_mfma_f32_16x16x32_bf16 v[140:143], v[212:215], a[184:187], v[140:143]\n"
"v_cvt_pk_bf16_f32 v219, v198, v199\n"
"s_xor_b32 s25, s25, 0x10000\n"
"v_mfma_f32_16x16x32_bf16 v[140:143], v[208:211], a[188:191], v[140:143]\n"
"v_exp_f32 v200, v200\n"
"v_mfma_f32_16x16x32_bf16 v[144:147], v[208:211], a[192:195], v[144:147]\n"
"s_mov_b32 m0, s25\n"
"s_add_u32 s24, s24, 8192\n"
"v_mfma_f32_16x16x32_bf16 v[144:147], v[212:215], a[196:199], v[144:147]\n"
"v_exp_f32 v201, v201\n"
"v_mfma_f32_16x16x32_bf16 v[148:151], v[212:215], a[200:203], v[148:151]\n"
"buffer_load_dwordx4 v248, s[36:39], s24 offen nt lds\n"
"v_mfma_f32_16x16x32_bf16 v[148:151], v[208:211], a[204:207], v[148:151]\n"
"v_exp_f32 v202, v202\n"
"v_mfma_f32_16x16x32_bf16 v[152:155], v[208:211], a[208:211], v[152:155]\n"
"ds_read_b128 a[148:151], v244 offset:25600\n"
"ds_read_b128 a[152:155], v245 offset:24576\n"
"ds_read_b128 a[156:159], v244 offset:24576\n"
"v_mfma_f32_16x16x32_bf16 v[152:155], v[212:215], a[212:215], v[152:155]\n"
"v_exp_f32 v203, v203\n"
"v_mfma_f32_16x16x32_bf16 v[156:159], v[212:215], a[216:219], v[156:159]\n"
"v_mfma_f32_16x16x32_bf16 v[156:159], v[208:211], a[220:223], v[156:159]\n"
"s_waitcnt lgkmcnt(8)\n"
"v_mfma_f32_16x16x32_bf16 v[192:195], a[96:99], a[0:3], v[220:223]\n"
"v_exp_f32 v204, v204\n"
"v_mfma_f32_16x16x32_bf16 v[192:195], a[100:103], a[4:7], v[192:195]\n"
"v_cvt_pk_bf16_f32 v212, v200, v201\n"
"v_cvt_pk_bf16_f32 v213, v202, v203\n"
"v_mfma_f32_16x16x32_bf16 v[192:195], a[104:107], a[8:11], v[192:195]\n"
"v_exp_f32 v205, v205\n"
"v_mfma_f32_16x16x32_bf16 v[192:195], a[108:111], a[12:15], v[192:195]\n"
"v_cvt_pk_bf16_f32 v214, v204, v205\n"
"v_mfma_f32_16x16x32_bf16 v[196:199], a[112:115], a[12:15], v[220:223]\n"
"v_exp_f32 v206, v206\n"
"v_mfma_f32_16x16x32_bf16 v[196:199], a[116:119], a[8:11], v[196:199]\n"
"buffer_load_dwordx4 v249, s[36:39], s24 offen offset:1024 nt lds\n"
"v_mfma_f32_16x16x32_bf16 v[196:199], a[120:123], a[4:7], v[196:199]\n"
"v_exp_f32 v207, v207\n"
"v_mfma_f32_16x16x32_bf16 v[196:199], a[124:127], a[0:3], v[196:199]\n"
"v_cvt_pk_bf16_f32 v215, v206, v207\n"
"s_waitcnt lgkmcnt(0)\n"
"v_mfma_f32_16x16x32_bf16 v[200:203], a[128:131], a[0:3], v[220:223]\n"
"v_exp_f32 v192, v192\n"
"v_mfma_f32_16x16x32_bf16 v[200:203], a[132:135], a[4:7], v[200:203]\n"
"v_xor_b32 v246, 0x10000, v246\n"
"v_xor_b32 v247, 0x10000, v247\n"
"v_mfma_f32_16x16x32_bf16 v[200:203], a[136:139], a[8:11], v[200:203]\n"
"v_exp_f32 v193, v193\n"
"v_mfma_f32_16x16x32_bf16 v[200:203], a[140:143], a[12:15], v[200:203]\n"
"v_cvt_pk_bf16_f32 v208, v192, v193\n"
"v_mfma_f32_16x16x32_bf16 v[204:207], a[144:147], a[12:15], v[220:223]\n"
"v_exp_f32 v194, v194\n"
"v_mfma_f32_16x16x32_bf16 v[204:207], a[148:151], a[8:11], v[204:207]\n"
"v_mfma_f32_16x16x32_bf16 v[204:207], a[152:155], a[4:7], v[204:207]\n"
"v_exp_f32 v195, v195\n"
"v_mfma_f32_16x16x32_bf16 v[204:207], a[156:159], a[0:3], v[204:207]\n"
"v_cvt_pk_bf16_f32 v209, v194, v195\n"
"v_smfmac_f32_16x16x64_bf16 a[244:247], a[248:251], v[212:219], v252\n"
"v_exp_f32 v196, v196\n"
"v_mfma_f32_16x16x32_bf16 v[160:163], v[216:219], a[160:163], v[160:163]\n"
"ds_read_b64_tr_b16 a[160:161], v246 offset:2048\n"
"ds_read_b64_tr_b16 a[162:163], v246 offset:10240\n"
"v_mfma_f32_16x16x32_bf16 v[160:163], v[212:215], a[164:167], v[160:163]\n"
"v_exp_f32 v197, v197\n"
"v_mfma_f32_16x16x32_bf16 v[164:167], v[212:215], a[168:171], v[164:167]\n"
"ds_read_b64_tr_b16 a[164:165], v246 offset:18432\n"
"v_cvt_pk_bf16_f32 v210, v196, v197\n"
"v_mfma_f32_16x16x32_bf16 v[164:167], v[216:219], a[172:175], v[164:167]\n"
"v_exp_f32 v198, v198\n"
"v_mfma_f32_16x16x32_bf16 v[168:171], v[216:219], a[176:179], v[168:171]\n"
"ds_read_b64_tr_b16 a[166:167], v246 offset:26624\n"
"ds_read_b64_tr_b16 a[168:169], v247 offset:18432\n"
"v_mfma_f32_16x16x32_bf16 v[168:171], v[212:215], a[180:183], v[168:171]\n"
"v_exp_f32 v199, v199\n"
"v_mfma_f32_16x16x32_bf16 v[172:175], v[212:215], a[184:187], v[172:175]\n"
"ds_read_b64_tr_b16 a[170:171], v247 offset:26624\n"
"v_cvt_pk_bf16_f32 v211, v198, v199\n"
"v_mfma_f32_16x16x32_bf16 v[172:175], v[216:219], a[188:191], v[172:175]\n"
"v_exp_f32 v200, v200\n"
"v_mfma_f32_16x16x32_bf16 v[176:179], v[216:219], a[192:195], v[176:179]\n"
"ds_read_b64_tr_b16 a[172:173], v247 offset:2048\n"
"ds_read_b64_tr_b16 a[174:175], v247 offset:10240\n"
"v_mfma_f32_16x16x32_bf16 v[176:179], v[212:215], a[196:199], v[176:179]\n"
"v_exp_f32 v201, v201\n"
"v_mfma_f32_16x16x32_bf16 v[180:183], v[212:215], a[200:203], v[180:183]\n"
"ds_read_b64_tr_b16 a[176:177], v246 offset:2560\n"
"ds_read_b64_tr_b16 a[178:179], v246 offset:10752\n"
"v_mfma_f32_16x16x32_bf16 v[180:183], v[216:219], a[204:207], v[180:183]\n"
"v_exp_f32 v202, v202\n"
"v_mfma_f32_16x16x32_bf16 v[184:187], v[216:219], a[208:211], v[184:187]\n"
"ds_read_b64_tr_b16 a[180:181], v246 offset:18944\n"
"ds_read_b64_tr_b16 a[182:183], v246 offset:27136\n"
"v_mfma_f32_16x16x32_bf16 v[184:187], v[212:215], a[212:215], v[184:187]\n"
"v_exp_f32 v203, v203\n"
"v_mfma_f32_16x16x32_bf16 v[188:191], v[212:215], a[216:219], v[188:191]\n"
"ds_read_b64_tr_b16 a[184:185], v247 offset:18944\n"
"ds_read_b64_tr_b16 a[186:187], v247 offset:27136\n"
"v_mfma_f32_16x16x32_bf16 v[188:191], v[216:219], a[220:223], v[188:191]\n"
"ds_read_b64_tr_b16 a[188:189], v247 offset:2560\n"
"ds_read_b64_tr_b16 a[190:191], v247 offset:10752\n"
"v_mfma_f32_16x16x32_bf16 v[192:195], a[96:99], a[16:19], v[224:227]\n"
"v_exp_f32 v204, v204\n"
"v_mfma_f32_16x16x32_bf16 v[192:195], a[100:103], a[20:23], v[192:195]\n"
"v_cvt_pk_bf16_f32 v212, v200, v201\n"
"v_cvt_pk_bf16_f32 v213, v202, v203\n"
"v_mfma_f32_16x16x32_bf16 v[192:195], a[104:107], a[24:27], v[192:195]\n"
"v_exp_f32 v205, v205\n"
"v_mfma_f32_16x16x32_bf16 v[192:195], a[108:111], a[28:31], v[192:195]\n"
"ds_read_b64_tr_b16 a[192:193], v246 offset:3072\n"
"v_cvt_pk_bf16_f32 v214, v204, v205\n"
"v_mfma_f32_16x16x32_bf16 v[196:199], a[112:115], a[28:31], v[224:227]\n"
"v_exp_f32 v206, v206\n"
"v_mfma_f32_16x16x32_bf16 v[196:199], a[116:119], a[24:27], v[196:199]\n"
"ds_read_b64_tr_b16 a[194:195], v246 offset:11264\n"
"ds_read_b64_tr_b16 a[196:197], v246 offset:19456\n"
"v_mfma_f32_16x16x32_bf16 v[196:199], a[120:123], a[20:23], v[196:199]\n"
"v_exp_f32 v207, v207\n"
"v_mfma_f32_16x16x32_bf16 v[196:199], a[124:127], a[16:19], v[196:199]\n"
"ds_read_b64_tr_b16 a[198:199], v246 offset:27648\n"
"v_cvt_pk_bf16_f32 v215, v206, v207\n"
"v_mfma_f32_16x16x32_bf16 v[200:203], a[128:131], a[16:19], v[224:227]\n"
"v_exp_f32 v192, v192\n"
"v_mfma_f32_16x16x32_bf16 v[200:203], a[132:135], a[20:23], v[200:203]\n"
"ds_read_b64_tr_b16 a[200:201], v247 offset:19456\n"
"ds_read_b64_tr_b16 a[202:203], v247 offset:27648\n"
"v_mfma_f32_16x16x32_bf16 v[200:203], a[136:139], a[24:27], v[200:203]\n"
"v_exp_f32 v193, v193\n"
"v_mfma_f32_16x16x32_bf16 v[200:203], a[140:143], a[28:31], v[200:203]\n"
"ds_read_b64_tr_b16 a[204:205], v247 offset:3072\n"
"v_cvt_pk_bf16_f32 v216, v192, v193\n"
"v_mfma_f32_16x16x32_bf16 v[204:207], a[144:147], a[28:31], v[224:227]\n"
"v_exp_f32 v194, v194\n"
"v_mfma_f32_16x16x32_bf16 v[204:207], a[148:151], a[24:27], v[204:207]\n"
"ds_read_b64_tr_b16 a[206:207], v247 offset:11264\n"
"ds_read_b64_tr_b16 a[208:209], v246 offset:3584\n"
"v_mfma_f32_16x16x32_bf16 v[204:207], a[152:155], a[20:23], v[204:207]\n"
"v_exp_f32 v195, v195\n"
"v_mfma_f32_16x16x32_bf16 v[204:207], a[156:159], a[16:19], v[204:207]\n"
"v_cvt_pk_bf16_f32 v217, v194, v195\n"
"s_waitcnt lgkmcnt(15)\n"
"v_smfmac_f32_16x16x64_bf16 a[224:227], a[248:251], v[208:215], v252\n"
"v_exp_f32 v196, v196\n"
"v_mfma_f32_16x16x32_bf16 v[0:3], v[208:211], a[160:163], v[0:3]\n"
"ds_read_b64_tr_b16 a[210:211], v246 offset:11776\n"
"ds_read_b64_tr_b16 a[212:213], v246 offset:19968\n"
"v_mfma_f32_16x16x32_bf16 v[0:3], v[212:215], a[164:167], v[0:3]\n"
"v_exp_f32 v197, v197\n"
"v_mfma_f32_16x16x32_bf16 v[4:7], v[212:215], a[168:171], v[4:7]\n"
"v_cvt_pk_bf16_f32 v218, v196, v197\n"
"s_waitcnt lgkmcnt(11)\n"
"v_mfma_f32_16x16x32_bf16 v[4:7], v[208:211], a[172:175], v[4:7]\n"
"v_exp_f32 v198, v198\n"
"v_mfma_f32_16x16x32_bf16 v[8:11], v[208:211], a[176:179], v[8:11]\n"
"ds_read_b64_tr_b16 a[214:215], v246 offset:28160\n"
"ds_read_b64_tr_b16 a[216:217], v247 offset:19968\n"
"ds_read_b64_tr_b16 a[218:219], v247 offset:28160\n"
"ds_read_b64_tr_b16 a[220:221], v247 offset:3584\n"
"ds_read_b64_tr_b16 a[222:223], v247 offset:11776\n"
"v_mfma_f32_16x16x32_bf16 v[8:11], v[212:215], a[180:183], v[8:11]\n"
"v_exp_f32 v199, v199\n"
"v_mfma_f32_16x16x32_bf16 v[12:15], v[212:215], a[184:187], v[12:15]\n"
"v_cvt_pk_bf16_f32 v219, v198, v199\n"
"s_waitcnt lgkmcnt(8)\n"
"v_mfma_f32_16x16x32_bf16 v[12:15], v[208:211], a[188:191], v[12:15]\n"
"v_exp_f32 v200, v200\n"
"v_mfma_f32_16x16x32_bf16 v[16:19], v[208:211], a[192:195], v[16:19]\n"
"buffer_load_dwordx4 v250, s[40:43], s24 offen offset:2048 nt lds\n"
"v_mfma_f32_16x16x32_bf16 v[16:19], v[212:215], a[196:199], v[16:19]\n"
"v_exp_f32 v201, v201\n"
"v_mfma_f32_16x16x32_bf16 v[20:23], v[212:215], a[200:203], v[20:23]\n"
"s_waitcnt lgkmcnt(0)\n"
"v_mfma_f32_16x16x32_bf16 v[20:23], v[208:211], a[204:207], v[20:23]\n"
"v_exp_f32 v202, v202\n"
"v_mfma_f32_16x16x32_bf16 v[24:27], v[208:211], a[208:211], v[24:27]\n"
"buffer_load_dwordx4 v251, s[40:43], s24 offen offset:3072 nt lds\n"
"v_mfma_f32_16x16x32_bf16 v[24:27], v[212:215], a[212:215], v[24:27]\n"
"v_exp_f32 v203, v203\n"
"v_mfma_f32_16x16x32_bf16 v[28:31], v[212:215], a[216:219], v[28:31]\n"
"v_mfma_f32_16x16x32_bf16 v[28:31], v[208:211], a[220:223], v[28:31]\n"
"v_mfma_f32_16x16x32_bf16 v[192:195], a[96:99], a[32:35], v[228:231]\n"
"v_exp_f32 v204, v204\n"
"v_mfma_f32_16x16x32_bf16 v[192:195], a[100:103], a[36:39], v[192:195]\n"
"v_cvt_pk_bf16_f32 v212, v200, v201\n"
"v_cvt_pk_bf16_f32 v213, v202, v203\n"
"v_mfma_f32_16x16x32_bf16 v[192:195], a[104:107], a[40:43], v[192:195]\n"
"v_exp_f32 v205, v205\n"
"v_mfma_f32_16x16x32_bf16 v[192:195], a[108:111], a[44:47], v[192:195]\n"
"v_cvt_pk_bf16_f32 v214, v204, v205\n"
"v_mfma_f32_16x16x32_bf16 v[196:199], a[112:115], a[44:47], v[228:231]\n"
"v_exp_f32 v206, v206\n"
"v_mfma_f32_16x16x32_bf16 v[196:199], a[116:119], a[40:43], v[196:199]\n"
"v_mfma_f32_16x16x32_bf16 v[196:199], a[120:123], a[36:39], v[196:199]\n"
"v_exp_f32 v207, v207\n"
"v_mfma_f32_16x16x32_bf16 v[196:199], a[124:127], a[32:35], v[196:199]\n"
"v_cvt_pk_bf16_f32 v215, v206, v207\n"
"v_mfma_f32_16x16x32_bf16 v[200:203], a[128:131], a[32:35], v[228:231]\n"
"v_exp_f32 v192, v192\n"
"v_mfma_f32_16x16x32_bf16 v[200:203], a[132:135], a[36:39], v[200:203]\n"
"v_mfma_f32_16x16x32_bf16 v[200:203], a[136:139], a[40:43], v[200:203]\n"
"v_exp_f32 v193, v193\n"
"v_mfma_f32_16x16x32_bf16 v[200:203], a[140:143], a[44:47], v[200:203]\n"
"v_cvt_pk_bf16_f32 v208, v192, v193\n"
"v_mfma_f32_16x16x32_bf16 v[204:207], a[144:147], a[44:47], v[228:231]\n"
"v_exp_f32 v194, v194\n"
"v_mfma_f32_16x16x32_bf16 v[204:207], a[148:151], a[40:43], v[204:207]\n"
"s_add_u32 m0, s25, 16384\n"
"s_add_u32 s24, s24, 8192\n"
"v_mfma_f32_16x16x32_bf16 v[204:207], a[152:155], a[36:39], v[204:207]\n"
"v_exp_f32 v195, v195\n"
"v_mfma_f32_16x16x32_bf16 v[204:207], a[156:159], a[32:35], v[204:207]\n"
"v_cvt_pk_bf16_f32 v209, v194, v195\n"
"v_smfmac_f32_16x16x64_bf16 a[228:231], a[248:251], v[212:219], v252\n"
"v_exp_f32 v196, v196\n"
"v_mfma_f32_16x16x32_bf16 v[32:35], v[216:219], a[160:163], v[32:35]\n"
"buffer_load_dwordx4 v248, s[36:39], s24 offen nt lds\n"
"v_mfma_f32_16x16x32_bf16 v[32:35], v[212:215], a[164:167], v[32:35]\n"
"v_exp_f32 v197, v197\n"
"v_mfma_f32_16x16x32_bf16 v[36:39], v[212:215], a[168:171], v[36:39]\n"
"v_cvt_pk_bf16_f32 v210, v196, v197\n"
"v_mfma_f32_16x16x32_bf16 v[36:39], v[216:219], a[172:175], v[36:39]\n"
"v_exp_f32 v198, v198\n"
"v_mfma_f32_16x16x32_bf16 v[40:43], v[216:219], a[176:179], v[40:43]\n"
"v_mfma_f32_16x16x32_bf16 v[40:43], v[212:215], a[180:183], v[40:43]\n"
"v_exp_f32 v199, v199\n"
"v_mfma_f32_16x16x32_bf16 v[44:47], v[212:215], a[184:187], v[44:47]\n"
"v_cvt_pk_bf16_f32 v211, v198, v199\n"
"v_mfma_f32_16x16x32_bf16 v[44:47], v[216:219], a[188:191], v[44:47]\n"
"v_exp_f32 v200, v200\n"
"v_mfma_f32_16x16x32_bf16 v[48:51], v[216:219], a[192:195], v[48:51]\n"
"v_mfma_f32_16x16x32_bf16 v[48:51], v[212:215], a[196:199], v[48:51]\n"
"v_exp_f32 v201, v201\n"
"v_mfma_f32_16x16x32_bf16 v[52:55], v[212:215], a[200:203], v[52:55]\n"
"v_mfma_f32_16x16x32_bf16 v[52:55], v[216:219], a[204:207], v[52:55]\n"
"v_exp_f32 v202, v202\n"
"v_mfma_f32_16x16x32_bf16 v[56:59], v[216:219], a[208:211], v[56:59]\n"
"v_mfma_f32_16x16x32_bf16 v[56:59], v[212:215], a[212:215], v[56:59]\n"
"v_exp_f32 v203, v203\n"
"v_mfma_f32_16x16x32_bf16 v[60:63], v[212:215], a[216:219], v[60:63]\n"
"v_mfma_f32_16x16x32_bf16 v[60:63], v[216:219], a[220:223], v[60:63]\n"
".p2align 6\n"
"L_loop6_%=:\n"
"v_mfma_f32_16x16x32_bf16 v[192:195], a[96:99], a[48:51], v[232:235]\n"
"v_exp_f32 v204, v204\n"
"v_mfma_f32_16x16x32_bf16 v[192:195], a[100:103], a[52:55], v[192:195]\n"
"v_cvt_pk_bf16_f32 v212, v200, v201\n"
"v_cvt_pk_bf16_f32 v213, v202, v203\n"
"v_mfma_f32_16x16x32_bf16 v[192:195], a[104:107], a[56:59], v[192:195]\n"
"v_exp_f32 v205, v205\n"
"v_mfma_f32_16x16x32_bf16 v[192:195], a[108:111], a[60:63], v[192:195]\n"
"v_cvt_pk_bf16_f32 v214, v204, v205\n"
"v_mfma_f32_16x16x32_bf16 v[196:199], a[112:115], a[60:63], v[232:235]\n"
"v_exp_f32 v206, v206\n"
"v_mfma_f32_16x16x32_bf16 v[196:199], a[116:119], a[56:59], v[196:199]\n"
"buffer_load_dwordx4 v249, s[36:39], s24 offen offset:1024 nt lds\n"
"v_mfma_f32_16x16x32_bf16 v[196:199], a[120:123], a[52:55], v[196:199]\n"
"v_exp_f32 v207, v207\n"
"v_mfma_f32_16x16x32_bf16 v[196:199], a[124:127], a[48:51], v[196:199]\n"
"v_cvt_pk_bf16_f32 v215, v206, v207\n"
"v_mfma_f32_16x16x32_bf16 v[200:203], a[128:131], a[48:51], v[232:235]\n"
"v_exp_f32 v192, v192\n"
"v_mfma_f32_16x16x32_bf16 v[200:203], a[132:135], a[52:55], v[200:203]\n"
"v_mfma_f32_16x16x32_bf16 v[200:203], a[136:139], a[56:59], v[200:203]\n"
"v_exp_f32 v193, v193\n"
"v_mfma_f32_16x16x32_bf16 v[200:203], a[140:143], a[60:63], v[200:203]\n"
"v_cvt_pk_bf16_f32 v216, v192, v193\n"
"v_mfma_f32_16x16x32_bf16 v[204:207], a[144:147], a[60:63], v[232:235]\n"
"v_exp_f32 v194, v194\n"
"v_mfma_f32_16x16x32_bf16 v[204:207], a[148:151], a[56:59], v[204:207]\n"
"v_mfma_f32_16x16x32_bf16 v[204:207], a[152:155], a[52:55], v[204:207]\n"
"v_exp_f32 v195, v195\n"
"v_mfma_f32_16x16x32_bf16 v[204:207], a[156:159], a[48:51], v[204:207]\n"
"v_cvt_pk_bf16_f32 v217, v194, v195\n"
"v_smfmac_f32_16x16x64_bf16 a[232:235], a[248:251], v[208:215], v252\n"
"v_exp_f32 v196, v196\n"
"v_mfma_f32_16x16x32_bf16 v[64:67], v[208:211], a[160:163], v[64:67]\n"
"v_mfma_f32_16x16x32_bf16 v[64:67], v[212:215], a[164:167], v[64:67]\n"
"v_exp_f32 v197, v197\n"
"v_mfma_f32_16x16x32_bf16 v[68:71], v[212:215], a[168:171], v[68:71]\n"
"v_cvt_pk_bf16_f32 v218, v196, v197\n"
"v_mfma_f32_16x16x32_bf16 v[68:71], v[208:211], a[172:175], v[68:71]\n"
"v_exp_f32 v198, v198\n"
"v_mfma_f32_16x16x32_bf16 v[72:75], v[208:211], a[176:179], v[72:75]\n"
"v_mfma_f32_16x16x32_bf16 v[72:75], v[212:215], a[180:183], v[72:75]\n"
"v_exp_f32 v199, v199\n"
"v_mfma_f32_16x16x32_bf16 v[76:79], v[212:215], a[184:187], v[76:79]\n"
"v_cvt_pk_bf16_f32 v219, v198, v199\n"
"v_mfma_f32_16x16x32_bf16 v[76:79], v[208:211], a[188:191], v[76:79]\n"
"v_exp_f32 v200, v200\n"
"v_mfma_f32_16x16x32_bf16 v[80:83], v[208:211], a[192:195], v[80:83]\n"
"s_add_u32 s27, s27, 1\n"
"v_mov_b32 v255, s27\n"
"v_mfma_f32_16x16x32_bf16 v[80:83], v[212:215], a[196:199], v[80:83]\n"
"v_exp_f32 v201, v201\n"
"v_mfma_f32_16x16x32_bf16 v[84:87], v[212:215], a[200:203], v[84:87]\n"
"v_mfma_f32_16x16x32_bf16 v[84:87], v[208:211], a[204:207], v[84:87]\n"
"v_exp_f32 v202, v202\n"
"v_mfma_f32_16x16x32_bf16 v[88:91], v[208:211], a[208:211], v[88:91]\n"
"v_mfma_f32_16x16x32_bf16 v[88:91], v[212:215], a[212:215], v[88:91]\n"
"v_exp_f32 v203, v203\n"
"v_mfma_f32_16x16x32_bf16 v[92:95], v[212:215], a[216:219], v[92:95]\n"
"s_waitcnt vmcnt(6)\n"
"ds_write_b32 v253, v255\n"
"v_mfma_f32_16x16x32_bf16 v[92:95], v[208:211], a[220:223], v[92:95]\n"
"v_mfma_f32_16x16x32_bf16 v[192:195], a[96:99], a[64:67], v[236:239]\n"
"v_exp_f32 v204, v204\n"
"v_mfma_f32_16x16x32_bf16 v[192:195], a[100:103], a[68:71], v[192:195]\n"
"v_cvt_pk_bf16_f32 v212, v200, v201\n"
"v_cvt_pk_bf16_f32 v213, v202, v203\n"
"v_mfma_f32_16x16x32_bf16 v[192:195], a[104:107], a[72:75], v[192:195]\n"
"v_exp_f32 v205, v205\n"
"v_mfma_f32_16x16x32_bf16 v[192:195], a[108:111], a[76:79], v[192:195]\n"
"v_cvt_pk_bf16_f32 v214, v204, v205\n"
"v_mfma_f32_16x16x32_bf16 v[196:199], a[112:115], a[76:79], v[236:239]\n"
"v_exp_f32 v206, v206\n"
"v_mfma_f32_16x16x32_bf16 v[196:199], a[116:119], a[72:75], v[196:199]\n"
"buffer_load_dwordx4 v250, s[40:43], s24 offen offset:2048 nt lds\n"
"v_mfma_f32_16x16x32_bf16 v[196:199], a[120:123], a[68:71], v[196:199]\n"
"v_exp_f32 v207, v207\n"
"v_mfma_f32_16x16x32_bf16 v[196:199], a[124:127], a[64:67], v[196:199]\n"
"v_cvt_pk_bf16_f32 v215, v206, v207\n"
"v_mfma_f32_16x16x32_bf16 v[200:203], a[128:131], a[64:67], v[236:239]\n"
"v_exp_f32 v192, v192\n"
"v_mfma_f32_16x16x32_bf16 v[200:203], a[132:135], a[68:71], v[200:203]\n"
"v_mfma_f32_16x16x32_bf16 v[200:203], a[136:139], a[72:75], v[200:203]\n"
"v_exp_f32 v193, v193\n"
"v_mfma_f32_16x16x32_bf16 v[200:203], a[140:143], a[76:79], v[200:203]\n"
"v_cvt_pk_bf16_f32 v208, v192, v193\n"
"v_mfma_f32_16x16x32_bf16 v[204:207], a[144:147], a[76:79], v[236:239]\n"
"v_exp_f32 v194, v194\n"
"v_mfma_f32_16x16x32_bf16 v[204:207], a[148:151], a[72:75], v[204:207]\n"
"v_mfma_f32_16x16x32_bf16 v[204:207], a[152:155], a[68:71], v[204:207]\n"
"v_exp_f32 v195, v195\n"
"v_mfma_f32_16x16x32_bf16 v[204:207], a[156:159], a[64:67], v[204:207]\n"
"v_cvt_pk_bf16_f32 v209, v194, v195\n"
"v_smfmac_f32_16x16x64_bf16 a[236:239], a[248:251], v[212:219], v252\n"
"v_exp_f32 v196, v196\n"
"v_mfma_f32_16x16x32_bf16 v[96:99], v[216:219], a[160:163], v[96:99]\n"
"v_mfma_f32_16x16x32_bf16 v[96:99], v[212:215], a[164:167], v[96:99]\n"
"v_exp_f32 v197, v197\n"
"v_mfma_f32_16x16x32_bf16 v[100:103], v[212:215], a[168:171], v[100:103]\n"
"v_cvt_pk_bf16_f32 v210, v196, v197\n"
"v_mfma_f32_16x16x32_bf16 v[100:103], v[216:219], a[172:175], v[100:103]\n"
"v_exp_f32 v198, v198\n"
"v_mfma_f32_16x16x32_bf16 v[104:107], v[216:219], a[176:179], v[104:107]\n"
"ds_read_b32 v255, v254\n"
"v_mfma_f32_16x16x32_bf16 v[104:107], v[212:215], a[180:183], v[104:107]\n"
"v_exp_f32 v199, v199\n"
"v_mfma_f32_16x16x32_bf16 v[108:111], v[212:215], a[184:187], v[108:111]\n"
"v_cvt_pk_bf16_f32 v211, v198, v199\n"
"v_mfma_f32_16x16x32_bf16 v[108:111], v[216:219], a[188:191], v[108:111]\n"
"v_exp_f32 v200, v200\n"
"v_mfma_f32_16x16x32_bf16 v[112:115], v[216:219], a[192:195], v[112:115]\n"
"buffer_load_dwordx4 v251, s[40:43], s24 offen offset:3072 nt lds\n"
"v_mfma_f32_16x16x32_bf16 v[112:115], v[212:215], a[196:199], v[112:115]\n"
"v_exp_f32 v201, v201\n"
"v_mfma_f32_16x16x32_bf16 v[116:119], v[212:215], a[200:203], v[116:119]\n"
"v_mfma_f32_16x16x32_bf16 v[116:119], v[216:219], a[204:207], v[116:119]\n"
"v_exp_f32 v202, v202\n"
"v_mfma_f32_16x16x32_bf16 v[120:123], v[216:219], a[208:211], v[120:123]\n"
"v_mfma_f32_16x16x32_bf16 v[120:123], v[212:215], a[212:215], v[120:123]\n"
"v_exp_f32 v203, v203\n"
"v_mfma_f32_16x16x32_bf16 v[124:127], v[212:215], a[216:219], v[124:127]\n"
"s_waitcnt lgkmcnt(0)\n"
"v_cmp_gt_u32 vcc, s27, v255\n"
"v_mfma_f32_16x16x32_bf16 v[124:127], v[216:219], a[220:223], v[124:127]\n"
"s_cbranch_vccnz L_late6_5_%=\n"
"L_back6_5_%=:\n"
"v_mfma_f32_16x16x32_bf16 v[192:195], a[96:99], a[80:83], v[240:243]\n"
"v_exp_f32 v204, v204\n"
"v_mfma_f32_16x16x32_bf16 v[192:195], a[100:103], a[84:87], v[192:195]\n"
"v_cvt_pk_bf16_f32 v212, v200, v201\n"
"v_cvt_pk_bf16_f32 v213, v202, v203\n"
"v_mfma_f32_16x16x32_bf16 v[192:195], a[104:107], a[88:91], v[192:195]\n"
"v_exp_f32 v205, v205\n"
"v_mfma_f32_16x16x32_bf16 v[192:195], a[108:111], a[92:95], v[192:195]\n"
"ds_read_b128 a[96:99], v244 offset:32768\n"
"v_cvt_pk_bf16_f32 v214, v204, v205\n"
"v_mfma_f32_16x16x32_bf16 v[196:199], a[112:115], a[92:95], v[240:243]\n"
"v_exp_f32 v206, v206\n"
"v_mfma_f32_16x16x32_bf16 v[196:199], a[116:119], a[88:91], v[196:199]\n"
"ds_read_b128 a[100:103], v245 offset:32768\n"
"ds_read_b128 a[104:107], v244 offset:33792\n"
"v_mfma_f32_16x16x32_bf16 v[196:199], a[120:123], a[84:87], v[196:199]\n"
"v_exp_f32 v207, v207\n"
"v_mfma_f32_16x16x32_bf16 v[196:199], a[124:127], a[80:83], v[196:199]\n"
"ds_read_b128 a[108:111], v245 offset:33792\n"
"v_cvt_pk_bf16_f32 v215, v206, v207\n"
"v_mfma_f32_16x16x32_bf16 v[200:203], a[128:131], a[80:83], v[240:243]\n"
"v_exp_f32 v192, v192\n"
"v_mfma_f32_16x16x32_bf16 v[200:203], a[132:135], a[84:87], v[200:203]\n"
"ds_read_b128 a[112:115], v245 offset:41984\n"
"ds_read_b128 a[116:119], v244 offset:41984\n"
"v_mfma_f32_16x16x32_bf16 v[200:203], a[136:139], a[88:91], v[200:203]\n"
"v_exp_f32 v193, v193\n"
"v_mfma_f32_16x16x32_bf16 v[200:203], a[140:143], a[92:95], v[200:203]\n"
"ds_read_b128 a[120:123], v245 offset:40960\n"
"v_cvt_pk_bf16_f32 v216, v192, v193\n"
"v_mfma_f32_16x16x32_bf16 v[204:207], a[144:147], a[92:95], v[240:243]\n"
"v_exp_f32 v194, v194\n"
"v_mfma_f32_16x16x32_bf16 v[204:207], a[148:151], a[88:91], v[204:207]\n"
"ds_read_b128 a[124:127], v244 offset:40960\n"
"v_mfma_f32_16x16x32_bf16 v[204:207], a[152:155], a[84:87], v[204:207]\n"
"v_exp_f32 v195, v195\n"
"v_mfma_f32_16x16x32_bf16 v[204:207], a[156:159], a[80:83], v[204:207]\n"
"ds_read_b128 a[128:131], v244 offset:49152\n"
"v_cvt_pk_bf16_f32 v217, v194, v195\n"
"v_smfmac_f32_16x16x64_bf16 a[240:243], a[248:251], v[208:215], v252\n"
"v_exp_f32 v196, v196\n"
"v_mfma_f32_16x16x32_bf16 v[128:131], v[208:211], a[160:163], v[128:131]\n"
"ds_read_b128 a[132:135], v245 offset:49152\n"
"v_mfma_f32_16x16x32_bf16 v[128:131], v[212:215], a[164:167], v[128:131]\n"
"v_exp_f32 v197, v197\n"
"v_mfma_f32_16x16x32_bf16 v[132:135], v[212:215], a[168:171], v[132:135]\n"
"ds_read_b128 a[136:139], v244 offset:50176\n"
"v_cvt_pk_bf16_f32 v218, v196, v197\n"
"v_mfma_f32_16x16x32_bf16 v[132:135], v[208:211], a[172:175], v[132:135]\n"
"v_exp_f32 v198, v198\n"
"v_mfma_f32_16x16x32_bf16 v[136:139], v[208:211], a[176:179], v[136:139]\n"
"ds_read_b128 a[140:143], v245 offset:50176\n"
"ds_read_b128 a[144:147], v245 offset:58368\n"
"v_mfma_f32_16x16x32_bf16 v[136:139], v[212:215], a[180:183], v[136:139]\n"
"v_exp_f32 v199, v199\n"
"v_mfma_f32_16x16x32_bf16 v[140:143], v[212:215], a[184:187], v[140:143]\n"
"v_cvt_pk_bf16_f32 v219, v198, v199\n"
"s_add_u32 m0, s25, 32768\n"
"v_mfma_f32_16x16x32_bf16 v[140:143], v[208:211], a[188:191], v[140:143]\n"
"v_exp_f32 v200, v200\n"
"v_mfma_f32_16x16x32_bf16 v[144:147], v[208:211], a[192:195], v[144:147]\n"
"ds_read_b128 a[148:151], v244 offset:58368\n"
"s_add_u32 s24, s24, 8192\n"
"v_mfma_f32_16x16x32_bf16 v[144:147], v[212:215], a[196:199], v[144:147]\n"
"v_exp_f32 v201, v201\n"
"v_mfma_f32_16x16x32_bf16 v[148:151], v[212:215], a[200:203], v[148:151]\n"
"buffer_load_dwordx4 v248, s[36:39], s24 offen nt lds\n"
"v_mfma_f32_16x16x32_bf16 v[148:151], v[208:211], a[204:207], v[148:151]\n"
"v_exp_f32 v202, v202\n"
"v_mfma_f32_16x16x32_bf16 v[152:155], v[208:211], a[208:211], v[152:155]\n"
"ds_read_b128 a[152:155], v245 offset:57344\n"
"ds_read_b128 a[156:159], v244 offset:57344\n"
"v_mfma_f32_16x16x32_bf16 v[152:155], v[212:215], a[212:215], v[152:155]\n"
"v_exp_f32 v203, v203\n"
"v_mfma_f32_16x16x32_bf16 v[156:159], v[212:215], a[216:219], v[156:159]\n"
"v_mfma_f32_16x16x32_bf16 v[156:159], v[208:211], a[220:223], v[156:159]\n"
"s_waitcnt lgkmcnt(8)\n"
"v_mfma_f32_16x16x32_bf16 v[192:195], a[96:99], a[0:3], v[220:223]\n"
"v_exp_f32 v204, v204\n"
"v_mfma_f32_16x16x32_bf16 v[192:195], a[100:103], a[4:7], v[192:195]\n"
"v_cvt_pk_bf16_f32 v212, v200, v201\n"
"v_cvt_pk_bf16_f32 v213, v202, v203\n"
"v_mfma_f32_16x16x32_bf16 v[192:195], a[104:107], a[8:11], v[192:195]\n"
"v_exp_f32 v205, v205\n"
"v_mfma_f32_16x16x32_bf16 v[192:195], a[108:111], a[12:15], v[192:195]\n"
"v_cvt_pk_bf16_f32 v214, v204, v205\n"
"v_mfma_f32_16x16x32_bf16 v[196:199], a[112:115], a[12:15], v[220:223]\n"
"v_exp_f32 v206, v206\n"
"v_mfma_f32_16x16x32_bf16 v[196:199], a[116:119], a[8:11], v[196:199]\n"
"buffer_load_dwordx4 v249, s[36:39], s24 offen offset:1024 nt lds\n"
"v_mfma_f32_16x16x32_bf16 v[196:199], a[120:123], a[4:7], v[196:199]\n"
"v_exp_f32 v207, v207\n"
"v_mfma_f32_16x16x32_bf16 v[196:199], a[124:127], a[0:3], v[196:199]\n"
"v_cvt_pk_bf16_f32 v215, v206, v207\n"
"s_waitcnt lgkmcnt(0)\n"
"v_mfma_f32_16x16x32_bf16 v[200:203], a[128:131], a[0:3], v[220:223]\n"
"v_exp_f32 v192, v192\n"
"v_mfma_f32_16x16x32_bf16 v[200:203], a[132:135], a[4:7], v[200:203]\n"
"v_mfma_f32_16x16x32_bf16 v[200:203], a[136:139], a[8:11], v[200:203]\n"
"v_exp_f32 v193, v193\n"
"v_mfma_f32_16x16x32_bf16 v[200:203], a[140:143], a[12:15], v[200:203]\n"
"v_cvt_pk_bf16_f32 v208, v192, v193\n"
"v_mfma_f32_16x16x32_bf16 v[204:207], a[144:147], a[12:15], v[220:223]\n"
"v_exp_f32 v194, v194\n"
"v_mfma_f32_16x16x32_bf16 v[204:207], a[148:151], a[8:11], v[204:207]\n"
"v_mfma_f32_16x16x32_bf16 v[204:207], a[152:155], a[4:7], v[204:207]\n"
"v_exp_f32 v195, v195\n"
"v_mfma_f32_16x16x32_bf16 v[204:207], a[156:159], a[0:3], v[204:207]\n"
"v_cvt_pk_bf16_f32 v209, v194, v195\n"
"v_smfmac_f32_16x16x64_bf16 a[244:247], a[248:251], v[212:219], v252\n"
"v_exp_f32 v196, v196\n"
"v_mfma_f32_16x16x32_bf16 v[160:163], v[216:219], a[160:163], v[160:163]\n"
"ds_read_b64_tr_b16 a[160:161], v246 offset:34816\n"
"ds_read_b64_tr_b16 a[162:163], v246 offset:43008\n"
"v_mfma_f32_16x16x32_bf16 v[160:163], v[212:215], a[164:167], v[160:163]\n"
"v_exp_f32 v197, v197\n"
"v_mfma_f32_16x16x32_bf16 v[164:167], v[212:215], a[168:171], v[164:167]\n"
"ds_read_b64_tr_b16 a[164:165], v246 offset:51200\n"
"v_cvt_pk_bf16_f32 v210, v196, v197\n"
"v_mfma_f32_16x16x32_bf16 v[164:167], v[216:219], a[172:175], v[164:167]\n"
"v_exp_f32 v198, v198\n"
"v_mfma_f32_16x16x32_bf16 v[168:171], v[216:219], a[176:179], v[168:171]\n"
"ds_read_b64_tr_b16 a[166:167], v246 offset:59392\n"
"ds_read_b64_tr_b16 a[168:169], v247 offset:51200\n"
"v_mfma_f32_16x16x32_bf16 v[168:171], v[212:215], a[180:183], v[168:171]\n"
"v_exp_f32 v199, v199\n"
"v_mfma_f32_16x16x32_bf16 v[172:175], v[212:215], a[184:187], v[172:175]\n"
"ds_read_b64_tr_b16 a[170:171], v247 offset:59392\n"
"v_cvt_pk_bf16_f32 v211, v198, v199\n"
"v_mfma_f32_16x16x32_bf16 v[172:175], v[216:219], a[188:191], v[172:175]\n"
"v_exp_f32 v200, v200\n"
"v_mfma_f32_16x16x32_bf16 v[176:179], v[216:219], a[192:195], v[176:179]\n"
"ds_read_b64_tr_b16 a[172:173], v247 offset:34816\n"
"ds_read_b64_tr_b16 a[174:175], v247 offset:43008\n"
"v_mfma_f32_16x16x32_bf16 v[176:179], v[212:215], a[196:199], v[176:179]\n"
"v_exp_f32 v201, v201\n"
"v_mfma_f32_16x16x32_bf16 v[180:183], v[212:215], a[200:203], v[180:183]\n"
"ds_read_b64_tr_b16 a[176:177], v246 offset:35328\n"
"ds_read_b64_tr_b16 a[178:179], v246 offset:43520\n"
"v_mfma_f32_16x16x32_bf16 v[180:183], v[216:219], a[204:207], v[180:183]\n"
"v_exp_f32 v202, v202\n"
"v_mfma_f32_16x16x32_bf16 v[184:187], v[216:219], a[208:211], v[184:187]\n"
"ds_read_b64_tr_b16 a[180:181], v246 offset:51712\n"
"ds_read_b64_tr_b16 a[182:183], v246 offset:59904\n"
"v_mfma_f32_16x16x32_bf16 v[184:187], v[212:215], a[212:215], v[184:187]\n"
"v_exp_f32 v203, v203\n"
"v_mfma_f32_16x16x32_bf16 v[188:191], v[212:215], a[216:219], v[188:191]\n"
"ds_read_b64_tr_b16 a[184:185], v247 offset:51712\n"
"ds_read_b64_tr_b16 a[186:187], v247 offset:59904\n"
"v_mfma_f32_16x16x32_bf16 v[188:191], v[216:219], a[220:223], v[188:191]\n"
"ds_read_b64_tr_b16 a[188:189], v247 offset:35328\n"
"ds_read_b64_tr_b16 a[190:191], v247 offset:43520\n"
"v_mfma_f32_16x16x32_bf16 v[192:195], a[96:99], a[16:19], v[224:227]\n"
"v_exp_f32 v204, v204\n"
"v_mfma_f32_16x16x32_bf16 v[192:195], a[100:103], a[20:23], v[192:195]\n"
"v_cvt_pk_bf16_f32 v212, v200, v201\n"
"v_cvt_pk_bf16_f32 v213, v202, v203\n"
"v_mfma_f32_16x16x32_bf16 v[192:195], a[104:107], a[24:27], v[192:195]\n"
"v_exp_f32 v205, v205\n"
"v_mfma_f32_16x16x32_bf16 v[192:195], a[108:111], a[28:31], v[192:195]\n"
"ds_read_b64_tr_b16 a[192:193], v246 offset:35840\n"
"v_cvt_pk_bf16_f32 v214, v204, v205\n"
"v_mfma_f32_16x16x32_bf16 v[196:199], a[112:115], a[28:31], v[224:227]\n"
"v_exp_f32 v206, v206\n"
"v_mfma_f32_16x16x32_bf16 v[196:199], a[116:119], a[24:27], v[196:199]\n"
"ds_read_b64_tr_b16 a[194:195], v246 offset:44032\n"
"ds_read_b64_tr_b16 a[196:197], v246 offset:52224\n"
"v_mfma_f32_16x16x32_bf16 v[196:199], a[120:123], a[20:23], v[196:199]\n"
"v_exp_f32 v207, v207\n"
"v_mfma_f32_16x16x32_bf16 v[196:199], a[124:127], a[16:19], v[196:199]\n"
"ds_read_b64_tr_b16 a[198:199], v246 offset:60416\n"
"v_cvt_pk_bf16_f32 v215, v206, v207\n"
"v_mfma_f32_16x16x32_bf16 v[200:203], a[128:131], a[16:19], v[224:227]\n"
"v_exp_f32 v192, v192\n"
"v_mfma_f32_16x16x32_bf16 v[200:203], a[132:135], a[20:23], v[200:203]\n"
"ds_read_b64_tr_b16 a[200:201], v247 offset:52224\n"
"ds_read_b64_tr_b16 a[202:203], v247 offset:60416\n"
"v_mfma_f32_16x16x32_bf16 v[200:203], a[136:139], a[24:27], v[200:203]\n"
"v_exp_f32 v193, v193\n"
"v_mfma_f32_16x16x32_bf16 v[200:203], a[140:143], a[28:31], v[200:203]\n"
"ds_read_b64_tr_b16 a[204:205], v247 offset:35840\n"
"v_cvt_pk_bf16_f32 v216, v192, v193\n"
"v_mfma_f32_16x16x32_bf16 v[204:207], a[144:147], a[28:31], v[224:227]\n"
"v_exp_f32 v194, v194\n"
"v_mfma_f32_16x16x32_bf16 v[204:207], a[148:151], a[24:27], v[204:207]\n"
"ds_read_b64_tr_b16 a[206:207], v247 offset:44032\n"
"ds_read_b64_tr_b16 a[208:209], v246 offset:36352\n"
"v_mfma_f32_16x16x32_bf16 v[204:207], a[152:155], a[20:23], v[204:207]\n"
"v_exp_f32 v195, v195\n"
"v_mfma_f32_16x16x32_bf16 v[204:207], a[156:159], a[16:19], v[204:207]\n"
"v_cvt_pk_bf16_f32 v217, v194, v195\n"
"s_waitcnt lgkmcnt(15)\n"
"v_smfmac_f32_16x16x64_bf16 a[224:227], a[248:251], v[208:215], v252\n"
"v_exp_f32 v196, v196\n"
"v_mfma_f32_16x16x32_bf16 v[0:3], v[208:211], a[160:163], v[0:3]\n"
"ds_read_b64_tr_b16 a[210:211], v246 offset:44544\n"
"ds_read_b64_tr_b16 a[212:213], v246 offset:52736\n"
"v_mfma_f32_16x16x32_bf16 v[0:3], v[212:215], a[164:167], v[0:3]\n"
"v_exp_f32 v197, v197\n"
"v_mfma_f32_16x16x32_bf16 v[4:7], v[212:215], a[168:171], v[4:7]\n"
"v_cvt_pk_bf16_f32 v218, v196, v197\n"
"s_waitcnt lgkmcnt(11)\n"
"v_mfma_f32_16x16x32_bf16 v[4:7], v[208:211], a[172:175], v[4:7]\n"
"v_exp_f32 v198, v198\n"
"v_mfma_f32_16x16x32_bf16 v[8:11], v[208:211], a[176:179], v[8:11]\n"
"ds_read_b64_tr_b16 a[214:215], v246 offset:60928\n"
"ds_read_b64_tr_b16 a[216:217], v247 offset:52736\n"
"ds_read_b64_tr_b16 a[218:219], v247 offset:60928\n"
"ds_read_b64_tr_b16 a[220:221], v247 offset:36352\n"
"ds_read_b64_tr_b16 a[222:223], v247 offset:44544\n"
"v_mfma_f32_16x16x32_bf16 v[8:11], v[212:215], a[180:183], v[8:11]\n"
"v_exp_f32 v199, v199\n"
"v_mfma_f32_16x16x32_bf16 v[12:15], v[212:215], a[184:187], v[12:15]\n"
"v_cvt_pk_bf16_f32 v219, v198, v199\n"
"s_waitcnt lgkmcnt(8)\n"
"v_mfma_f32_16x16x32_bf16 v[12:15], v[208:211], a[188:191], v[12:15]\n"
"v_exp_f32 v200, v200\n"
"v_mfma_f32_16x16x32_bf16 v[16:19], v[208:211], a[192:195], v[16:19]\n"
"buffer_load_dwordx4 v250, s[40:43], s24 offen offset:2048 nt lds\n"
"v_mfma_f32_16x16x32_bf16 v[16:19], v[212:215], a[196:199], v[16:19]\n"
"v_exp_f32 v201, v201\n"
"v_mfma_f32_16x16x32_bf16 v[20:23], v[212:215], a[200:203], v[20:23]\n"
"s_waitcnt lgkmcnt(0)\n"
"v_mfma_f32_16x16x32_bf16 v[20:23], v[208:211], a[204:207], v[20:23]\n"
"v_exp_f32 v202, v202\n"
"v_mfma_f32_16x16x32_bf16 v[24:27], v[208:211], a[208:211], v[24:27]\n"
"buffer_load_dwordx4 v251, s[40:43], s24 offen offset:3072 nt lds\n"
"v_mfma_f32_16x16x32_bf16 v[24:27], v[212:215], a[212:215], v[24:27]\n"
"v_exp_f32 v203, v203\n"
"v_mfma_f32_16x16x32_bf16 v[28:31], v[212:215], a[216:219], v[28:31]\n"
"v_mfma_f32_16x16x32_bf16 v[28:31], v[208:211], a[220:223], v[28:31]\n"
"v_mfma_f32_16x16x32_bf16 v[192:195], a[96:99], a[32:35], v[228:231]\n"
"v_exp_f32 v204, v204\n"
"v_mfma_f32_16x16x32_bf16 v[192:195], a[100:103], a[36:39], v[192:195]\n"
"v_cvt_pk_bf16_f32 v212, v200, v201\n"
"v_cvt_pk_bf16_f32 v213, v202, v203\n"
"v_mfma_f32_16x16x32_bf16 v[192:195], a[104:107], a[40:43], v[192:195]\n"
"v_exp_f32 v205, v205\n"
"v_mfma_f32_16x16x32_bf16 v[192:195], a[108:111], a[44:47], v[192:195]\n"
"v_cvt_pk_bf16_f32 v214, v204, v205\n"
"v_mfma_f32_16x16x32_bf16 v[196:199], a[112:115], a[44:47], v[228:231]\n"
"v_exp_f32 v206, v206\n"
"v_mfma_f32_16x16x32_bf16 v[196:199], a[116:119], a[40:43], v[196:199]\n"
"v_mfma_f32_16x16x32_bf16 v[196:199], a[120:123], a[36:39], v[196:199]\n"
"v_exp_f32 v207, v207\n"
"v_mfma_f32_16x16x32_bf16 v[196:199], a[124:127], a[32:35], v[196:199]\n"
"v_cvt_pk_bf16_f32 v215, v206, v207\n"
"v_mfma_f32_16x16x32_bf16 v[200:203], a[128:131], a[32:35], v[228:231]\n"
"v_exp_f32 v192, v192\n"
"v_mfma_f32_16x16x32_bf16 v[200:203], a[132:135], a[36:39], v[200:203]\n"
"v_mfma_f32_16x16x32_bf16 v[200:203], a[136:139], a[40:43], v[200:203]\n"
"v_exp_f32 v193, v193\n"
"v_mfma_f32_16x16x32_bf16 v[200:203], a[140:143], a[44:47], v[200:203]\n"
"v_cvt_pk_bf16_f32 v208, v192, v193\n"
"v_mfma_f32_16x16x32_bf16 v[204:207], a[144:147], a[44:47], v[228:231]\n"
"v_exp_f32 v194, v194\n"
"v_mfma_f32_16x16x32_bf16 v[204:207], a[148:151], a[40:43], v[204:207]\n"
"s_add_u32 m0, s25, 49152\n"
"s_add_u32 s24, s24, 8192\n"
"v_mfma_f32_16x16x32_bf16 v[204:207], a[152:155], a[36:39], v[204:207]\n"
"v_exp_f32 v195, v195\n"
"v_mfma_f32_16x16x32_bf16 v[204:207], a[156:159], a[32:35], v[204:207]\n"
"v_cvt_pk_bf16_f32 v209, v194, v195\n"
"v_smfmac_f32_16x16x64_bf16 a[228:231], a[248:251], v[212:219], v252\n"
"v_exp_f32 v196, v196\n"
"v_mfma_f32_16x16x32_bf16 v[32:35], v[216:219], a[160:163], v[32:35]\n"
"buffer_load_dwordx4 v248, s[36:39], s24 offen nt lds\n"
"v_mfma_f32_16x16x32_bf16 v[32:35], v[212:215], a[164:167], v[32:35]\n"
"v_exp_f32 v197, v197\n"
"v_mfma_f32_16x16x32_bf16 v[36:39], v[212:215], a[168:171], v[36:39]\n"
"v_cvt_pk_bf16_f32 v210, v196, v197\n"
"v_mfma_f32_16x16x32_bf16 v[36:39], v[216:219], a[172:175], v[36:39]\n"
"v_exp_f32 v198, v198\n"
"v_mfma_f32_16x16x32_bf16 v[40:43], v[216:219], a[176:179], v[40:43]\n"
"v_mfma_f32_16x16x32_bf16 v[40:43], v[212:215], a[180:183], v[40:43]\n"
"v_exp_f32 v199, v199\n"
"v_mfma_f32_16x16x32_bf16 v[44:47], v[212:215], a[184:187], v[44:47]\n"
"v_cvt_pk_bf16_f32 v211, v198, v199\n"
"v_mfma_f32_16x16x32_bf16 v[44:47], v[216:219], a[188:191], v[44:47]\n"
"v_exp_f32 v200, v200\n"
"v_mfma_f32_16x16x32_bf16 v[48:51], v[216:219], a[192:195], v[48:51]\n"
"v_mfma_f32_16x16x32_bf16 v[48:51], v[212:215], a[196:199], v[48:51]\n"
"v_exp_f32 v201, v201\n"
"v_mfma_f32_16x16x32_bf16 v[52:55], v[212:215], a[200:203], v[52:55]\n"
"v_mfma_f32_16x16x32_bf16 v[52:55], v[216:219], a[204:207], v[52:55]\n"
"v_exp_f32 v202, v202\n"
"v_mfma_f32_16x16x32_bf16 v[56:59], v[216:219], a[208:211], v[56:59]\n"
"v_mfma_f32_16x16x32_bf16 v[56:59], v[212:215], a[212:215], v[56:59]\n"
"v_exp_f32 v203, v203\n"
"v_mfma_f32_16x16x32_bf16 v[60:63], v[212:215], a[216:219], v[60:63]\n"
"v_mfma_f32_16x16x32_bf16 v[60:63], v[216:219], a[220:223], v[60:63]\n"
"v_mfma_f32_16x16x32_bf16 v[192:195], a[96:99], a[48:51], v[232:235]\n"
"v_exp_f32 v204, v204\n"
"v_mfma_f32_16x16x32_bf16 v[192:195], a[100:103], a[52:55], v[192:195]\n"
"v_cvt_pk_bf16_f32 v212, v200, v201\n"
"v_cvt_pk_bf16_f32 v213, v202, v203\n"
"v_mfma_f32_16x16x32_bf16 v[192:195], a[104:107], a[56:59], v[192:195]\n"
"v_exp_f32 v205, v205\n"
"v_mfma_f32_16x16x32_bf16 v[192:195], a[108:111], a[60:63], v[192:195]\n"
"v_cvt_pk_bf16_f32 v214, v204, v205\n"
"v_mfma_f32_16x16x32_bf16 v[196:199], a[112:115], a[60:63], v[232:235]\n"
"v_exp_f32 v206, v206\n"
"v_mfma_f32_16x16x32_bf16 v[196:199], a[116:119], a[56:59], v[196:199]\n"
"buffer_load_dwordx4 v249, s[36:39], s24 offen offset:1024 nt lds\n"
"v_mfma_f32_16x16x32_bf16 v[196:199], a[120:123], a[52:55], v[196:199]\n"
"v_exp_f32 v207, v207\n"
"v_mfma_f32_16x16x32_bf16 v[196:199], a[124:127], a[48:51], v[196:199]\n"
"v_cvt_pk_bf16_f32 v215, v206, v207\n"
"v_mfma_f32_16x16x32_bf16 v[200:203], a[128:131], a[48:51], v[232:235]\n"
"v_exp_f32 v192, v192\n"
"v_mfma_f32_16x16x32_bf16 v[200:203], a[132:135], a[52:55], v[200:203]\n"
"v_mfma_f32_16x16x32_bf16 v[200:203], a[136:139], a[56:59], v[200:203]\n"
"v_exp_f32 v193, v193\n"
"v_mfma_f32_16x16x32_bf16 v[200:203], a[140:143], a[60:63], v[200:203]\n"
"v_cvt_pk_bf16_f32 v216, v192, v193\n"
"v_mfma_f32_16x16x32_bf16 v[204:207], a[144:147], a[60:63], v[232:235]\n"
"v_exp_f32 v194, v194\n"
"v_mfma_f32_16x16x32_bf16 v[204:207], a[148:151], a[56:59], v[204:207]\n"
"v_mfma_f32_16x16x32_bf16 v[204:207], a[152:155], a[52:55], v[204:207]\n"
"v_exp_f32 v195, v195\n"
"v_mfma_f32_16x16x32_bf16 v[204:207], a[156:159], a[48:51], v[204:207]\n"
"v_cvt_pk_bf16_f32 v217, v194, v195\n"
"v_smfmac_f32_16x16x64_bf16 a[232:235], a[248:251], v[208:215], v252\n"
"v_exp_f32 v196, v196\n"
"v_mfma_f32_16x16x32_bf16 v[64:67], v[208:211], a[160:163], v[64:67]\n"
"v_mfma_f32_16x16x32_bf16 v[64:67], v[212:215], a[164:167], v[64:67]\n"
"v_exp_f32 v197, v197\n"
"v_mfma_f32_16x16x32_bf16 v[68:71], v[212:215], a[168:171], v[68:71]\n"
"v_cvt_pk_bf16_f32 v218, v196, v197\n"
"v_mfma_f32_16x16x32_bf16 v[68:71], v[208:211], a[172:175], v[68:71]\n"
"v_exp_f32 v198, v198\n"
"v_mfma_f32_16x16x32_bf16 v[72:75], v[208:211], a[176:179], v[72:75]\n"
"v_mfma_f32_16x16x32_bf16 v[72:75], v[212:215], a[180:183], v[72:75]\n"
"v_exp_f32 v199, v199\n"
"v_mfma_f32_16x16x32_bf16 v[76:79], v[212:215], a[184:187], v[76:79]\n"
"v_cvt_pk_bf16_f32 v219, v198, v199\n"
"v_mfma_f32_16x16x32_bf16 v[76:79], v[208:211], a[188:191], v[76:79]\n"
"v_exp_f32 v200, v200\n"
"v_mfma_f32_16x16x32_bf16 v[80:83], v[208:211], a[192:195], v[80:83]\n"
"s_add_u32 s27, s27, 1\n"
"v_mov_b32 v255, s27\n"
"v_mfma_f32_16x16x32_bf16 v[80:83], v[212:215], a[196:199], v[80:83]\n"
"v_exp_f32 v201, v201\n"
"v_mfma_f32_16x16x32_bf16 v[84:87], v[212:215], a[200:203], v[84:87]\n"
"v_mfma_f32_16x16x32_bf16 v[84:87], v[208:211], a[204:207], v[84:87]\n"
"v_exp_f32 v202, v202\n"
"v_mfma_f32_16x16x32_bf16 v[88:91], v[208:211], a[208:211], v[88:91]\n"
"v_mfma_f32_16x16x32_bf16 v[88:91], v[212:215], a[212:215], v[88:91]\n"
"v_exp_f32 v203, v203\n"
"v_mfma_f32_16x16x32_bf16 v[92:95], v[212:215], a[216:219], v[92:95]\n"
"s_waitcnt vmcnt(6)\n"
"ds_write_b32 v253, v255\n"
"v_mfma_f32_16x16x32_bf16 v[92:95], v[208:211], a[220:223], v[92:95]\n"
"v_mfma_f32_16x16x32_bf16 v[192:195], a[96:99], a[64:67], v[236:239]\n"
"v_exp_f32 v204, v204\n"
"v_mfma_f32_16x16x32_bf16 v[192:195], a[100:103], a[68:71], v[192:195]\n"
"v_cvt_pk_bf16_f32 v212, v200, v201\n"
"v_cvt_pk_bf16_f32 v213, v202, v203\n"
"v_mfma_f32_16x16x32_bf16 v[192:195], a[104:107], a[72:75], v[192:195]\n"
"v_exp_f32 v205, v205\n"
"v_mfma_f32_16x16x32_bf16 v[192:195], a[108:111], a[76:79], v[192:195]\n"
"v_cvt_pk_bf16_f32 v214, v204, v205\n"
"v_mfma_f32_16x16x32_bf16 v[196:199], a[112:115], a[76:79], v[236:239]\n"
"v_exp_f32 v206, v206\n"
"v_mfma_f32_16x16x32_bf16 v[196:199], a[116:119], a[72:75], v[196:199]\n"
"buffer_load_dwordx4 v250, s[40:43], s24 offen offset:2048 nt lds\n"
"v_mfma_f32_16x16x32_bf16 v[196:199], a[120:123], a[68:71], v[196:199]\n"
"v_exp_f32 v207, v207\n"
"v_mfma_f32_16x16x32_bf16 v[196:199], a[124:127], a[64:67], v[196:199]\n"
"v_cvt_pk_bf16_f32 v215, v206, v207\n"
"v_mfma_f32_16x16x32_bf16 v[200:203], a[128:131], a[64:67], v[236:239]\n"
"v_exp_f32 v192, v192\n"
"v_mfma_f32_16x16x32_bf16 v[200:203], a[132:135], a[68:71], v[200:203]\n"
"v_xor_b32 v244, 0x10000, v244\n"
"v_xor_b32 v245, 0x10000, v245\n"
"v_mfma_f32_16x16x32_bf16 v[200:203], a[136:139], a[72:75], v[200:203]\n"
"v_exp_f32 v193, v193\n"
"v_mfma_f32_16x16x32_bf16 v[200:203], a[140:143], a[76:79], v[200:203]\n"
"v_cvt_pk_bf16_f32 v208, v192, v193\n"
"v_mfma_f32_16x16x32_bf16 v[204:207], a[144:147], a[76:79], v[236:239]\n"
"v_exp_f32 v194, v194\n"
"v_mfma_f32_16x16x32_bf16 v[204:207], a[148:151], a[72:75], v[204:207]\n"
"v_mfma_f32_16x16x32_bf16 v[204:207], a[152:155], a[68:71], v[204:207]\n"
"v_exp_f32 v195, v195\n"
"v_mfma_f32_16x16x32_bf16 v[204:207], a[156:159], a[64:67], v[204:207]\n"
"v_cvt_pk_bf16_f32 v209, v194, v195\n"
"v_smfmac_f32_16x16x64_bf16 a[236:239], a[248:251], v[212:219], v252\n"
"v_exp_f32 v196, v196\n"
"v_mfma_f32_16x16x32_bf16 v[96:99], v[216:219], a[160:163], v[96:99]\n"
"v_mfma_f32_16x16x32_bf16 v[96:99], v[212:215], a[164:167], v[96:99]\n"
"v_exp_f32 v197, v197\n"
"v_mfma_f32_16x16x32_bf16 v[100:103], v[212:215], a[168:171], v[100:103]\n"
"v_cvt_pk_bf16_f32 v210, v196, v197\n"
"v_mfma_f32_16x16x32_bf16 v[100:103], v[216:219], a[172:175], v[100:103]\n"
"v_exp_f32 v198, v198\n"
"v_mfma_f32_16x16x32_bf16 v[104:107], v[216:219], a[176:179], v[104:107]\n"
"ds_read_b32 v255, v254\n"
"v_mfma_f32_16x16x32_bf16 v[104:107], v[212:215], a[180:183], v[104:107]\n"
"v_exp_f32 v199, v199\n"
"v_mfma_f32_16x16x32_bf16 v[108:111], v[212:215], a[184:187], v[108:111]\n"
"v_cvt_pk_bf16_f32 v211, v198, v199\n"
"v_mfma_f32_16x16x32_bf16 v[108:111], v[216:219], a[188:191], v[108:111]\n"
"v_exp_f32 v200, v200\n"
"v_mfma_f32_16x16x32_bf16 v[112:115], v[216:219], a[192:195], v[112:115]\n"
"buffer_load_dwordx4 v251, s[40:43], s24 offen offset:3072 nt lds\n"
"v_mfma_f32_16x16x32_bf16 v[112:115], v[212:215], a[196:199], v[112:115]\n"
"v_exp_f32 v201, v201\n"
"v_mfma_f32_16x16x32_bf16 v[116:119], v[212:215], a[200:203], v[116:119]\n"
"v_mfma_f32_16x16x32_bf16 v[116:119], v[216:219], a[204:207], v[116:119]\n"
"v_exp_f32 v202, v202\n"
"v_mfma_f32_16x16x32_bf16 v[120:123], v[216:219], a[208:211], v[120:123]\n"
"v_mfma_f32_16x16x32_bf16 v[120:123], v[212:215], a[212:215], v[120:123]\n"
"v_exp_f32 v203, v203\n"
"v_mfma_f32_16x16x32_bf16 v[124:127], v[212:215], a[216:219], v[124:127]\n"
"s_waitcnt lgkmcnt(0)\n"
"v_cmp_gt_u32 vcc, s27, v255\n"
"v_mfma_f32_16x16x32_bf16 v[124:127], v[216:219], a[220:223], v[124:127]\n"
"s_cbranch_vccnz L_late6_6_%=\n"
"L_back6_6_%=:\n"
"v_mfma_f32_16x16x32_bf16 v[192:195], a[96:99], a[80:83], v[240:243]\n"
"v_exp_f32 v204, v204\n"
"v_mfma_f32_16x16x32_bf16 v[192:195], a[100:103], a[84:87], v[192:195]\n"
"v_cvt_pk_bf16_f32 v212, v200, v201\n"
"v_cvt_pk_bf16_f32 v213, v202, v203\n"
"v_mfma_f32_16x16x32_bf16 v[192:195], a[104:107], a[88:91], v[192:195]\n"
"v_exp_f32 v205, v205\n"
"v_mfma_f32_16x16x32_bf16 v[192:195], a[108:111], a[92:95], v[192:195]\n"
"ds_read_b128 a[96:99], v244 offset:0\n"
"v_cvt_pk_bf16_f32 v214, v204, v205\n"
"v_mfma_f32_16x16x32_bf16 v[196:199], a[112:115], a[92:95], v[240:243]\n"
"v_exp_f32 v206, v206\n"
"v_mfma_f32_16x16x32_bf16 v[196:199], a[116:119], a[88:91], v[196:199]\n"
"ds_read_b128 a[100:103], v245 offset:0\n"
"ds_read_b128 a[104:107], v244 offset:1024\n"
"v_mfma_f32_16x16x32_bf16 v[196:199], a[120:123], a[84:87], v[196:199]\n"
"v_exp_f32 v207, v207\n"
"v_mfma_f32_16x16x32_bf16 v[196:199], a[124:127], a[80:83], v[196:199]\n"
"ds_read_b128 a[108:111], v245 offset:1024\n"
"v_cvt_pk_bf16_f32 v215, v206, v207\n"
"v_mfma_f32_16x16x32_bf16 v[200:203], a[128:131], a[80:83], v[240:243]\n"
"v_exp_f32 v192, v192\n"
"v_mfma_f32_16x16x32_bf16 v[200:203], a[132:135], a[84:87], v[200:203]\n"
"ds_read_b128 a[112:115], v245 offset:9216\n"
"ds_read_b128 a[116:119], v244 offset:9216\n"
"v_mfma_f32_16x16x32_bf16 v[200:203], a[136:139], a[88:91], v[200:203]\n"
"v_exp_f32 v193, v193\n"
"v_mfma_f32_16x16x32_bf16 v[200:203], a[140:143], a[92:95], v[200:203]\n"
"ds_read_b128 a[120:123], v245 offset:8192\n"
"v_cvt_pk_bf16_f32 v216, v192, v193\n"
"v_mfma_f32_16x16x32_bf16 v[204:207], a[144:147], a[92:95], v[240:243]\n"
"v_exp_f32 v194, v194\n"
"v_mfma_f32_16x16x32_bf16 v[204:207], a[148:151], a[88:91], v[204:207]\n"
"ds_read_b128 a[124:127], v244 offset:8192\n"
"v_mfma_f32_16x16x32_bf16 v[204:207], a[152:155], a[84:87], v[204:207]\n"
"v_exp_f32 v195, v195\n"
"v_mfma_f32_16x16x32_bf16 v[204:207], a[156:159], a[80:83], v[204:207]\n"
"ds_read_b128 a[128:131], v244 offset:16384\n"
"v_cvt_pk_bf16_f32 v217, v194, v195\n"
"v_smfmac_f32_16x16x64_bf16 a[240:243], a[248:251], v[208:215], v252\n"
"v_exp_f32 v196, v196\n"
"v_mfma_f32_16x16x32_bf16 v[128:131], v[208:211], a[160:163], v[128:131]\n"
"ds_read_b128 a[132:135], v245 offset:16384\n"
"v_mfma_f32_16x16x32_bf16 v[128:131], v[212:215], a[164:167], v[128:131]\n"
"v_exp_f32 v197, v197\n"
"v_mfma_f32_16x16x32_bf16 v[132:135], v[212:215], a[168:171], v[132:135]\n"
"ds_read_b128 a[136:139], v244 offset:17408\n"
"v_cvt_pk_bf16_f32 v218, v196, v197\n"
"v_mfma_f32_16x16x32_bf16 v[132:135], v[208:211], a[172:175], v[132:135]\n"
"v_exp_f32 v198, v198\n"
"v_mfma_f32_16x16x32_bf16 v[136:139], v[208:211], a[176:179], v[136:139]\n"
"ds_read_b128 a[140:143], v245 offset:17408\n"
"ds_read_b128 a[144:147], v245 offset:25600\n"
"v_mfma_f32_16x16x32_bf16 v[136:139], v[212:215], a[180:183], v[136:139]\n"
"v_exp_f32 v199, v199\n"
"v_mfma_f32_16x16x32_bf16 v[140:143], v[212:215], a[184:187], v[140:143]\n"
"v_cvt_pk_bf16_f32 v219, v198, v199\n"
"s_xor_b32 s25, s25, 0x10000\n"
"v_mfma_f32_16x16x32_bf16 v[140:143], v[208:211], a[188:191], v[140:143]\n"
"v_exp_f32 v200, v200\n"
"v_mfma_f32_16x16x32_bf16 v[144:147], v[208:211], a[192:195], v[144:147]\n"
"s_mov_b32 m0, s25\n"
"s_add_u32 s24, s24, 8192\n"
"v_mfma_f32_16x16x32_bf16 v[144:147], v[212:215], a[196:199], v[144:147]\n"
"v_exp_f32 v201, v201\n"
"v_mfma_f32_16x16x32_bf16 v[148:151], v[212:215], a[200:203], v[148:151]\n"
"buffer_load_dwordx4 v248, s[36:39], s24 offen nt lds\n"
"v_mfma_f32_16x16x32_bf16 v[148:151], v[208:211], a[204:207], v[148:151]\n"
"v_exp_f32 v202, v202\n"
"v_mfma_f32_16x16x32_bf16 v[152:155], v[208:211], a[208:211], v[152:155]\n"
"ds_read_b128 a[148:151], v244 offset:25600\n"
"ds_read_b128 a[152:155], v245 offset:24576\n"
"ds_read_b128 a[156:159], v244 offset:24576\n"
"v_mfma_f32_16x16x32_bf16 v[152:155], v[212:215], a[212:215], v[152:155]\n"
"v_exp_f32 v203, v203\n"
"v_mfma_f32_16x16x32_bf16 v[156:159], v[212:215], a[216:219], v[156:159]\n"
"v_mfma_f32_16x16x32_bf16 v[156:159], v[208:211], a[220:223], v[156:159]\n"
"s_waitcnt lgkmcnt(8)\n"
"v_mfma_f32_16x16x32_bf16 v[192:195], a[96:99], a[0:3], v[220:223]\n"
"v_exp_f32 v204, v204\n"
"v_mfma_f32_16x16x32_bf16 v[192:195], a[100:103], a[4:7], v[192:195]\n"
"v_cvt_pk_bf16_f32 v212, v200, v201\n"
"v_cvt_pk_bf16_f32 v213, v202, v203\n"
"v_mfma_f32_16x16x32_bf16 v[192:195], a[104:107], a[8:11], v[192:195]\n"
"v_exp_f32 v205, v205\n"
"v_mfma_f32_16x16x32_bf16 v[192:195], a[108:111], a[12:15], v[192:195]\n"
"v_cvt_pk_bf16_f32 v214, v204, v205\n"
"v_mfma_f32_16x16x32_bf16 v[196:199], a[112:115], a[12:15], v[220:223]\n"
"v_exp_f32 v206, v206\n"
"v_mfma_f32_16x16x32_bf16 v[196:199], a[116:119], a[8:11], v[196:199]\n"
"buffer_load_dwordx4 v249, s[36:39], s24 offen offset:1024 nt lds\n"
"v_mfma_f32_16x16x32_bf16 v[196:199], a[120:123], a[4:7], v[196:199]\n"
"v_exp_f32 v207, v207\n"
"v_mfma_f32_16x16x32_bf16 v[196:199], a[124:127], a[0:3], v[196:199]\n"
"v_cvt_pk_bf16_f32 v215, v206, v207\n"
"s_waitcnt lgkmcnt(0)\n"
"v_mfma_f32_16x16x32_bf16 v[200:203], a[128:131], a[0:3], v[220:223]\n"
"v_exp_f32 v192, v192\n"
"v_mfma_f32_16x16x32_bf16 v[200:203], a[132:135], a[4:7], v[200:203]\n"
"v_xor_b32 v246, 0x10000, v246\n"
"v_xor_b32 v247, 0x10000, v247\n"
"v_mfma_f32_16x16x32_bf16 v[200:203], a[136:139], a[8:11], v[200:203]\n"
"v_exp_f32 v193, v193\n"
"v_mfma_f32_16x16x32_bf16 v[200:203], a[140:143], a[12:15], v[200:203]\n"
"v_cvt_pk_bf16_f32 v208, v192, v193\n"
"v_mfma_f32_16x16x32_bf16 v[204:207], a[144:147], a[12:15], v[220:223]\n"
"v_exp_f32 v194, v194\n"
"v_mfma_f32_16x16x32_bf16 v[204:207], a[148:151], a[8:11], v[204:207]\n"
"v_mfma_f32_16x16x32_bf16 v[204:207], a[152:155], a[4:7], v[204:207]\n"
"v_exp_f32 v195, v195\n"
"v_mfma_f32_16x16x32_bf16 v[204:207], a[156:159], a[0:3], v[204:207]\n"
"v_cvt_pk_bf16_f32 v209, v194, v195\n"
"v_smfmac_f32_16x16x64_bf16 a[244:247], a[248:251], v[212:219], v252\n"
"v_exp_f32 v196, v196\n"
"v_mfma_f32_16x16x32_bf16 v[160:163], v[216:219], a[160:163], v[160:163]\n"
"ds_read_b64_tr_b16 a[160:161], v246 offset:2048\n"
"ds_read_b64_tr_b16 a[162:163], v246 offset:10240\n"
"v_mfma_f32_16x16x32_bf16 v[160:163], v[212:215], a[164:167], v[160:163]\n"
"v_exp_f32 v197, v197\n"
"v_mfma_f32_16x16x32_bf16 v[164:167], v[212:215], a[168:171], v[164:167]\n"
"ds_read_b64_tr_b16 a[164:165], v246 offset:18432\n"
"v_cvt_pk_bf16_f32 v210, v196, v197\n"
"v_mfma_f32_16x16x32_bf16 v[164:167], v[216:219], a[172:175], v[164:167]\n"
"v_exp_f32 v198, v198\n"
"v_mfma_f32_16x16x32_bf16 v[168:171], v[216:219], a[176:179], v[168:171]\n"
"ds_read_b64_tr_b16 a[166:167], v246 offset:26624\n"
"ds_read_b64_tr_b16 a[168:169], v247 offset:18432\n"
"v_mfma_f32_16x16x32_bf16 v[168:171], v[212:215], a[180:183], v[168:171]\n"
"v_exp_f32 v199, v199\n"
"v_mfma_f32_16x16x32_bf16 v[172:175], v[212:215], a[184:187], v[172:175]\n"
"ds_read_b64_tr_b16 a[170:171], v247 offset:26624\n"
"v_cvt_pk_bf16_f32 v211, v198, v199\n"
"v_mfma_f32_16x16x32_bf16 v[172:175], v[216:219], a[188:191], v[172:175]\n"
"v_exp_f32 v200, v200\n"
"v_mfma_f32_16x16x32_bf16 v[176:179], v[216:219], a[192:195], v[176:179]\n"
"ds_read_b64_tr_b16 a[172:173], v247 offset:2048\n"
"ds_read_b64_tr_b16 a[174:175], v247 offset:10240\n"
"v_mfma_f32_16x16x32_bf16 v[176:179], v[212:215], a[196:199], v[176:179]\n"
"v_exp_f32 v201, v201\n"
"v_mfma_f32_16x16x32_bf16 v[180:183], v[212:215], a[200:203], v[180:183]\n"
"ds_read_b64_tr_b16 a[176:177], v246 offset:2560\n"
"ds_read_b64_tr_b16 a[178:179], v246 offset:10752\n"
"v_mfma_f32_16x16x32_bf16 v[180:183], v[216:219], a[204:207], v[180:183]\n"
"v_exp_f32 v202, v202\n"
"v_mfma_f32_16x16x32_bf16 v[184:187], v[216:219], a[208:211], v[184:187]\n"
"ds_read_b64_tr_b16 a[180:181], v246 offset:18944\n"
"ds_read_b64_tr_b16 a[182:183], v246 offset:27136\n"
"v_mfma_f32_16x16x32_bf16 v[184:187], v[212:215], a[212:215], v[184:187]\n"
"v_exp_f32 v203, v203\n"
"v_mfma_f32_16x16x32_bf16 v[188:191], v[212:215], a[216:219], v[188:191]\n"
"ds_read_b64_tr_b16 a[184:185], v247 offset:18944\n"
"ds_read_b64_tr_b16 a[186:187], v247 offset:27136\n"
"v_mfma_f32_16x16x32_bf16 v[188:191], v[216:219], a[220:223], v[188:191]\n"
"ds_read_b64_tr_b16 a[188:189], v247 offset:2560\n"
"ds_read_b64_tr_b16 a[190:191], v247 offset:10752\n"
"v_mfma_f32_16x16x32_bf16 v[192:195], a[96:99], a[16:19], v[224:227]\n"
"v_exp_f32 v204, v204\n"
"v_mfma_f32_16x16x32_bf16 v[192:195], a[100:103], a[20:23], v[192:195]\n"
"v_cvt_pk_bf16_f32 v212, v200, v201\n"
"v_cvt_pk_bf16_f32 v213, v202, v203\n"
"v_mfma_f32_16x16x32_bf16 v[192:195], a[104:107], a[24:27], v[192:195]\n"
"v_exp_f32 v205, v205\n"
"v_mfma_f32_16x16x32_bf16 v[192:195], a[108:111], a[28:31], v[192:195]\n"
"ds_read_b64_tr_b16 a[192:193], v246 offset:3072\n"
"v_cvt_pk_bf16_f32 v214, v204, v205\n"
"v_mfma_f32_16x16x32_bf16 v[196:199], a[112:115], a[28:31], v[224:227]\n"
"v_exp_f32 v206, v206\n"
"v_mfma_f32_16x16x32_bf16 v[196:199], a[116:119], a[24:27], v[196:199]\n"
"ds_read_b64_tr_b16 a[194:195], v246 offset:11264\n"
"ds_read_b64_tr_b16 a[196:197], v246 offset:19456\n"
"v_mfma_f32_16x16x32_bf16 v[196:199], a[120:123], a[20:23], v[196:199]\n"
"v_exp_f32 v207, v207\n"
"v_mfma_f32_16x16x32_bf16 v[196:199], a[124:127], a[16:19], v[196:199]\n"
"ds_read_b64_tr_b16 a[198:199], v246 offset:27648\n"
"v_cvt_pk_bf16_f32 v215, v206, v207\n"
"v_mfma_f32_16x16x32_bf16 v[200:203], a[128:131], a[16:19], v[224:227]\n"
"v_exp_f32 v192, v192\n"
"v_mfma_f32_16x16x32_bf16 v[200:203], a[132:135], a[20:23], v[200:203]\n"
"ds_read_b64_tr_b16 a[200:201], v247 offset:19456\n"
"ds_read_b64_tr_b16 a[202:203], v247 offset:27648\n"
"v_mfma_f32_16x16x32_bf16 v[200:203], a[136:139], a[24:27], v[200:203]\n"
"v_exp_f32 v193, v193\n"
"v_mfma_f32_16x16x32_bf16 v[200:203], a[140:143], a[28:31], v[200:203]\n"
"ds_read_b64_tr_b16 a[204:205], v247 offset:3072\n"
"v_cvt_pk_bf16_f32 v216, v192, v193\n"
"v_mfma_f32_16x16x32_bf16 v[204:207], a[144:147], a[28:31], v[224:227]\n"
"v_exp_f32 v194, v194\n"
"v_mfma_f32_16x16x32_bf16 v[204:207], a[148:151], a[24:27], v[204:207]\n"
"ds_read_b64_tr_b16 a[206:207], v247 offset:11264\n"
"ds_read_b64_tr_b16 a[208:209], v246 offset:3584\n"
"v_mfma_f32_16x16x32_bf16 v[204:207], a[152:155], a[20:23], v[204:207]\n"
"v_exp_f32 v195, v195\n"
"v_mfma_f32_16x16x32_bf16 v[204:207], a[156:159], a[16:19], v[204:207]\n"
"v_cvt_pk_bf16_f32 v217, v194, v195\n"
"s_waitcnt lgkmcnt(15)\n"
"v_smfmac_f32_16x16x64_bf16 a[224:227], a[248:251], v[208:215], v252\n"
"v_exp_f32 v196, v196\n"
"v_mfma_f32_16x16x32_bf16 v[0:3], v[208:211], a[160:163], v[0:3]\n"
"ds_read_b64_tr_b16 a[210:211], v246 offset:11776\n"
"ds_read_b64_tr_b16 a[212:213], v246 offset:19968\n"
"v_mfma_f32_16x16x32_bf16 v[0:3], v[212:215], a[164:167], v[0:3]\n"
"v_exp_f32 v197, v197\n"
"v_mfma_f32_16x16x32_bf16 v[4:7], v[212:215], a[168:171], v[4:7]\n"
"v_cvt_pk_bf16_f32 v218, v196, v197\n"
"s_waitcnt lgkmcnt(11)\n"
"v_mfma_f32_16x16x32_bf16 v[4:7], v[208:211], a[172:175], v[4:7]\n"
"v_exp_f32 v198, v198\n"
"v_mfma_f32_16x16x32_bf16 v[8:11], v[208:211], a[176:179], v[8:11]\n"
"ds_read_b64_tr_b16 a[214:215], v246 offset:28160\n"
"ds_read_b64_tr_b16 a[216:217], v247 offset:19968\n"
"ds_read_b64_tr_b16 a[218:219], v247 offset:28160\n"
"ds_read_b64_tr_b16 a[220:221], v247 offset:3584\n"
"ds_read_b64_tr_b16 a[222:223], v247 offset:11776\n"
"v_mfma_f32_16x16x32_bf16 v[8:11], v[212:215], a[180:183], v[8:11]\n"
"v_exp_f32 v199, v199\n"
"v_mfma_f32_16x16x32_bf16 v[12:15], v[212:215], a[184:187], v[12:15]\n"
"v_cvt_pk_bf16_f32 v219, v198, v199\n"
"s_waitcnt lgkmcnt(8)\n"
"v_mfma_f32_16x16x32_bf16 v[12:15], v[208:211], a[188:191], v[12:15]\n"
"v_exp_f32 v200, v200\n"
"v_mfma_f32_16x16x32_bf16 v[16:19], v[208:211], a[192:195], v[16:19]\n"
"buffer_load_dwordx4 v250, s[40:43], s24 offen offset:2048 nt lds\n"
"v_mfma_f32_16x16x32_bf16 v[16:19], v[212:215], a[196:199], v[16:19]\n"
"v_exp_f32 v201, v201\n"
"v_mfma_f32_16x16x32_bf16 v[20:23], v[212:215], a[200:203], v[20:23]\n"
"s_waitcnt lgkmcnt(0)\n"
"v_mfma_f32_16x16x32_bf16 v[20:23], v[208:211], a[204:207], v[20:23]\n"
"v_exp_f32 v202, v202\n"
"v_mfma_f32_16x16x32_bf16 v[24:27], v[208:211], a[208:211], v[24:27]\n"
"buffer_load_dwordx4 v251, s[40:43], s24 offen offset:3072 nt lds\n"
"v_mfma_f32_16x16x32_bf16 v[24:27], v[212:215], a[212:215], v[24:27]\n"
"v_exp_f32 v203, v203\n"
"v_mfma_f32_16x16x32_bf16 v[28:31], v[212:215], a[216:219], v[28:31]\n"
"v_mfma_f32_16x16x32_bf16 v[28:31], v[208:211], a[220:223], v[28:31]\n"
"v_mfma_f32_16x16x32_bf16 v[192:195], a[96:99], a[32:35], v[228:231]\n"
"v_exp_f32 v204, v204\n"
"v_mfma_f32_16x16x32_bf16 v[192:195], a[100:103], a[36:39], v[192:195]\n"
"v_cvt_pk_bf16_f32 v212, v200, v201\n"
"v_cvt_pk_bf16_f32 v213, v202, v203\n"
"v_mfma_f32_16x16x32_bf16 v[192:195], a[104:107], a[40:43], v[192:195]\n"
"v_exp_f32 v205, v205\n"
"v_mfma_f32_16x16x32_bf16 v[192:195], a[108:111], a[44:47], v[192:195]\n"
"v_cvt_pk_bf16_f32 v214, v204, v205\n"
"v_mfma_f32_16x16x32_bf16 v[196:199], a[112:115], a[44:47], v[228:231]\n"
"v_exp_f32 v206, v206\n"
"v_mfma_f32_16x16x32_bf16 v[196:199], a[116:119], a[40:43], v[196:199]\n"
"v_mfma_f32_16x16x32_bf16 v[196:199], a[120:123], a[36:39], v[196:199]\n"
"v_exp_f32 v207, v207\n"
"v_mfma_f32_16x16x32_bf16 v[196:199], a[124:127], a[32:35], v[196:199]\n"
"v_cvt_pk_bf16_f32 v215, v206, v207\n"
"v_mfma_f32_16x16x32_bf16 v[200:203], a[128:131], a[32:35], v[228:231]\n"
"v_exp_f32 v192, v192\n"
"v_mfma_f32_16x16x32_bf16 v[200:203], a[132:135], a[36:39], v[200:203]\n"
"v_mfma_f32_16x16x32_bf16 v[200:203], a[136:139], a[40:43], v[200:203]\n"
"v_exp_f32 v193, v193\n"
"v_mfma_f32_16x16x32_bf16 v[200:203], a[140:143], a[44:47], v[200:203]\n"
"v_cvt_pk_bf16_f32 v208, v192, v193\n"
"v_mfma_f32_16x16x32_bf16 v[204:207], a[144:147], a[44:47], v[228:231]\n"
"v_exp_f32 v194, v194\n"
"v_mfma_f32_16x16x32_bf16 v[204:207], a[148:151], a[40:43], v[204:207]\n"
"s_add_u32 m0, s25, 16384\n"
"s_add_u32 s24, s24, 8192\n"
"v_mfma_f32_16x16x32_bf16 v[204:207], a[152:155], a[36:39], v[204:207]\n"
"v_exp_f32 v195, v195\n"
"v_mfma_f32_16x16x32_bf16 v[204:207], a[156:159], a[32:35], v[204:207]\n"
"v_cvt_pk_bf16_f32 v209, v194, v195\n"
"v_smfmac_f32_16x16x64_bf16 a[228:231], a[248:251], v[212:219], v252\n"
"v_exp_f32 v196, v196\n"
"v_mfma_f32_16x16x32_bf16 v[32:35], v[216:219], a[160:163], v[32:35]\n"
"buffer_load_dwordx4 v248, s[36:39], s24 offen nt lds\n"
"v_mfma_f32_16x16x32_bf16 v[32:35], v[212:215], a[164:167], v[32:35]\n"
"v_exp_f32 v197, v197\n"
"v_mfma_f32_16x16x32_bf16 v[36:39], v[212:215], a[168:171], v[36:39]\n"
"v_cvt_pk_bf16_f32 v210, v196, v197\n"
"v_mfma_f32_16x16x32_bf16 v[36:39], v[216:219], a[172:175], v[36:39]\n"
"v_exp_f32 v198, v198\n"
"v_mfma_f32_16x16x32_bf16 v[40:43], v[216:219], a[176:179], v[40:43]\n"
"v_mfma_f32_16x16x32_bf16 v[40:43], v[212:215], a[180:183], v[40:43]\n"
"v_exp_f32 v199, v199\n"
"v_mfma_f32_16x16x32_bf16 v[44:47], v[212:215], a[184:187], v[44:47]\n"
"v_cvt_pk_bf16_f32 v211, v198, v199\n"
"v_mfma_f32_16x16x32_bf16 v[44:47], v[216:219], a[188:191], v[44:47]\n"
"v_exp_f32 v200, v200\n"
"v_mfma_f32_16x16x32_bf16 v[48:51], v[216:219], a[192:195], v[48:51]\n"
"v_mfma_f32_16x16x32_bf16 v[48:51], v[212:215], a[196:199], v[48:51]\n"
"v_exp_f32 v201, v201\n"
"v_mfma_f32_16x16x32_bf16 v[52:55], v[212:215], a[200:203], v[52:55]\n"
"v_mfma_f32_16x16x32_bf16 v[52:55], v[216:219], a[204:207], v[52:55]\n"
"v_exp_f32 v202, v202\n"
"v_mfma_f32_16x16x32_bf16 v[56:59], v[216:219], a[208:211], v[56:59]\n"
"v_mfma_f32_16x16x32_bf16 v[56:59], v[212:215], a[212:215], v[56:59]\n"
"v_exp_f32 v203, v203\n"
"v_mfma_f32_16x16x32_bf16 v[60:63], v[212:215], a[216:219], v[60:63]\n"
"v_mfma_f32_16x16x32_bf16 v[60:63], v[216:219], a[220:223], v[60:63]\n"
"s_sub_u32 s26, s26, 1\n"
"s_cbranch_scc0 L_loop6_%=\n"
"v_mfma_f32_16x16x32_bf16 v[192:195], a[96:99], a[48:51], v[232:235]\n"
"v_exp_f32 v204, v204\n"
"v_mfma_f32_16x16x32_bf16 v[192:195], a[100:103], a[52:55], v[192:195]\n"
"v_cvt_pk_bf16_f32 v212, v200, v201\n"
"v_cvt_pk_bf16_f32 v213, v202, v203\n"
"v_mfma_f32_16x16x32_bf16 v[192:195], a[104:107], a[56:59], v[192:195]\n"
"v_exp_f32 v205, v205\n"
"v_mfma_f32_16x16x32_bf16 v[192:195], a[108:111], a[60:63], v[192:195]\n"
"v_cvt_pk_bf16_f32 v214, v204, v205\n"
"v_mfma_f32_16x16x32_bf16 v[196:199], a[112:115], a[60:63], v[232:235]\n"
"v_exp_f32 v206, v206\n"
"v_mfma_f32_16x16x32_bf16 v[196:199], a[116:119], a[56:59], v[196:199]\n"
"buffer_load_dwordx4 v249, s[36:39], s24 offen offset:1024 nt lds\n"
"v_mfma_f32_16x16x32_bf16 v[196:199], a[120:123], a[52:55], v[196:199]\n"
"v_exp_f32 v207, v207\n"
"v_mfma_f32_16x16x32_bf16 v[196:199], a[124:127], a[48:51], v[196:199]\n"
"v_cvt_pk_bf16_f32 v215, v206, v207\n"
"v_mfma_f32_16x16x32_bf16 v[200:203], a[128:131], a[48:51], v[232:235]\n"
"v_exp_f32 v192, v192\n"
"v_mfma_f32_16x16x32_bf16 v[200:203], a[132:135], a[52:55], v[200:203]\n"
"v_mfma_f32_16x16x32_bf16 v[200:203], a[136:139], a[56:59], v[200:203]\n"
"v_exp_f32 v193, v193\n"
"v_mfma_f32_16x16x32_bf16 v[200:203], a[140:143], a[60:63], v[200:203]\n"
"v_cvt_pk_bf16_f32 v216, v192, v193\n"
"v_mfma_f32_16x16x32_bf16 v[204:207], a[144:147], a[60:63], v[232:235]\n"
"v_exp_f32 v194, v194\n"
"v_mfma_f32_16x16x32_bf16 v[204:207], a[148:151], a[56:59], v[204:207]\n"
"v_mfma_f32_16x16x32_bf16 v[204:207], a[152:155], a[52:55], v[204:207]\n"
"v_exp_f32 v195, v195\n"
"v_mfma_f32_16x16x32_bf16 v[204:207], a[156:159], a[48:51], v[204:207]\n"
"v_cvt_pk_bf16_f32 v217, v194, v195\n"
"v_smfmac_f32_16x16x64_bf16 a[232:235], a[248:251], v[208:215], v252\n"
"v_exp_f32 v196, v196\n"
"v_mfma_f32_16x16x32_bf16 v[64:67], v[208:211], a[160:163], v[64:67]\n"
"v_mfma_f32_16x16x32_bf16 v[64:67], v[212:215], a[164:167], v[64:67]\n"
"v_exp_f32 v197, v197\n"
"v_mfma_f32_16x16x32_bf16 v[68:71], v[212:215], a[168:171], v[68:71]\n"
"v_cvt_pk_bf16_f32 v218, v196, v197\n"
"v_mfma_f32_16x16x32_bf16 v[68:71], v[208:211], a[172:175], v[68:71]\n"
"v_exp_f32 v198, v198\n"
"v_mfma_f32_16x16x32_bf16 v[72:75], v[208:211], a[176:179], v[72:75]\n"
"v_mfma_f32_16x16x32_bf16 v[72:75], v[212:215], a[180:183], v[72:75]\n"
"v_exp_f32 v199, v199\n"
"v_mfma_f32_16x16x32_bf16 v[76:79], v[212:215], a[184:187], v[76:79]\n"
"v_cvt_pk_bf16_f32 v219, v198, v199\n"
"v_mfma_f32_16x16x32_bf16 v[76:79], v[208:211], a[188:191], v[76:79]\n"
"v_exp_f32 v200, v200\n"
"v_mfma_f32_16x16x32_bf16 v[80:83], v[208:211], a[192:195], v[80:83]\n"
"s_add_u32 s27, s27, 1\n"
"v_mov_b32 v255, s27\n"
"v_mfma_f32_16x16x32_bf16 v[80:83], v[212:215], a[196:199], v[80:83]\n"
"v_exp_f32 v201, v201\n"
"v_mfma_f32_16x16x32_bf16 v[84:87], v[212:215], a[200:203], v[84:87]\n"
"v_mfma_f32_16x16x32_bf16 v[84:87], v[208:211], a[204:207], v[84:87]\n"
"v_exp_f32 v202, v202\n"
"v_mfma_f32_16x16x32_bf16 v[88:91], v[208:211], a[208:211], v[88:91]\n"
"v_mfma_f32_16x16x32_bf16 v[88:91], v[212:215], a[212:215], v[88:91]\n"
"v_exp_f32 v203, v203\n"
"v_mfma_f32_16x16x32_bf16 v[92:95], v[212:215], a[216:219], v[92:95]\n"
"s_waitcnt vmcnt(6)\n"
"ds_write_b32 v253, v255\n"
"v_mfma_f32_16x16x32_bf16 v[92:95], v[208:211], a[220:223], v[92:95]\n"
"v_mfma_f32_16x16x32_bf16 v[192:195], a[96:99], a[64:67], v[236:239]\n"
"v_exp_f32 v204, v204\n"
"v_mfma_f32_16x16x32_bf16 v[192:195], a[100:103], a[68:71], v[192:195]\n"
"v_cvt_pk_bf16_f32 v212, v200, v201\n"
"v_cvt_pk_bf16_f32 v213, v202, v203\n"
"v_mfma_f32_16x16x32_bf16 v[192:195], a[104:107], a[72:75], v[192:195]\n"
"v_exp_f32 v205, v205\n"
"v_mfma_f32_16x16x32_bf16 v[192:195], a[108:111], a[76:79], v[192:195]\n"
"v_cvt_pk_bf16_f32 v214, v204, v205\n"
"v_mfma_f32_16x16x32_bf16 v[196:199], a[112:115], a[76:79], v[236:239]\n"
"v_exp_f32 v206, v206\n"
"v_mfma_f32_16x16x32_bf16 v[196:199], a[116:119], a[72:75], v[196:199]\n"
"buffer_load_dwordx4 v250, s[40:43], s24 offen offset:2048 nt lds\n"
"v_mfma_f32_16x16x32_bf16 v[196:199], a[120:123], a[68:71], v[196:199]\n"
"v_exp_f32 v207, v207\n"
"v_mfma_f32_16x16x32_bf16 v[196:199], a[124:127], a[64:67], v[196:199]\n"
"v_cvt_pk_bf16_f32 v215, v206, v207\n"
"v_mfma_f32_16x16x32_bf16 v[200:203], a[128:131], a[64:67], v[236:239]\n"
"v_exp_f32 v192, v192\n"
"v_mfma_f32_16x16x32_bf16 v[200:203], a[132:135], a[68:71], v[200:203]\n"
"v_mfma_f32_16x16x32_bf16 v[200:203], a[136:139], a[72:75], v[200:203]\n"
"v_exp_f32 v193, v193\n"
"v_mfma_f32_16x16x32_bf16 v[200:203], a[140:143], a[76:79], v[200:203]\n"
"v_cvt_pk_bf16_f32 v208, v192, v193\n"
"v_mfma_f32_16x16x32_bf16 v[204:207], a[144:147], a[76:79], v[236:239]\n"
"v_exp_f32 v194, v194\n"
"v_mfma_f32_16x16x32_bf16 v[204:207], a[148:151], a[72:75], v[204:207]\n"
"v_mfma_f32_16x16x32_bf16 v[204:207], a[152:155], a[68:71], v[204:207]\n"
"v_exp_f32 v195, v195\n"
"v_mfma_f32_16x16x32_bf16 v[204:207], a[156:159], a[64:67], v[204:207]\n"
"v_cvt_pk_bf16_f32 v209, v194, v195\n"
"v_smfmac_f32_16x16x64_bf16 a[236:239], a[248:251], v[212:219], v252\n"
"v_exp_f32 v196, v196\n"
"v_mfma_f32_16x16x32_bf16 v[96:99], v[216:219], a[160:163], v[96:99]\n"
"v_mfma_f32_16x16x32_bf16 v[96:99], v[212:215], a[164:167], v[96:99]\n"
"v_exp_f32 v197, v197\n"
"v_mfma_f32_16x16x32_bf16 v[100:103], v[212:215], a[168:171], v[100:103]\n"
"v_cvt_pk_bf16_f32 v210, v196, v197\n"
"v_mfma_f32_16x16x32_bf16 v[100:103], v[216:219], a[172:175], v[100:103]\n"
"v_exp_f32 v198, v198\n"
"v_mfma_f32_16x16x32_bf16 v[104:107], v[216:219], a[176:179], v[104:107]\n"
"ds_read_b32 v255, v254\n"
"v_mfma_f32_16x16x32_bf16 v[104:107], v[212:215], a[180:183], v[104:107]\n"
"v_exp_f32 v199, v199\n"
"v_mfma_f32_16x16x32_bf16 v[108:111], v[212:215], a[184:187], v[108:111]\n"
"v_cvt_pk_bf16_f32 v211, v198, v199\n"
"v_mfma_f32_16x16x32_bf16 v[108:111], v[216:219], a[188:191], v[108:111]\n"
"v_exp_f32 v200, v200\n"
"v_mfma_f32_16x16x32_bf16 v[112:115], v[216:219], a[192:195], v[112:115]\n"
"buffer_load_dwordx4 v251, s[40:43], s24 offen offset:3072 nt lds\n"
"v_mfma_f32_16x16x32_bf16 v[112:115], v[212:215], a[196:199], v[112:115]\n"
"v_exp_f32 v201, v201\n"
"v_mfma_f32_16x16x32_bf16 v[116:119], v[212:215], a[200:203], v[116:119]\n"
"v_mfma_f32_16x16x32_bf16 v[116:119], v[216:219], a[204:207], v[116:119]\n"
"v_exp_f32 v202, v202\n"
"v_mfma_f32_16x16x32_bf16 v[120:123], v[216:219], a[208:211], v[120:123]\n"
"v_mfma_f32_16x16x32_bf16 v[120:123], v[212:215], a[212:215], v[120:123]\n"
"v_exp_f32 v203, v203\n"
"v_mfma_f32_16x16x32_bf16 v[124:127], v[212:215], a[216:219], v[124:127]\n"
"s_waitcnt lgkmcnt(0)\n"
"v_cmp_gt_u32 vcc, s27, v255\n"
"v_mfma_f32_16x16x32_bf16 v[124:127], v[216:219], a[220:223], v[124:127]\n"
"s_cbranch_vccnz L_late6_13_%=\n"
"L_back6_13_%=:\n"
"v_mfma_f32_16x16x32_bf16 v[192:195], a[96:99], a[80:83], v[240:243]\n"
"v_exp_f32 v204, v204\n"
"v_mfma_f32_16x16x32_bf16 v[192:195], a[100:103], a[84:87], v[192:195]\n"
"v_cvt_pk_bf16_f32 v212, v200, v201\n"
"v_cvt_pk_bf16_f32 v213, v202, v203\n"
"v_mfma_f32_16x16x32_bf16 v[192:195], a[104:107], a[88:91], v[192:195]\n"
"v_exp_f32 v205, v205\n"
"v_mfma_f32_16x16x32_bf16 v[192:195], a[108:111], a[92:95], v[192:195]\n"
"ds_read_b128 a[96:99], v244 offset:32768\n"
"v_cvt_pk_bf16_f32 v214, v204, v205\n"
"v_mfma_f32_16x16x32_bf16 v[196:199], a[112:115], a[92:95], v[240:243]\n"
"v_exp_f32 v206, v206\n"
"v_mfma_f32_16x16x32_bf16 v[196:199], a[116:119], a[88:91], v[196:199]\n"
"ds_read_b128 a[100:103], v245 offset:32768\n"
"ds_read_b128 a[104:107], v244 offset:33792\n"
"v_mfma_f32_16x16x32_bf16 v[196:199], a[120:123], a[84:87], v[196:199]\n"
"v_exp_f32 v207, v207\n"
"v_mfma_f32_16x16x32_bf16 v[196:199], a[124:127], a[80:83], v[196:199]\n"
"ds_read_b128 a[108:111], v245 offset:33792\n"
"v_cvt_pk_bf16_f32 v215, v206, v207\n"
"v_mfma_f32_16x16x32_bf16 v[200:203], a[128:131], a[80:83], v[240:243]\n"
"v_exp_f32 v192, v192\n"
"v_mfma_f32_16x16x32_bf16 v[200:203], a[132:135], a[84:87], v[200:203]\n"
"ds_read_b128 a[112:115], v245 offset:41984\n"
"ds_read_b128 a[116:119], v244 offset:41984\n"
"v_mfma_f32_16x16x32_bf16 v[200:203], a[136:139], a[88:91], v[200:203]\n"
"v_exp_f32 v193, v193\n"
"v_mfma_f32_16x16x32_bf16 v[200:203], a[140:143], a[92:95], v[200:203]\n"
"ds_read_b128 a[120:123], v245 offset:40960\n"
"v_cvt_pk_bf16_f32 v216, v192, v193\n"
"v_mfma_f32_16x16x32_bf16 v[204:207], a[144:147], a[92:95], v[240:243]\n"
"v_exp_f32 v194, v194\n"
"v_mfma_f32_16x16x32_bf16 v[204:207], a[148:151], a[88:91], v[204:207]\n"
"ds_read_b128 a[124:127], v244 offset:40960\n"
"v_mfma_f32_16x16x32_bf16 v[204:207], a[152:155], a[84:87], v[204:207]\n"
"v_exp_f32 v195, v195\n"
"v_mfma_f32_16x16x32_bf16 v[204:207], a[156:159], a[80:83], v[204:207]\n"
"ds_read_b128 a[128:131], v244 offset:49152\n"
"v_cvt_pk_bf16_f32 v217, v194, v195\n"
"v_smfmac_f32_16x16x64_bf16 a[240:243], a[248:251], v[208:215], v252\n"
"v_exp_f32 v196, v196\n"
"v_mfma_f32_16x16x32_bf16 v[128:131], v[208:211], a[160:163], v[128:131]\n"
"ds_read_b128 a[132:135], v245 offset:49152\n"
"v_mfma_f32_16x16x32_bf16 v[128:131], v[212:215], a[164:167], v[128:131]\n"
"v_exp_f32 v197, v197\n"
"v_mfma_f32_16x16x32_bf16 v[132:135], v[212:215], a[168:171], v[132:135]\n"
"ds_read_b128 a[136:139], v244 offset:50176\n"
"v_cvt_pk_bf16_f32 v218, v196, v197\n"
"v_mfma_f32_16x16x32_bf16 v[132:135], v[208:211], a[172:175], v[132:135]\n"
"v_exp_f32 v198, v198\n"
"v_mfma_f32_16x16x32_bf16 v[136:139], v[208:211], a[176:179], v[136:139]\n"
"ds_read_b128 a[140:143], v245 offset:50176\n"
"ds_read_b128 a[144:147], v245 offset:58368\n"
"v_mfma_f32_16x16x32_bf16 v[136:139], v[212:215], a[180:183], v[136:139]\n"
"v_exp_f32 v199, v199\n"
"v_mfma_f32_16x16x32_bf16 v[140:143], v[212:215], a[184:187], v[140:143]\n"
"v_cvt_pk_bf16_f32 v219, v198, v199\n"
"s_add_u32 m0, s25, 32768\n"
"v_mfma_f32_16x16x32_bf16 v[140:143], v[208:211], a[188:191], v[140:143]\n"
"v_exp_f32 v200, v200\n"
"v_mfma_f32_16x16x32_bf16 v[144:147], v[208:211], a[192:195], v[144:147]\n"
"ds_read_b128 a[148:151], v244 offset:58368\n"
"s_add_u32 s24, s24, 8192\n"
"v_mfma_f32_16x16x32_bf16 v[144:147], v[212:215], a[196:199], v[144:147]\n"
"v_exp_f32 v201, v201\n"
"v_mfma_f32_16x16x32_bf16 v[148:151], v[212:215], a[200:203], v[148:151]\n"
"buffer_load_dwordx4 v248, s[36:39], s24 offen nt lds\n"
"v_mfma_f32_16x16x32_bf16 v[148:151], v[208:211], a[204:207], v[148:151]\n"
"v_exp_f32 v202, v202\n"
"v_mfma_f32_16x16x32_bf16 v[152:155], v[208:211], a[208:211], v[152:155]\n"
"ds_read_b128 a[152:155], v245 offset:57344\n"
"ds_read_b128 a[156:159], v244 offset:57344\n"
"v_mfma_f32_16x16x32_bf16 v[152:155], v[212:215], a[212:215], v[152:155]\n"
"v_exp_f32 v203, v203\n"
"v_mfma_f32_16x16x32_bf16 v[156:159], v[212:215], a[216:219], v[156:159]\n"
"v_mfma_f32_16x16x32_bf16 v[156:159], v[208:211], a[220:223], v[156:159]\n"
"s_waitcnt lgkmcnt(8)\n"
"v_mfma_f32_16x16x32_bf16 v[192:195], a[96:99], a[0:3], v[220:223]\n"
"v_exp_f32 v204, v204\n"
"v_mfma_f32_16x16x32_bf16 v[192:195], a[100:103], a[4:7], v[192:195]\n"
"v_cvt_pk_bf16_f32 v212, v200, v201\n"
"v_cvt_pk_bf16_f32 v213, v202, v203\n"
"v_mfma_f32_16x16x32_bf16 v[192:195], a[104:107], a[8:11], v[192:195]\n"
"v_exp_f32 v205, v205\n"
"v_mfma_f32_16x16x32_bf16 v[192:195], a[108:111], a[12:15], v[192:195]\n"
"v_cvt_pk_bf16_f32 v214, v204, v205\n"
"v_mfma_f32_16x16x32_bf16 v[196:199], a[112:115], a[12:15], v[220:223]\n"
"v_exp_f32 v206, v206\n"
"v_mfma_f32_16x16x32_bf16 v[196:199], a[116:119], a[8:11], v[196:199]\n"
"buffer_load_dwordx4 v249, s[36:39], s24 offen offset:1024 nt lds\n"
"v_mfma_f32_16x16x32_bf16 v[196:199], a[120:123], a[4:7], v[196:199]\n"
"v_exp_f32 v207, v207\n"
"v_mfma_f32_16x16x32_bf16 v[196:199], a[124:127], a[0:3], v[196:199]\n"
"v_cvt_pk_bf16_f32 v215, v206, v207\n"
"s_waitcnt lgkmcnt(0)\n"
"v_mfma_f32_16x16x32_bf16 v[200:203], a[128:131], a[0:3], v[220:223]\n"
"v_exp_f32 v192, v192\n"
"v_mfma_f32_16x16x32_bf16 v[200:203], a[132:135], a[4:7], v[200:203]\n"
"v_mfma_f32_16x16x32_bf16 v[200:203], a[136:139], a[8:11], v[200:203]\n"
"v_exp_f32 v193, v193\n"
"v_mfma_f32_16x16x32_bf16 v[200:203], a[140:143], a[12:15], v[200:203]\n"
"v_cvt_pk_bf16_f32 v208, v192, v193\n"
"v_mfma_f32_16x16x32_bf16 v[204:207], a[144:147], a[12:15], v[220:223]\n"
"v_exp_f32 v194, v194\n"
"v_mfma_f32_16x16x32_bf16 v[204:207], a[148:151], a[8:11], v[204:207]\n"
"v_mfma_f32_16x16x32_bf16 v[204:207], a[152:155], a[4:7], v[204:207]\n"
"v_exp_f32 v195, v195\n"
"v_mfma_f32_16x16x32_bf16 v[204:207], a[156:159], a[0:3], v[204:207]\n"
"v_cvt_pk_bf16_f32 v209, v194, v195\n"
"v_smfmac_f32_16x16x64_bf16 a[244:247], a[248:251], v[212:219], v252\n"
"v_exp_f32 v196, v196\n"
"v_mfma_f32_16x16x32_bf16 v[160:163], v[216:219], a[160:163], v[160:163]\n"
"ds_read_b64_tr_b16 a[160:161], v246 offset:34816\n"
"ds_read_b64_tr_b16 a[162:163], v246 offset:43008\n"
"v_mfma_f32_16x16x32_bf16 v[160:163], v[212:215], a[164:167], v[160:163]\n"
"v_exp_f32 v197, v197\n"
"v_mfma_f32_16x16x32_bf16 v[164:167], v[212:215], a[168:171], v[164:167]\n"
"ds_read_b64_tr_b16 a[164:165], v246 offset:51200\n"
"v_cvt_pk_bf16_f32 v210, v196, v197\n"
"v_mfma_f32_16x16x32_bf16 v[164:167], v[216:219], a[172:175], v[164:167]\n"
"v_exp_f32 v198, v198\n"
"v_mfma_f32_16x16x32_bf16 v[168:171], v[216:219], a[176:179], v[168:171]\n"
"ds_read_b64_tr_b16 a[166:167], v246 offset:59392\n"
"ds_read_b64_tr_b16 a[168:169], v247 offset:51200\n"
"v_mfma_f32_16x16x32_bf16 v[168:171], v[212:215], a[180:183], v[168:171]\n"
"v_exp_f32 v199, v199\n"
"v_mfma_f32_16x16x32_bf16 v[172:175], v[212:215], a[184:187], v[172:175]\n"
"ds_read_b64_tr_b16 a[170:171], v247 offset:59392\n"
"v_cvt_pk_bf16_f32 v211, v198, v199\n"
"v_mfma_f32_16x16x32_bf16 v[172:175], v[216:219], a[188:191], v[172:175]\n"
"v_exp_f32 v200, v200\n"
"v_mfma_f32_16x16x32_bf16 v[176:179], v[216:219], a[192:195], v[176:179]\n"
"ds_read_b64_tr_b16 a[172:173], v247 offset:34816\n"
"ds_read_b64_tr_b16 a[174:175], v247 offset:43008\n"
"v_mfma_f32_16x16x32_bf16 v[176:179], v[212:215], a[196:199], v[176:179]\n"
"v_exp_f32 v201, v201\n"
"v_mfma_f32_16x16x32_bf16 v[180:183], v[212:215], a[200:203], v[180:183]\n"
"ds_read_b64_tr_b16 a[176:177], v246 offset:35328\n"
"ds_read_b64_tr_b16 a[178:179], v246 offset:43520\n"
"v_mfma_f32_16x16x32_bf16 v[180:183], v[216:219], a[204:207], v[180:183]\n"
"v_exp_f32 v202, v202\n"
"v_mfma_f32_16x16x32_bf16 v[184:187], v[216:219], a[208:211], v[184:187]\n"
"ds_read_b64_tr_b16 a[180:181], v246 offset:51712\n"
"ds_read_b64_tr_b16 a[182:183], v246 offset:59904\n"
"v_mfma_f32_16x16x32_bf16 v[184:187], v[212:215], a[212:215], v[184:187]\n"
"v_exp_f32 v203, v203\n"
"v_mfma_f32_16x16x32_bf16 v[188:191], v[212:215], a[216:219], v[188:191]\n"
"ds_read_b64_tr_b16 a[184:185], v247 offset:51712\n"
"ds_read_b64_tr_b16 a[186:187], v247 offset:59904\n"
"v_mfma_f32_16x16x32_bf16 v[188:191], v[216:219], a[220:223], v[188:191]\n"
"ds_read_b64_tr_b16 a[188:189], v247 offset:35328\n"
"ds_read_b64_tr_b16 a[190:191], v247 offset:43520\n"
"v_mfma_f32_16x16x32_bf16 v[192:195], a[96:99], a[16:19], v[224:227]\n"
"v_exp_f32 v204, v204\n"
"v_mfma_f32_16x16x32_bf16 v[192:195], a[100:103], a[20:23], v[192:195]\n"
"v_cvt_pk_bf16_f32 v212, v200, v201\n"
"v_cvt_pk_bf16_f32 v213, v202, v203\n"
"v_mfma_f32_16x16x32_bf16 v[192:195], a[104:107], a[24:27], v[192:195]\n"
"v_exp_f32 v205, v205\n"
"v_mfma_f32_16x16x32_bf16 v[192:195], a[108:111], a[28:31], v[192:195]\n"
"ds_read_b64_tr_b16 a[192:193], v246 offset:35840\n"
"v_cvt_pk_bf16_f32 v214, v204, v205\n"
"v_mfma_f32_16x16x32_bf16 v[196:199], a[112:115], a[28:31], v[224:227]\n"
"v_exp_f32 v206, v206\n"
"v_mfma_f32_16x16x32_bf16 v[196:199], a[116:119], a[24:27], v[196:199]\n"
"ds_read_b64_tr_b16 a[194:195], v246 offset:44032\n"
"ds_read_b64_tr_b16 a[196:197], v246 offset:52224\n"
"v_mfma_f32_16x16x32_bf16 v[196:199], a[120:123], a[20:23], v[196:199]\n"
"v_exp_f32 v207, v207\n"
"v_mfma_f32_16x16x32_bf16 v[196:199], a[124:127], a[16:19], v[196:199]\n"
"ds_read_b64_tr_b16 a[198:199], v246 offset:60416\n"
"v_cvt_pk_bf16_f32 v215, v206, v207\n"
"v_mfma_f32_16x16x32_bf16 v[200:203], a[128:131], a[16:19], v[224:227]\n"
"v_exp_f32 v192, v192\n"
"v_mfma_f32_16x16x32_bf16 v[200:203], a[132:135], a[20:23], v[200:203]\n"
"ds_read_b64_tr_b16 a[200:201], v247 offset:52224\n"
"ds_read_b64_tr_b16 a[202:203], v247 offset:60416\n"
"v_mfma_f32_16x16x32_bf16 v[200:203], a[136:139], a[24:27], v[200:203]\n"
"v_exp_f32 v193, v193\n"
"v_mfma_f32_16x16x32_bf16 v[200:203], a[140:143], a[28:31], v[200:203]\n"
"ds_read_b64_tr_b16 a[204:205], v247 offset:35840\n"
"v_cvt_pk_bf16_f32 v216, v192, v193\n"
"v_mfma_f32_16x16x32_bf16 v[204:207], a[144:147], a[28:31], v[224:227]\n"
"v_exp_f32 v194, v194\n"
"v_mfma_f32_16x16x32_bf16 v[204:207], a[148:151], a[24:27], v[204:207]\n"
"ds_read_b64_tr_b16 a[206:207], v247 offset:44032\n"
"ds_read_b64_tr_b16 a[208:209], v246 offset:36352\n"
"v_mfma_f32_16x16x32_bf16 v[204:207], a[152:155], a[20:23], v[204:207]\n"
"v_exp_f32 v195, v195\n"
"v_mfma_f32_16x16x32_bf16 v[204:207], a[156:159], a[16:19], v[204:207]\n"
"v_cvt_pk_bf16_f32 v217, v194, v195\n"
"s_waitcnt lgkmcnt(15)\n"
"v_smfmac_f32_16x16x64_bf16 a[224:227], a[248:251], v[208:215], v252\n"
"v_exp_f32 v196, v196\n"
"v_mfma_f32_16x16x32_bf16 v[0:3], v[208:211], a[160:163], v[0:3]\n"
"ds_read_b64_tr_b16 a[210:211], v246 offset:44544\n"
"ds_read_b64_tr_b16 a[212:213], v246 offset:52736\n"
"v_mfma_f32_16x16x32_bf16 v[0:3], v[212:215], a[164:167], v[0:3]\n"
"v_exp_f32 v197, v197\n"
"v_mfma_f32_16x16x32_bf16 v[4:7], v[212:215], a[168:171], v[4:7]\n"
"v_cvt_pk_bf16_f32 v218, v196, v197\n"
"s_waitcnt lgkmcnt(11)\n"
"v_mfma_f32_16x16x32_bf16 v[4:7], v[208:211], a[172:175], v[4:7]\n"
"v_exp_f32 v198, v198\n"
"v_mfma_f32_16x16x32_bf16 v[8:11], v[208:211], a[176:179], v[8:11]\n"
"ds_read_b64_tr_b16 a[214:215], v246 offset:60928\n"
"ds_read_b64_tr_b16 a[216:217], v247 offset:52736\n"
"ds_read_b64_tr_b16 a[218:219], v247 offset:60928\n"
"ds_read_b64_tr_b16 a[220:221], v247 offset:36352\n"
"ds_read_b64_tr_b16 a[222:223], v247 offset:44544\n"
"v_mfma_f32_16x16x32_bf16 v[8:11], v[212:215], a[180:183], v[8:11]\n"
"v_exp_f32 v199, v199\n"
"v_mfma_f32_16x16x32_bf16 v[12:15], v[212:215], a[184:187], v[12:15]\n"
"v_cvt_pk_bf16_f32 v219, v198, v199\n"
"s_waitcnt lgkmcnt(8)\n"
"v_mfma_f32_16x16x32_bf16 v[12:15], v[208:211], a[188:191], v[12:15]\n"
"v_exp_f32 v200, v200\n"
"v_mfma_f32_16x16x32_bf16 v[16:19], v[208:211], a[192:195], v[16:19]\n"
"buffer_load_dwordx4 v250, s[40:43], s24 offen offset:2048 nt lds\n"
"v_mfma_f32_16x16x32_bf16 v[16:19], v[212:215], a[196:199], v[16:19]\n"
"v_exp_f32 v201, v201\n"
"v_mfma_f32_16x16x32_bf16 v[20:23], v[212:215], a[200:203], v[20:23]\n"
"s_waitcnt lgkmcnt(0)\n"
"v_mfma_f32_16x16x32_bf16 v[20:23], v[208:211], a[204:207], v[20:23]\n"
"v_exp_f32 v202, v202\n"
"v_mfma_f32_16x16x32_bf16 v[24:27], v[208:211], a[208:211], v[24:27]\n"
"buffer_load_dwordx4 v251, s[40:43], s24 offen offset:3072 nt lds\n"
"v_mfma_f32_16x16x32_bf16 v[24:27], v[212:215], a[212:215], v[24:27]\n"
"v_exp_f32 v203, v203\n"
"v_mfma_f32_16x16x32_bf16 v[28:31], v[212:215], a[216:219], v[28:31]\n"
"v_mfma_f32_16x16x32_bf16 v[28:31], v[208:211], a[220:223], v[28:31]\n"
"v_mfma_f32_16x16x32_bf16 v[192:195], a[96:99], a[32:35], v[228:231]\n"
"v_exp_f32 v204, v204\n"
"v_mfma_f32_16x16x32_bf16 v[192:195], a[100:103], a[36:39], v[192:195]\n"
"v_cvt_pk_bf16_f32 v212, v200, v201\n"
"v_cvt_pk_bf16_f32 v213, v202, v203\n"
"v_mfma_f32_16x16x32_bf16 v[192:195], a[104:107], a[40:43], v[192:195]\n"
"v_exp_f32 v205, v205\n"
"v_mfma_f32_16x16x32_bf16 v[192:195], a[108:111], a[44:47], v[192:195]\n"
"v_cvt_pk_bf16_f32 v214, v204, v205\n"
"v_mfma_f32_16x16x32_bf16 v[196:199], a[112:115], a[44:47], v[228:231]\n"
"v_exp_f32 v206, v206\n"
"v_mfma_f32_16x16x32_bf16 v[196:199], a[116:119], a[40:43], v[196:199]\n"
"v_mfma_f32_16x16x32_bf16 v[196:199], a[120:123], a[36:39], v[196:199]\n"
"v_exp_f32 v207, v207\n"
"v_mfma_f32_16x16x32_bf16 v[196:199], a[124:127], a[32:35], v[196:199]\n"
"v_cvt_pk_bf16_f32 v215, v206, v207\n"
"v_mfma_f32_16x16x32_bf16 v[200:203], a[128:131], a[32:35], v[228:231]\n"
"v_exp_f32 v192, v192\n"
"v_mfma_f32_16x16x32_bf16 v[200:203], a[132:135], a[36:39], v[200:203]\n"
"v_mfma_f32_16x16x32_bf16 v[200:203], a[136:139], a[40:43], v[200:203]\n"
"v_exp_f32 v193, v193\n"
"v_mfma_f32_16x16x32_bf16 v[200:203], a[140:143], a[44:47], v[200:203]\n"
"v_cvt_pk_bf16_f32 v208, v192, v193\n"
"v_mfma_f32_16x16x32_bf16 v[204:207], a[144:147], a[44:47], v[228:231]\n"
"v_exp_f32 v194, v194\n"
"v_mfma_f32_16x16x32_bf16 v[204:207], a[148:151], a[40:43], v[204:207]\n"
"s_add_u32 m0, s25, 49152\n"
"s_add_u32 s24, s24, 8192\n"
"v_mfma_f32_16x16x32_bf16 v[204:207], a[152:155], a[36:39], v[204:207]\n"
"v_exp_f32 v195, v195\n"
"v_mfma_f32_16x16x32_bf16 v[204:207], a[156:159], a[32:35], v[204:207]\n"
"v_cvt_pk_bf16_f32 v209, v194, v195\n"
"v_smfmac_f32_16x16x64_bf16 a[228:231], a[248:251], v[212:219], v252\n"
"v_exp_f32 v196, v196\n"
"v_mfma_f32_16x16x32_bf16 v[32:35], v[216:219], a[160:163], v[32:35]\n"
"buffer_load_dwordx4 v248, s[36:39], s24 offen nt lds\n"
"v_mfma_f32_16x16x32_bf16 v[32:35], v[212:215], a[164:167], v[32:35]\n"
"v_exp_f32 v197, v197\n"
"v_mfma_f32_16x16x32_bf16 v[36:39], v[212:215], a[168:171], v[36:39]\n"
"v_cvt_pk_bf16_f32 v210, v196, v197\n"
"v_mfma_f32_16x16x32_bf16 v[36:39], v[216:219], a[172:175], v[36:39]\n"
"v_exp_f32 v198, v198\n"
"v_mfma_f32_16x16x32_bf16 v[40:43], v[216:219], a[176:179], v[40:43]\n"
"v_mfma_f32_16x16x32_bf16 v[40:43], v[212:215], a[180:183], v[40:43]\n"
"v_exp_f32 v199, v199\n"
"v_mfma_f32_16x16x32_bf16 v[44:47], v[212:215], a[184:187], v[44:47]\n"
"v_cvt_pk_bf16_f32 v211, v198, v199\n"
"v_mfma_f32_16x16x32_bf16 v[44:47], v[216:219], a[188:191], v[44:47]\n"
"v_exp_f32 v200, v200\n"
"v_mfma_f32_16x16x32_bf16 v[48:51], v[216:219], a[192:195], v[48:51]\n"
"v_mfma_f32_16x16x32_bf16 v[48:51], v[212:215], a[196:199], v[48:51]\n"
"v_exp_f32 v201, v201\n"
"v_mfma_f32_16x16x32_bf16 v[52:55], v[212:215], a[200:203], v[52:55]\n"
"v_mfma_f32_16x16x32_bf16 v[52:55], v[216:219], a[204:207], v[52:55]\n"
"v_exp_f32 v202, v202\n"
"v_mfma_f32_16x16x32_bf16 v[56:59], v[216:219], a[208:211], v[56:59]\n"
"v_mfma_f32_16x16x32_bf16 v[56:59], v[212:215], a[212:215], v[56:59]\n"
"v_exp_f32 v203, v203\n"
"v_mfma_f32_16x16x32_bf16 v[60:63], v[212:215], a[216:219], v[60:63]\n"
"v_mfma_f32_16x16x32_bf16 v[60:63], v[216:219], a[220:223], v[60:63]\n"
"v_mfma_f32_16x16x32_bf16 v[192:195], a[96:99], a[48:51], v[232:235]\n"
"v_exp_f32 v204, v204\n"
"v_mfma_f32_16x16x32_bf16 v[192:195], a[100:103], a[52:55], v[192:195]\n"
"v_cvt_pk_bf16_f32 v212, v200, v201\n"
"v_cvt_pk_bf16_f32 v213, v202, v203\n"
"v_mfma_f32_16x16x32_bf16 v[192:195], a[104:107], a[56:59], v[192:195]\n"
"v_exp_f32 v205, v205\n"
"v_mfma_f32_16x16x32_bf16 v[192:195], a[108:111], a[60:63], v[192:195]\n"
"v_cvt_pk_bf16_f32 v214, v204, v205\n"
"v_mfma_f32_16x16x32_bf16 v[196:199], a[112:115], a[60:63], v[232:235]\n"
"v_exp_f32 v206, v206\n"
"v_mfma_f32_16x16x32_bf16 v[196:199], a[116:119], a[56:59], v[196:199]\n"
"buffer_load_dwordx4 v249, s[36:39], s24 offen offset:1024 nt lds\n"
"v_mfma_f32_16x16x32_bf16 v[196:199], a[120:123], a[52:55], v[196:199]\n"
"v_exp_f32 v207, v207\n"
"v_mfma_f32_16x16x32_bf16 v[196:199], a[124:127], a[48:51], v[196:199]\n"
"v_cvt_pk_bf16_f32 v215, v206, v207\n"
"v_mfma_f32_16x16x32_bf16 v[200:203], a[128:131], a[48:51], v[232:235]\n"
"v_exp_f32 v192, v192\n"
"v_mfma_f32_16x16x32_bf16 v[200:203], a[132:135], a[52:55], v[200:203]\n"
"v_mfma_f32_16x16x32_bf16 v[200:203], a[136:139], a[56:59], v[200:203]\n"
"v_exp_f32 v193, v193\n"
"v_mfma_f32_16x16x32_bf16 v[200:203], a[140:143], a[60:63], v[200:203]\n"
"v_cvt_pk_bf16_f32 v216, v192, v193\n"
"v_mfma_f32_16x16x32_bf16 v[204:207], a[144:147], a[60:63], v[232:235]\n"
"v_exp_f32 v194, v194\n"
"v_mfma_f32_16x16x32_bf16 v[204:207], a[148:151], a[56:59], v[204:207]\n"
"v_mfma_f32_16x16x32_bf16 v[204:207], a[152:155], a[52:55], v[204:207]\n"
"v_exp_f32 v195, v195\n"
"v_mfma_f32_16x16x32_bf16 v[204:207], a[156:159], a[48:51], v[204:207]\n"
"v_cvt_pk_bf16_f32 v217, v194, v195\n"
"v_smfmac_f32_16x16x64_bf16 a[232:235], a[248:251], v[208:215], v252\n"
"v_exp_f32 v196, v196\n"
"v_mfma_f32_16x16x32_bf16 v[64:67], v[208:211], a[160:163], v[64:67]\n"
"v_mfma_f32_16x16x32_bf16 v[64:67], v[212:215], a[164:167], v[64:67]\n"
"v_exp_f32 v197, v197\n"
"v_mfma_f32_16x16x32_bf16 v[68:71], v[212:215], a[168:171], v[68:71]\n"
"v_cvt_pk_bf16_f32 v218, v196, v197\n"
"v_mfma_f32_16x16x32_bf16 v[68:71], v[208:211], a[172:175], v[68:71]\n"
"v_exp_f32 v198, v198\n"
"v_mfma_f32_16x16x32_bf16 v[72:75], v[208:211], a[176:179], v[72:75]\n"
"v_mfma_f32_16x16x32_bf16 v[72:75], v[212:215], a[180:183], v[72:75]\n"
"v_exp_f32 v199, v199\n"
"v_mfma_f32_16x16x32_bf16 v[76:79], v[212:215], a[184:187], v[76:79]\n"
"v_cvt_pk_bf16_f32 v219, v198, v199\n"
"v_mfma_f32_16x16x32_bf16 v[76:79], v[208:211], a[188:191], v[76:79]\n"
"v_exp_f32 v200, v200\n"
"v_mfma_f32_16x16x32_bf16 v[80:83], v[208:211], a[192:195], v[80:83]\n"
"s_add_u32 s27, s27, 1\n"
"v_mov_b32 v255, s27\n"
"v_mfma_f32_16x16x32_bf16 v[80:83], v[212:215], a[196:199], v[80:83]\n"
"v_exp_f32 v201, v201\n"
"v_mfma_f32_16x16x32_bf16 v[84:87], v[212:215], a[200:203], v[84:87]\n"
"v_mfma_f32_16x16x32_bf16 v[84:87], v[208:211], a[204:207], v[84:87]\n"
"v_exp_f32 v202, v202\n"
"v_mfma_f32_16x16x32_bf16 v[88:91], v[208:211], a[208:211], v[88:91]\n"
"v_mfma_f32_16x16x32_bf16 v[88:91], v[212:215], a[212:215], v[88:91]\n"
"v_exp_f32 v203, v203\n"
"v_mfma_f32_16x16x32_bf16 v[92:95], v[212:215], a[216:219], v[92:95]\n"
"s_waitcnt vmcnt(6)\n"
"ds_write_b32 v253, v255\n"
"v_mfma_f32_16x16x32_bf16 v[92:95], v[208:211], a[220:223], v[92:95]\n"
"v_mfma_f32_16x16x32_bf16 v[192:195], a[96:99], a[64:67], v[236:239]\n"
"v_exp_f32 v204, v204\n"
"v_mfma_f32_16x16x32_bf16 v[192:195], a[100:103], a[68:71], v[192:195]\n"
"v_cvt_pk_bf16_f32 v212, v200, v201\n"
"v_cvt_pk_bf16_f32 v213, v202, v203\n"
"v_mfma_f32_16x16x32_bf16 v[192:195], a[104:107], a[72:75], v[192:195]\n"
"v_exp_f32 v205, v205\n"
"v_mfma_f32_16x16x32_bf16 v[192:195], a[108:111], a[76:79], v[192:195]\n"
"v_cvt_pk_bf16_f32 v214, v204, v205\n"
"v_mfma_f32_16x16x32_bf16 v[196:199], a[112:115], a[76:79], v[236:239]\n"
"v_exp_f32 v206, v206\n"
"v_mfma_f32_16x16x32_bf16 v[196:199], a[116:119], a[72:75], v[196:199]\n"
"buffer_load_dwordx4 v250, s[40:43], s24 offen offset:2048 nt lds\n"
"v_mfma_f32_16x16x32_bf16 v[196:199], a[120:123], a[68:71], v[196:199]\n"
"v_exp_f32 v207, v207\n"
"v_mfma_f32_16x16x32_bf16 v[196:199], a[124:127], a[64:67], v[196:199]\n"
"v_cvt_pk_bf16_f32 v215, v206, v207\n"
"v_mfma_f32_16x16x32_bf16 v[200:203], a[128:131], a[64:67], v[236:239]\n"
"v_exp_f32 v192, v192\n"
"v_mfma_f32_16x16x32_bf16 v[200:203], a[132:135], a[68:71], v[200:203]\n"
"v_xor_b32 v244, 0x10000, v244\n"
"v_xor_b32 v245, 0x10000, v245\n"
"v_mfma_f32_16x16x32_bf16 v[200:203], a[136:139], a[72:75], v[200:203]\n"
"v_exp_f32 v193, v193\n"
"v_mfma_f32_16x16x32_bf16 v[200:203], a[140:143], a[76:79], v[200:203]\n"
"v_cvt_pk_bf16_f32 v208, v192, v193\n"
"v_mfma_f32_16x16x32_bf16 v[204:207], a[144:147], a[76:79], v[236:239]\n"
"v_exp_f32 v194, v194\n"
"v_mfma_f32_16x16x32_bf16 v[204:207], a[148:151], a[72:75], v[204:207]\n"
"v_mfma_f32_16x16x32_bf16 v[204:207], a[152:155], a[68:71], v[204:207]\n"
"v_exp_f32 v195, v195\n"
"v_mfma_f32_16x16x32_bf16 v[204:207], a[156:159], a[64:67], v[204:207]\n"
"v_cvt_pk_bf16_f32 v209, v194, v195\n"
"v_smfmac_f32_16x16x64_bf16 a[236:239], a[248:251], v[212:219], v252\n"
"v_exp_f32 v196, v196\n"
"v_mfma_f32_16x16x32_bf16 v[96:99], v[216:219], a[160:163], v[96:99]\n"
"v_mfma_f32_16x16x32_bf16 v[96:99], v[212:215], a[164:167], v[96:99]\n"
"v_exp_f32 v197, v197\n"
"v_mfma_f32_16x16x32_bf16 v[100:103], v[212:215], a[168:171], v[100:103]\n"
"v_cvt_pk_bf16_f32 v210, v196, v197\n"
"v_mfma_f32_16x16x32_bf16 v[100:103], v[216:219], a[172:175], v[100:103]\n"
"v_exp_f32 v198, v198\n"
"v_mfma_f32_16x16x32_bf16 v[104:107], v[216:219], a[176:179], v[104:107]\n"
"ds_read_b32 v255, v254\n"
"v_mfma_f32_16x16x32_bf16 v[104:107], v[212:215], a[180:183], v[104:107]\n"
"v_exp_f32 v199, v199\n"
"v_mfma_f32_16x16x32_bf16 v[108:111], v[212:215], a[184:187], v[108:111]\n"
"v_cvt_pk_bf16_f32 v211, v198, v199\n"
"v_mfma_f32_16x16x32_bf16 v[108:111], v[216:219], a[188:191], v[108:111]\n"
"v_exp_f32 v200, v200\n"
"v_mfma_f32_16x16x32_bf16 v[112:115], v[216:219], a[192:195], v[112:115]\n"
"buffer_load_dwordx4 v251, s[40:43], s24 offen offset:3072 nt lds\n"
"v_mfma_f32_16x16x32_bf16 v[112:115], v[212:215], a[196:199], v[112:115]\n"
"v_exp_f32 v201, v201\n"
"v_mfma_f32_16x16x32_bf16 v[116:119], v[212:215], a[200:203], v[116:119]\n"
"v_mfma_f32_16x16x32_bf16 v[116:119], v[216:219], a[204:207], v[116:119]\n"
"v_exp_f32 v202, v202\n"
"v_mfma_f32_16x16x32_bf16 v[120:123], v[216:219], a[208:211], v[120:123]\n"
"v_mfma_f32_16x16x32_bf16 v[120:123], v[212:215], a[212:215], v[120:123]\n"
"v_exp_f32 v203, v203\n"
"v_mfma_f32_16x16x32_bf16 v[124:127], v[212:215], a[216:219], v[124:127]\n"
"s_waitcnt lgkmcnt(0)\n"
"v_cmp_gt_u32 vcc, s27, v255\n"
"v_mfma_f32_16x16x32_bf16 v[124:127], v[216:219], a[220:223], v[124:127]\n"
"s_cbranch_vccnz L_late6_14_%=\n"
"L_back6_14_%=:\n"
"v_mfma_f32_16x16x32_bf16 v[192:195], a[96:99], a[80:83], v[240:243]\n"
"v_exp_f32 v204, v204\n"
"v_mfma_f32_16x16x32_bf16 v[192:195], a[100:103], a[84:87], v[192:195]\n"
"v_cvt_pk_bf16_f32 v212, v200, v201\n"
"v_cvt_pk_bf16_f32 v213, v202, v203\n"
"v_mfma_f32_16x16x32_bf16 v[192:195], a[104:107], a[88:91], v[192:195]\n"
"v_exp_f32 v205, v205\n"
"v_mfma_f32_16x16x32_bf16 v[192:195], a[108:111], a[92:95], v[192:195]\n"
"ds_read_b128 a[96:99], v244 offset:0\n"
"v_cvt_pk_bf16_f32 v214, v204, v205\n"
"v_mfma_f32_16x16x32_bf16 v[196:199], a[112:115], a[92:95], v[240:243]\n"
"v_exp_f32 v206, v206\n"
"v_mfma_f32_16x16x32_bf16 v[196:199], a[116:119], a[88:91], v[196:199]\n"
"ds_read_b128 a[100:103], v245 offset:0\n"
"ds_read_b128 a[104:107], v244 offset:1024\n"
"v_mfma_f32_16x16x32_bf16 v[196:199], a[120:123], a[84:87], v[196:199]\n"
"v_exp_f32 v207, v207\n"
"v_mfma_f32_16x16x32_bf16 v[196:199], a[124:127], a[80:83], v[196:199]\n"
"ds_read_b128 a[108:111], v245 offset:1024\n"
"v_cvt_pk_bf16_f32 v215, v206, v207\n"
"v_mfma_f32_16x16x32_bf16 v[200:203], a[128:131], a[80:83], v[240:243]\n"
"v_exp_f32 v192, v192\n"
"v_mfma_f32_16x16x32_bf16 v[200:203], a[132:135], a[84:87], v[200:203]\n"
"ds_read_b128 a[112:115], v245 offset:9216\n"
"ds_read_b128 a[116:119], v244 offset:9216\n"
"v_mfma_f32_16x16x32_bf16 v[200:203], a[136:139], a[88:91], v[200:203]\n"
"v_exp_f32 v193, v193\n"
"v_mfma_f32_16x16x32_bf16 v[200:203], a[140:143], a[92:95], v[200:203]\n"
"ds_read_b128 a[120:123], v245 offset:8192\n"
"v_cvt_pk_bf16_f32 v216, v192, v193\n"
"v_mfma_f32_16x16x32_bf16 v[204:207], a[144:147], a[92:95], v[240:243]\n"
"v_exp_f32 v194, v194\n"
"v_mfma_f32_16x16x32_bf16 v[204:207], a[148:151], a[88:91], v[204:207]\n"
"ds_read_b128 a[124:127], v244 offset:8192\n"
"v_mfma_f32_16x16x32_bf16 v[204:207], a[152:155], a[84:87], v[204:207]\n"
"v_exp_f32 v195, v195\n"
"v_mfma_f32_16x16x32_bf16 v[204:207], a[156:159], a[80:83], v[204:207]\n"
"ds_read_b128 a[128:131], v244 offset:16384\n"
"v_cvt_pk_bf16_f32 v217, v194, v195\n"
"v_smfmac_f32_16x16x64_bf16 a[240:243], a[248:251], v[208:215], v252\n"
"v_exp_f32 v196, v196\n"
"v_mfma_f32_16x16x32_bf16 v[128:131], v[208:211], a[160:163], v[128:131]\n"
"ds_read_b128 a[132:135], v245 offset:16384\n"
"v_mfma_f32_16x16x32_bf16 v[128:131], v[212:215], a[164:167], v[128:131]\n"
"v_exp_f32 v197, v197\n"
"v_mfma_f32_16x16x32_bf16 v[132:135], v[212:215], a[168:171], v[132:135]\n"
"ds_read_b128 a[136:139], v244 offset:17408\n"
"v_cvt_pk_bf16_f32 v218, v196, v197\n"
"v_mfma_f32_16x16x32_bf16 v[132:135], v[208:211], a[172:175], v[132:135]\n"
"v_exp_f32 v198, v198\n"
"v_mfma_f32_16x16x32_bf16 v[136:139], v[208:211], a[176:179], v[136:139]\n"
"ds_read_b128 a[140:143], v245 offset:17408\n"
"ds_read_b128 a[144:147], v245 offset:25600\n"
"v_mfma_f32_16x16x32_bf16 v[136:139], v[212:215], a[180:183], v[136:139]\n"
"v_exp_f32 v199, v199\n"
"v_mfma_f32_16x16x32_bf16 v[140:143], v[212:215], a[184:187], v[140:143]\n"
"ds_read_b128 a[148:151], v244 offset:25600\n"
"v_cvt_pk_bf16_f32 v219, v198, v199\n"
"v_mfma_f32_16x16x32_bf16 v[140:143], v[208:211], a[188:191], v[140:143]\n"
"v_exp_f32 v200, v200\n"
"v_mfma_f32_16x16x32_bf16 v[144:147], v[208:211], a[192:195], v[144:147]\n"
"ds_read_b128 a[152:155], v245 offset:24576\n"
"v_mfma_f32_16x16x32_bf16 v[144:147], v[212:215], a[196:199], v[144:147]\n"
"v_exp_f32 v201, v201\n"
"v_mfma_f32_16x16x32_bf16 v[148:151], v[212:215], a[200:203], v[148:151]\n"
"ds_read_b128 a[156:159], v244 offset:24576\n"
"v_mfma_f32_16x16x32_bf16 v[148:151], v[208:211], a[204:207], v[148:151]\n"
"v_exp_f32 v202, v202\n"
"v_mfma_f32_16x16x32_bf16 v[152:155], v[208:211], a[208:211], v[152:155]\n"
"v_mfma_f32_16x16x32_bf16 v[152:155], v[212:215], a[212:215], v[152:155]\n"
"v_exp_f32 v203, v203\n"
"v_mfma_f32_16x16x32_bf16 v[156:159], v[212:215], a[216:219], v[156:159]\n"
"v_mfma_f32_16x16x32_bf16 v[156:159], v[208:211], a[220:223], v[156:159]\n"
"s_waitcnt lgkmcnt(8)\n"
"v_mfma_f32_16x16x32_bf16 v[192:195], a[96:99], a[0:3], v[220:223]\n"
"v_exp_f32 v204, v204\n"
"v_mfma_f32_16x16x32_bf16 v[192:195], a[100:103], a[4:7], v[192:195]\n"
"v_cvt_pk_bf16_f32 v212, v200, v201\n"
"v_cvt_pk_bf16_f32 v213, v202, v203\n"
"v_mfma_f32_16x16x32_bf16 v[192:195], a[104:107], a[8:11], v[192:195]\n"
"v_exp_f32 v205, v205\n"
"v_mfma_f32_16x16x32_bf16 v[192:195], a[108:111], a[12:15], v[192:195]\n"
"v_cvt_pk_bf16_f32 v214, v204, v205\n"
"v_mfma_f32_16x16x32_bf16 v[196:199], a[112:115], a[12:15], v[220:223]\n"
"v_exp_f32 v206, v206\n"
"v_mfma_f32_16x16x32_bf16 v[196:199], a[116:119], a[8:11], v[196:199]\n"
"v_mfma_f32_16x16x32_bf16 v[196:199], a[120:123], a[4:7], v[196:199]\n"
"v_exp_f32 v207, v207\n"
"v_mfma_f32_16x16x32_bf16 v[196:199], a[124:127], a[0:3], v[196:199]\n"
"v_cvt_pk_bf16_f32 v215, v206, v207\n"
"s_waitcnt lgkmcnt(0)\n"
"v_mfma_f32_16x16x32_bf16 v[200:203], a[128:131], a[0:3], v[220:223]\n"
"v_exp_f32 v192, v192\n"
"v_mfma_f32_16x16x32_bf16 v[200:203], a[132:135], a[4:7], v[200:203]\n"
"v_xor_b32 v246, 0x10000, v246\n"
"v_xor_b32 v247, 0x10000, v247\n"
"v_mfma_f32_16x16x32_bf16 v[200:203], a[136:139], a[8:11], v[200:203]\n"
"v_exp_f32 v193, v193\n"
"v_mfma_f32_16x16x32_bf16 v[200:203], a[140:143], a[12:15], v[200:203]\n"
"v_cvt_pk_bf16_f32 v208, v192, v193\n"
"v_mfma_f32_16x16x32_bf16 v[204:207], a[144:147], a[12:15], v[220:223]\n"
"v_exp_f32 v194, v194\n"
"v_mfma_f32_16x16x32_bf16 v[204:207], a[148:151], a[8:11], v[204:207]\n"
"v_mfma_f32_16x16x32_bf16 v[204:207], a[152:155], a[4:7], v[204:207]\n"
"v_exp_f32 v195, v195\n"
"v_mfma_f32_16x16x32_bf16 v[204:207], a[156:159], a[0:3], v[204:207]\n"
"v_cvt_pk_bf16_f32 v209, v194, v195\n"
"v_smfmac_f32_16x16x64_bf16 a[244:247], a[248:251], v[212:219], v252\n"
"v_exp_f32 v196, v196\n"
"v_mfma_f32_16x16x32_bf16 v[160:163], v[216:219], a[160:163], v[160:163]\n"
"ds_read_b64_tr_b16 a[160:161], v246 offset:2048\n"
"ds_read_b64_tr_b16 a[162:163], v246 offset:10240\n"
"v_mfma_f32_16x16x32_bf16 v[160:163], v[212:215], a[164:167], v[160:163]\n"
"v_exp_f32 v197, v197\n"
"v_mfma_f32_16x16x32_bf16 v[164:167], v[212:215], a[168:171], v[164:167]\n"
"ds_read_b64_tr_b16 a[164:165], v246 offset:18432\n"
"v_cvt_pk_bf16_f32 v210, v196, v197\n"
"v_mfma_f32_16x16x32_bf16 v[164:167], v[216:219], a[172:175], v[164:167]\n"
"v_exp_f32 v198, v198\n"
"v_mfma_f32_16x16x32_bf16 v[168:171], v[216:219], a[176:179], v[168:171]\n"
"ds_read_b64_tr_b16 a[166:167], v246 offset:26624\n"
"ds_read_b64_tr_b16 a[168:169], v247 offset:18432\n"
"v_mfma_f32_16x16x32_bf16 v[168:171], v[212:215], a[180:183], v[168:171]\n"
"v_exp_f32 v199, v199\n"
"v_mfma_f32_16x16x32_bf16 v[172:175], v[212:215], a[184:187], v[172:175]\n"
"ds_read_b64_tr_b16 a[170:171], v247 offset:26624\n"
"v_cvt_pk_bf16_f32 v211, v198, v199\n"
"v_mfma_f32_16x16x32_bf16 v[172:175], v[216:219], a[188:191], v[172:175]\n"
"v_exp_f32 v200, v200\n"
"v_mfma_f32_16x16x32_bf16 v[176:179], v[216:219], a[192:195], v[176:179]\n"
"ds_read_b64_tr_b16 a[172:173], v247 offset:2048\n"
"ds_read_b64_tr_b16 a[174:175], v247 offset:10240\n"
"v_mfma_f32_16x16x32_bf16 v[176:179], v[212:215], a[196:199], v[176:179]\n"
"v_exp_f32 v201, v201\n"
"v_mfma_f32_16x16x32_bf16 v[180:183], v[212:215], a[200:203], v[180:183]\n"
"ds_read_b64_tr_b16 a[176:177], v246 offset:2560\n"
"ds_read_b64_tr_b16 a[178:179], v246 offset:10752\n"
"v_mfma_f32_16x16x32_bf16 v[180:183], v[216:219], a[204:207], v[180:183]\n"
"v_exp_f32 v202, v202\n"
"v_mfma_f32_16x16x32_bf16 v[184:187], v[216:219], a[208:211], v[184:187]\n"
"ds_read_b64_tr_b16 a[180:181], v246 offset:18944\n"
"ds_read_b64_tr_b16 a[182:183], v246 offset:27136\n"
"v_mfma_f32_16x16x32_bf16 v[184:187], v[212:215], a[212:215], v[184:187]\n"
"v_exp_f32 v203, v203\n"
"v_mfma_f32_16x16x32_bf16 v[188:191], v[212:215], a[216:219], v[188:191]\n"
"ds_read_b64_tr_b16 a[184:185], v247 offset:18944\n"
"ds_read_b64_tr_b16 a[186:187], v247 offset:27136\n"
"v_mfma_f32_16x16x32_bf16 v[188:191], v[216:219], a[220:223], v[188:191]\n"
"ds_read_b64_tr_b16 a[188:189], v247 offset:2560\n"
"ds_read_b64_tr_b16 a[190:191], v247 offset:10752\n"
"v_mfma_f32_16x16x32_bf16 v[192:195], a[96:99], a[16:19], v[224:227]\n"
"v_exp_f32 v204, v204\n"
"v_mfma_f32_16x16x32_bf16 v[192:195], a[100:103], a[20:23], v[192:195]\n"
"v_cvt_pk_bf16_f32 v212, v200, v201\n"
"v_cvt_pk_bf16_f32 v213, v202, v203\n"
"v_mfma_f32_16x16x32_bf16 v[192:195], a[104:107], a[24:27], v[192:195]\n"
"v_exp_f32 v205, v205\n"
"v_mfma_f32_16x16x32_bf16 v[192:195], a[108:111], a[28:31], v[192:195]\n"
"ds_read_b64_tr_b16 a[192:193], v246 offset:3072\n"
"v_cvt_pk_bf16_f32 v214, v204, v205\n"
"v_mfma_f32_16x16x32_bf16 v[196:199], a[112:115], a[28:31], v[224:227]\n"
"v_exp_f32 v206, v206\n"
"v_mfma_f32_16x16x32_bf16 v[196:199], a[116:119], a[24:27], v[196:199]\n"
"ds_read_b64_tr_b16 a[194:195], v246 offset:11264\n"
"ds_read_b64_tr_b16 a[196:197], v246 offset:19456\n"
"v_mfma_f32_16x16x32_bf16 v[196:199], a[120:123], a[20:23], v[196:199]\n"
"v_exp_f32 v207, v207\n"
"v_mfma_f32_16x16x32_bf16 v[196:199], a[124:127], a[16:19], v[196:199]\n"
"ds_read_b64_tr_b16 a[198:199], v246 offset:27648\n"
"v_cvt_pk_bf16_f32 v215, v206, v207\n"
"v_mfma_f32_16x16x32_bf16 v[200:203], a[128:131], a[16:19], v[224:227]\n"
"v_exp_f32 v192, v192\n"
"v_mfma_f32_16x16x32_bf16 v[200:203], a[132:135], a[20:23], v[200:203]\n"
"ds_read_b64_tr_b16 a[200:201], v247 offset:19456\n"
"ds_read_b64_tr_b16 a[202:203], v247 offset:27648\n"
"v_mfma_f32_16x16x32_bf16 v[200:203], a[136:139], a[24:27], v[200:203]\n"
"v_exp_f32 v193, v193\n"
"v_mfma_f32_16x16x32_bf16 v[200:203], a[140:143], a[28:31], v[200:203]\n"
"ds_read_b64_tr_b16 a[204:205], v247 offset:3072\n"
"v_cvt_pk_bf16_f32 v216, v192, v193\n"
"v_mfma_f32_16x16x32_bf16 v[204:207], a[144:147], a[28:31], v[224:227]\n"
"v_exp_f32 v194, v194\n"
"v_mfma_f32_16x16x32_bf16 v[204:207], a[148:151], a[24:27], v[204:207]\n"
"ds_read_b64_tr_b16 a[206:207], v247 offset:11264\n"
"ds_read_b64_tr_b16 a[208:209], v246 offset:3584\n"
"v_mfma_f32_16x16x32_bf16 v[204:207], a[152:155], a[20:23], v[204:207]\n"
"v_exp_f32 v195, v195\n"
"v_mfma_f32_16x16x32_bf16 v[204:207], a[156:159], a[16:19], v[204:207]\n"
"v_cvt_pk_bf16_f32 v217, v194, v195\n"
"s_waitcnt lgkmcnt(15)\n"
"v_smfmac_f32_16x16x64_bf16 a[224:227], a[248:251], v[208:215], v252\n"
"v_exp_f32 v196, v196\n"
"v_mfma_f32_16x16x32_bf16 v[0:3], v[208:211], a[160:163], v[0:3]\n"
"ds_read_b64_tr_b16 a[210:211], v246 offset:11776\n"
"ds_read_b64_tr_b16 a[212:213], v246 offset:19968\n"
"v_mfma_f32_16x16x32_bf16 v[0:3], v[212:215], a[164:167], v[0:3]\n"
"v_exp_f32 v197, v197\n"
"v_mfma_f32_16x16x32_bf16 v[4:7], v[212:215], a[168:171], v[4:7]\n"
"v_cvt_pk_bf16_f32 v218, v196, v197\n"
"s_waitcnt lgkmcnt(11)\n"
"v_mfma_f32_16x16x32_bf16 v[4:7], v[208:211], a[172:175], v[4:7]\n"
"v_exp_f32 v198, v198\n"
"v_mfma_f32_16x16x32_bf16 v[8:11], v[208:211], a[176:179], v[8:11]\n"
"ds_read_b64_tr_b16 a[214:215], v246 offset:28160\n"
"ds_read_b64_tr_b16 a[216:217], v247 offset:19968\n"
"ds_read_b64_tr_b16 a[218:219], v247 offset:28160\n"
"ds_read_b64_tr_b16 a[220:221], v247 offset:3584\n"
"ds_read_b64_tr_b16 a[222:223], v247 offset:11776\n"
"v_mfma_f32_16x16x32_bf16 v[8:11], v[212:215], a[180:183], v[8:11]\n"
"v_exp_f32 v199, v199\n"
"v_mfma_f32_16x16x32_bf16 v[12:15], v[212:215], a[184:187], v[12:15]\n"
"v_cvt_pk_bf16_f32 v219, v198, v199\n"
"s_waitcnt lgkmcnt(8)\n"
"v_mfma_f32_16x16x32_bf16 v[12:15], v[208:211], a[188:191], v[12:15]\n"
"v_exp_f32 v200, v200\n"
"v_mfma_f32_16x16x32_bf16 v[16:19], v[208:211], a[192:195], v[16:19]\n"
"v_mfma_f32_16x16x32_bf16 v[16:19], v[212:215], a[196:199], v[16:19]\n"
"v_exp_f32 v201, v201\n"
"v_mfma_f32_16x16x32_bf16 v[20:23], v[212:215], a[200:203], v[20:23]\n"
"s_waitcnt lgkmcnt(0)\n"
"v_mfma_f32_16x16x32_bf16 v[20:23], v[208:211], a[204:207], v[20:23]\n"
"v_exp_f32 v202, v202\n"
"v_mfma_f32_16x16x32_bf16 v[24:27], v[208:211], a[208:211], v[24:27]\n"
"v_mfma_f32_16x16x32_bf16 v[24:27], v[212:215], a[212:215], v[24:27]\n"
"v_exp_f32 v203, v203\n"
"v_mfma_f32_16x16x32_bf16 v[28:31], v[212:215], a[216:219], v[28:31]\n"
"v_mfma_f32_16x16x32_bf16 v[28:31], v[208:211], a[220:223], v[28:31]\n"
"v_mfma_f32_16x16x32_bf16 v[192:195], a[96:99], a[32:35], v[228:231]\n"
"v_exp_f32 v204, v204\n"
"v_mfma_f32_16x16x32_bf16 v[192:195], a[100:103], a[36:39], v[192:195]\n"
"v_cvt_pk_bf16_f32 v212, v200, v201\n"
"v_cvt_pk_bf16_f32 v213, v202, v203\n"
"v_mfma_f32_16x16x32_bf16 v[192:195], a[104:107], a[40:43], v[192:195]\n"
"v_exp_f32 v205, v205\n"
"v_mfma_f32_16x16x32_bf16 v[192:195], a[108:111], a[44:47], v[192:195]\n"
"v_cvt_pk_bf16_f32 v214, v204, v205\n"
"v_mfma_f32_16x16x32_bf16 v[196:199], a[112:115], a[44:47], v[228:231]\n"
"v_exp_f32 v206, v206\n"
"v_mfma_f32_16x16x32_bf16 v[196:199], a[116:119], a[40:43], v[196:199]\n"
"v_mfma_f32_16x16x32_bf16 v[196:199], a[120:123], a[36:39], v[196:199]\n"
"v_exp_f32 v207, v207\n"
"v_mfma_f32_16x16x32_bf16 v[196:199], a[124:127], a[32:35], v[196:199]\n"
"v_cvt_pk_bf16_f32 v215, v206, v207\n"
"v_mfma_f32_16x16x32_bf16 v[200:203], a[128:131], a[32:35], v[228:231]\n"
"v_exp_f32 v192, v192\n"
"v_mfma_f32_16x16x32_bf16 v[200:203], a[132:135], a[36:39], v[200:203]\n"
"v_mfma_f32_16x16x32_bf16 v[200:203], a[136:139], a[40:43], v[200:203]\n"
"v_exp_f32 v193, v193\n"
"v_mfma_f32_16x16x32_bf16 v[200:203], a[140:143], a[44:47], v[200:203]\n"
"v_cvt_pk_bf16_f32 v208, v192, v193\n"
"v_mfma_f32_16x16x32_bf16 v[204:207], a[144:147], a[44:47], v[228:231]\n"
"v_exp_f32 v194, v194\n"
"v_mfma_f32_16x16x32_bf16 v[204:207], a[148:151], a[40:43], v[204:207]\n"
"v_mfma_f32_16x16x32_bf16 v[204:207], a[152:155], a[36:39], v[204:207]\n"
"v_exp_f32 v195, v195\n"
"v_mfma_f32_16x16x32_bf16 v[204:207], a[156:159], a[32:35], v[204:207]\n"
"v_cvt_pk_bf16_f32 v209, v194, v195\n"
"v_smfmac_f32_16x16x64_bf16 a[228:231], a[248:251], v[212:219], v252\n"
"v_exp_f32 v196, v196\n"
"v_mfma_f32_16x16x32_bf16 v[32:35], v[216:219], a[160:163], v[32:35]\n"
"v_mfma_f32_16x16x32_bf16 v[32:35], v[212:215], a[164:167], v[32:35]\n"
"v_exp_f32 v197, v197\n"
"v_mfma_f32_16x16x32_bf16 v[36:39], v[212:215], a[168:171], v[36:39]\n"
"v_cvt_pk_bf16_f32 v210, v196, v197\n"
"v_mfma_f32_16x16x32_bf16 v[36:39], v[216:219], a[172:175], v[36:39]\n"
"v_exp_f32 v198, v198\n"
"v_mfma_f32_16x16x32_bf16 v[40:43], v[216:219], a[176:179], v[40:43]\n"
"v_mfma_f32_16x16x32_bf16 v[40:43], v[212:215], a[180:183], v[40:43]\n"
"v_exp_f32 v199, v199\n"
"v_mfma_f32_16x16x32_bf16 v[44:47], v[212:215], a[184:187], v[44:47]\n"
"v_cvt_pk_bf16_f32 v211, v198, v199\n"
"v_mfma_f32_16x16x32_bf16 v[44:47], v[216:219], a[188:191], v[44:47]\n"
"v_exp_f32 v200, v200\n"
"v_mfma_f32_16x16x32_bf16 v[48:51], v[216:219], a[192:195], v[48:51]\n"
"v_mfma_f32_16x16x32_bf16 v[48:51], v[212:215], a[196:199], v[48:51]\n"
"v_exp_f32 v201, v201\n"
"v_mfma_f32_16x16x32_bf16 v[52:55], v[212:215], a[200:203], v[52:55]\n"
"v_mfma_f32_16x16x32_bf16 v[52:55], v[216:219], a[204:207], v[52:55]\n"
"v_exp_f32 v202, v202\n"
"v_mfma_f32_16x16x32_bf16 v[56:59], v[216:219], a[208:211], v[56:59]\n"
"v_mfma_f32_16x16x32_bf16 v[56:59], v[212:215], a[212:215], v[56:59]\n"
"v_exp_f32 v203, v203\n"
"v_mfma_f32_16x16x32_bf16 v[60:63], v[212:215], a[216:219], v[60:63]\n"
"v_mfma_f32_16x16x32_bf16 v[60:63], v[216:219], a[220:223], v[60:63]\n"
"v_mfma_f32_16x16x32_bf16 v[192:195], a[96:99], a[48:51], v[232:235]\n"
"v_exp_f32 v204, v204\n"
"v_mfma_f32_16x16x32_bf16 v[192:195], a[100:103], a[52:55], v[192:195]\n"
"v_cvt_pk_bf16_f32 v212, v200, v201\n"
"v_cvt_pk_bf16_f32 v213, v202, v203\n"
"v_mfma_f32_16x16x32_bf16 v[192:195], a[104:107], a[56:59], v[192:195]\n"
"v_exp_f32 v205, v205\n"
"v_mfma_f32_16x16x32_bf16 v[192:195], a[108:111], a[60:63], v[192:195]\n"
"v_cvt_pk_bf16_f32 v214, v204, v205\n"
"v_mfma_f32_16x16x32_bf16 v[196:199], a[112:115], a[60:63], v[232:235]\n"
"v_exp_f32 v206, v206\n"
"v_mfma_f32_16x16x32_bf16 v[196:199], a[116:119], a[56:59], v[196:199]\n"
"v_mfma_f32_16x16x32_bf16 v[196:199], a[120:123], a[52:55], v[196:199]\n"
"v_exp_f32 v207, v207\n"
"v_mfma_f32_16x16x32_bf16 v[196:199], a[124:127], a[48:51], v[196:199]\n"
"v_cvt_pk_bf16_f32 v215, v206, v207\n"
"v_mfma_f32_16x16x32_bf16 v[200:203], a[128:131], a[48:51], v[232:235]\n"
"v_exp_f32 v192, v192\n"
"v_mfma_f32_16x16x32_bf16 v[200:203], a[132:135], a[52:55], v[200:203]\n"
"v_mfma_f32_16x16x32_bf16 v[200:203], a[136:139], a[56:59], v[200:203]\n"
"v_exp_f32 v193, v193\n"
"v_mfma_f32_16x16x32_bf16 v[200:203], a[140:143], a[60:63], v[200:203]\n"
"v_cvt_pk_bf16_f32 v216, v192, v193\n"
"v_mfma_f32_16x16x32_bf16 v[204:207], a[144:147], a[60:63], v[232:235]\n"
"v_exp_f32 v194, v194\n"
"v_mfma_f32_16x16x32_bf16 v[204:207], a[148:151], a[56:59], v[204:207]\n"
"v_mfma_f32_16x16x32_bf16 v[204:207], a[152:155], a[52:55], v[204:207]\n"
"v_exp_f32 v195, v195\n"
"v_mfma_f32_16x16x32_bf16 v[204:207], a[156:159], a[48:51], v[204:207]\n"
"v_cvt_pk_bf16_f32 v217, v194, v195\n"
"v_smfmac_f32_16x16x64_bf16 a[232:235], a[248:251], v[208:215], v252\n"
"v_exp_f32 v196, v196\n"
"v_mfma_f32_16x16x32_bf16 v[64:67], v[208:211], a[160:163], v[64:67]\n"
"v_mfma_f32_16x16x32_bf16 v[64:67], v[212:215], a[164:167], v[64:67]\n"
"v_exp_f32 v197, v197\n"
"v_mfma_f32_16x16x32_bf16 v[68:71], v[212:215], a[168:171], v[68:71]\n"
"v_cvt_pk_bf16_f32 v218, v196, v197\n"
"v_mfma_f32_16x16x32_bf16 v[68:71], v[208:211], a[172:175], v[68:71]\n"
"v_exp_f32 v198, v198\n"
"v_mfma_f32_16x16x32_bf16 v[72:75], v[208:211], a[176:179], v[72:75]\n"
"v_mfma_f32_16x16x32_bf16 v[72:75], v[212:215], a[180:183], v[72:75]\n"
"v_exp_f32 v199, v199\n"
"v_mfma_f32_16x16x32_bf16 v[76:79], v[212:215], a[184:187], v[76:79]\n"
"v_cvt_pk_bf16_f32 v219, v198, v199\n"
"v_mfma_f32_16x16x32_bf16 v[76:79], v[208:211], a[188:191], v[76:79]\n"
"v_exp_f32 v200, v200\n"
"v_mfma_f32_16x16x32_bf16 v[80:83], v[208:211], a[192:195], v[80:83]\n"
"s_add_u32 s27, s27, 1\n"
"v_mov_b32 v255, s27\n"
"v_mfma_f32_16x16x32_bf16 v[80:83], v[212:215], a[196:199], v[80:83]\n"
"v_exp_f32 v201, v201\n"
"v_mfma_f32_16x16x32_bf16 v[84:87], v[212:215], a[200:203], v[84:87]\n"
"v_mfma_f32_16x16x32_bf16 v[84:87], v[208:211], a[204:207], v[84:87]\n"
"v_exp_f32 v202, v202\n"
"v_mfma_f32_16x16x32_bf16 v[88:91], v[208:211], a[208:211], v[88:91]\n"
"v_mfma_f32_16x16x32_bf16 v[88:91], v[212:215], a[212:215], v[88:91]\n"
"v_exp_f32 v203, v203\n"
"v_mfma_f32_16x16x32_bf16 v[92:95], v[212:215], a[216:219], v[92:95]\n"
"s_waitcnt vmcnt(0)\n"
"ds_write_b32 v253, v255\n"
"v_mfma_f32_16x16x32_bf16 v[92:95], v[208:211], a[220:223], v[92:95]\n"
"v_mfma_f32_16x16x32_bf16 v[192:195], a[96:99], a[64:67], v[236:239]\n"
"v_exp_f32 v204, v204\n"
"v_mfma_f32_16x16x32_bf16 v[192:195], a[100:103], a[68:71], v[192:195]\n"
"v_cvt_pk_bf16_f32 v212, v200, v201\n"
"v_cvt_pk_bf16_f32 v213, v202, v203\n"
"v_mfma_f32_16x16x32_bf16 v[192:195], a[104:107], a[72:75], v[192:195]\n"
"v_exp_f32 v205, v205\n"
"v_mfma_f32_16x16x32_bf16 v[192:195], a[108:111], a[76:79], v[192:195]\n"
"v_cvt_pk_bf16_f32 v214, v204, v205\n"
"v_mfma_f32_16x16x32_bf16 v[196:199], a[112:115], a[76:79], v[236:239]\n"
"v_exp_f32 v206, v206\n"
"v_mfma_f32_16x16x32_bf16 v[196:199], a[116:119], a[72:75], v[196:199]\n"
"v_mfma_f32_16x16x32_bf16 v[196:199], a[120:123], a[68:71], v[196:199]\n"
"v_exp_f32 v207, v207\n"
"v_mfma_f32_16x16x32_bf16 v[196:199], a[124:127], a[64:67], v[196:199]\n"
"v_cvt_pk_bf16_f32 v215, v206, v207\n"
"v_mfma_f32_16x16x32_bf16 v[200:203], a[128:131], a[64:67], v[236:239]\n"
"v_exp_f32 v192, v192\n"
"v_mfma_f32_16x16x32_bf16 v[200:203], a[132:135], a[68:71], v[200:203]\n"
"v_mfma_f32_16x16x32_bf16 v[200:203], a[136:139], a[72:75], v[200:203]\n"
"v_exp_f32 v193, v193\n"
"v_mfma_f32_16x16x32_bf16 v[200:203], a[140:143], a[76:79], v[200:203]\n"
"v_cvt_pk_bf16_f32 v208, v192, v193\n"
"v_mfma_f32_16x16x32_bf16 v[204:207], a[144:147], a[76:79], v[236:239]\n"
"v_exp_f32 v194, v194\n"
"v_mfma_f32_16x16x32_bf16 v[204:207], a[148:151], a[72:75], v[204:207]\n"
"v_mfma_f32_16x16x32_bf16 v[204:207], a[152:155], a[68:71], v[204:207]\n"
"v_exp_f32 v195, v195\n"
"v_mfma_f32_16x16x32_bf16 v[204:207], a[156:159], a[64:67], v[204:207]\n"
"v_cvt_pk_bf16_f32 v209, v194, v195\n"
"v_smfmac_f32_16x16x64_bf16 a[236:239], a[248:251], v[212:219], v252\n"
"v_exp_f32 v196, v196\n"
"v_mfma_f32_16x16x32_bf16 v[96:99], v[216:219], a[160:163], v[96:99]\n"
"v_mfma_f32_16x16x32_bf16 v[96:99], v[212:215], a[164:167], v[96:99]\n"
"v_exp_f32 v197, v197\n"
"v_mfma_f32_16x16x32_bf16 v[100:103], v[212:215], a[168:171], v[100:103]\n"
"v_cvt_pk_bf16_f32 v210, v196, v197\n"
"v_mfma_f32_16x16x32_bf16 v[100:103], v[216:219], a[172:175], v[100:103]\n"
"v_exp_f32 v198, v198\n"
"v_mfma_f32_16x16x32_bf16 v[104:107], v[216:219], a[176:179], v[104:107]\n"
"ds_read_b32 v255, v254\n"
"v_mfma_f32_16x16x32_bf16 v[104:107], v[212:215], a[180:183], v[104:107]\n"
"v_exp_f32 v199, v199\n"
"v_mfma_f32_16x16x32_bf16 v[108:111], v[212:215], a[184:187], v[108:111]\n"
"v_cvt_pk_bf16_f32 v211, v198, v199\n"
"v_mfma_f32_16x16x32_bf16 v[108:111], v[216:219], a[188:191], v[108:111]\n"
"v_exp_f32 v200, v200\n"
"v_mfma_f32_16x16x32_bf16 v[112:115], v[216:219], a[192:195], v[112:115]\n"
"v_mfma_f32_16x16x32_bf16 v[112:115], v[212:215], a[196:199], v[112:115]\n"
"v_exp_f32 v201, v201\n"
"v_mfma_f32_16x16x32_bf16 v[116:119], v[212:215], a[200:203], v[116:119]\n"
"v_mfma_f32_16x16x32_bf16 v[116:119], v[216:219], a[204:207], v[116:119]\n"
"v_exp_f32 v202, v202\n"
"v_mfma_f32_16x16x32_bf16 v[120:123], v[216:219], a[208:211], v[120:123]\n"
"v_mfma_f32_16x16x32_bf16 v[120:123], v[212:215], a[212:215], v[120:123]\n"
"v_exp_f32 v203, v203\n"
"v_mfma_f32_16x16x32_bf16 v[124:127], v[212:215], a[216:219], v[124:127]\n"
"s_waitcnt lgkmcnt(0)\n"
"v_cmp_gt_u32 vcc, s27, v255\n"
"v_mfma_f32_16x16x32_bf16 v[124:127], v[216:219], a[220:223], v[124:127]\n"
"s_cbranch_vccnz L_late6_15_%=\n"
"L_back6_15_%=:\n"
"v_mfma_f32_16x16x32_bf16 v[192:195], a[96:99], a[80:83], v[240:243]\n"
"v_exp_f32 v204, v204\n"
"v_mfma_f32_16x16x32_bf16 v[192:195], a[100:103], a[84:87], v[192:195]\n"
"v_cvt_pk_bf16_f32 v212, v200, v201\n"
"v_cvt_pk_bf16_f32 v213, v202, v203\n"
"v_mfma_f32_16x16x32_bf16 v[192:195], a[104:107], a[88:91], v[192:195]\n"
"v_exp_f32 v205, v205\n"
"v_mfma_f32_16x16x32_bf16 v[192:195], a[108:111], a[92:95], v[192:195]\n"
"ds_read_b128 a[96:99], v244 offset:32768\n"
"v_cvt_pk_bf16_f32 v214, v204, v205\n"
"v_mfma_f32_16x16x32_bf16 v[196:199], a[112:115], a[92:95], v[240:243]\n"
"v_exp_f32 v206, v206\n"
"v_mfma_f32_16x16x32_bf16 v[196:199], a[116:119], a[88:91], v[196:199]\n"
"ds_read_b128 a[100:103], v245 offset:32768\n"
"ds_read_b128 a[104:107], v244 offset:33792\n"
"v_mfma_f32_16x16x32_bf16 v[196:199], a[120:123], a[84:87], v[196:199]\n"
"v_exp_f32 v207, v207\n"
"v_mfma_f32_16x16x32_bf16 v[196:199], a[124:127], a[80:83], v[196:199]\n"
"ds_read_b128 a[108:111], v245 offset:33792\n"
"v_cvt_pk_bf16_f32 v215, v206, v207\n"
"v_mfma_f32_16x16x32_bf16 v[200:203], a[128:131], a[80:83], v[240:243]\n"
"v_exp_f32 v192, v192\n"
"v_mfma_f32_16x16x32_bf16 v[200:203], a[132:135], a[84:87], v[200:203]\n"
"ds_read_b128 a[112:115], v245 offset:41984\n"
"ds_read_b128 a[116:119], v244 offset:41984\n"
"v_mfma_f32_16x16x32_bf16 v[200:203], a[136:139], a[88:91], v[200:203]\n"
"v_exp_f32 v193, v193\n"
"v_mfma_f32_16x16x32_bf16 v[200:203], a[140:143], a[92:95], v[200:203]\n"
"ds_read_b128 a[120:123], v245 offset:40960\n"
"v_cvt_pk_bf16_f32 v216, v192, v193\n"
"v_mfma_f32_16x16x32_bf16 v[204:207], a[144:147], a[92:95], v[240:243]\n"
"v_exp_f32 v194, v194\n"
"v_mfma_f32_16x16x32_bf16 v[204:207], a[148:151], a[88:91], v[204:207]\n"
"ds_read_b128 a[124:127], v244 offset:40960\n"
"v_mfma_f32_16x16x32_bf16 v[204:207], a[152:155], a[84:87], v[204:207]\n"
"v_exp_f32 v195, v195\n"
"v_mfma_f32_16x16x32_bf16 v[204:207], a[156:159], a[80:83], v[204:207]\n"
"ds_read_b128 a[128:131], v244 offset:49152\n"
"v_cvt_pk_bf16_f32 v217, v194, v195\n"
"v_smfmac_f32_16x16x64_bf16 a[240:243], a[248:251], v[208:215], v252\n"
"v_exp_f32 v196, v196\n"
"v_mfma_f32_16x16x32_bf16 v[128:131], v[208:211], a[160:163], v[128:131]\n"
"ds_read_b128 a[132:135], v245 offset:49152\n"
"v_mfma_f32_16x16x32_bf16 v[128:131], v[212:215], a[164:167], v[128:131]\n"
"v_exp_f32 v197, v197\n"
"v_mfma_f32_16x16x32_bf16 v[132:135], v[212:215], a[168:171], v[132:135]\n"
"ds_read_b128 a[136:139], v244 offset:50176\n"
"v_cvt_pk_bf16_f32 v218, v196, v197\n"
"v_mfma_f32_16x16x32_bf16 v[132:135], v[208:211], a[172:175], v[132:135]\n"
"v_exp_f32 v198, v198\n"
"v_mfma_f32_16x16x32_bf16 v[136:139], v[208:211], a[176:179], v[136:139]\n"
"ds_read_b128 a[140:143], v245 offset:50176\n"
"ds_read_b128 a[144:147], v245 offset:58368\n"
"v_mfma_f32_16x16x32_bf16 v[136:139], v[212:215], a[180:183], v[136:139]\n"
"v_exp_f32 v199, v199\n"
"v_mfma_f32_16x16x32_bf16 v[140:143], v[212:215], a[184:187], v[140:143]\n"
"ds_read_b128 a[148:151], v244 offset:58368\n"
"v_cvt_pk_bf16_f32 v219, v198, v199\n"
"v_mfma_f32_16x16x32_bf16 v[140:143], v[208:211], a[188:191], v[140:143]\n"
"v_exp_f32 v200, v200\n"
"v_mfma_f32_16x16x32_bf16 v[144:147], v[208:211], a[192:195], v[144:147]\n"
"ds_read_b128 a[152:155], v245 offset:57344\n"
"v_mfma_f32_16x16x32_bf16 v[144:147], v[212:215], a[196:199], v[144:147]\n"
"v_exp_f32 v201, v201\n"
"v_mfma_f32_16x16x32_bf16 v[148:151], v[212:215], a[200:203], v[148:151]\n"
"ds_read_b128 a[156:159], v244 offset:57344\n"
"v_mfma_f32_16x16x32_bf16 v[148:151], v[208:211], a[204:207], v[148:151]\n"
"v_exp_f32 v202, v202\n"
"v_mfma_f32_16x16x32_bf16 v[152:155], v[208:211], a[208:211], v[152:155]\n"
"v_mfma_f32_16x16x32_bf16 v[152:155], v[212:215], a[212:215], v[152:155]\n"
"v_exp_f32 v203, v203\n"
"v_mfma_f32_16x16x32_bf16 v[156:159], v[212:215], a[216:219], v[156:159]\n"
"v_mfma_f32_16x16x32_bf16 v[156:159], v[208:211], a[220:223], v[156:159]\n"
"s_waitcnt lgkmcnt(8)\n"
"v_mfma_f32_16x16x32_bf16 v[192:195], a[96:99], a[0:3], v[220:223]\n"
"v_exp_f32 v204, v204\n"
"v_mfma_f32_16x16x32_bf16 v[192:195], a[100:103], a[4:7], v[192:195]\n"
"v_cvt_pk_bf16_f32 v212, v200, v201\n"
"v_cvt_pk_bf16_f32 v213, v202, v203\n"
"v_mfma_f32_16x16x32_bf16 v[192:195], a[104:107], a[8:11], v[192:195]\n"
"v_exp_f32 v205, v205\n"
"v_mfma_f32_16x16x32_bf16 v[192:195], a[108:111], a[12:15], v[192:195]\n"
"v_cvt_pk_bf16_f32 v214, v204, v205\n"
"v_mfma_f32_16x16x32_bf16 v[196:199], a[112:115], a[12:15], v[220:223]\n"
"v_exp_f32 v206, v206\n"
"v_mfma_f32_16x16x32_bf16 v[196:199], a[116:119], a[8:11], v[196:199]\n"
"v_mfma_f32_16x16x32_bf16 v[196:199], a[120:123], a[4:7], v[196:199]\n"
"v_exp_f32 v207, v207\n"
"v_mfma_f32_16x16x32_bf16 v[196:199], a[124:127], a[0:3], v[196:199]\n"
"v_cvt_pk_bf16_f32 v215, v206, v207\n"
"s_waitcnt lgkmcnt(0)\n"
"v_mfma_f32_16x16x32_bf16 v[200:203], a[128:131], a[0:3], v[220:223]\n"
"v_exp_f32 v192, v192\n"
"v_mfma_f32_16x16x32_bf16 v[200:203], a[132:135], a[4:7], v[200:203]\n"
"v_mfma_f32_16x16x32_bf16 v[200:203], a[136:139], a[8:11], v[200:203]\n"
"v_exp_f32 v193, v193\n"
"v_mfma_f32_16x16x32_bf16 v[200:203], a[140:143], a[12:15], v[200:203]\n"
"v_cvt_pk_bf16_f32 v208, v192, v193\n"
"v_mfma_f32_16x16x32_bf16 v[204:207], a[144:147], a[12:15], v[220:223]\n"
"v_exp_f32 v194, v194\n"
"v_mfma_f32_16x16x32_bf16 v[204:207], a[148:151], a[8:11], v[204:207]\n"
"v_mfma_f32_16x16x32_bf16 v[204:207], a[152:155], a[4:7], v[204:207]\n"
"v_exp_f32 v195, v195\n"
"v_mfma_f32_16x16x32_bf16 v[204:207], a[156:159], a[0:3], v[204:207]\n"
"v_cvt_pk_bf16_f32 v209, v194, v195\n"
"v_smfmac_f32_16x16x64_bf16 a[244:247], a[248:251], v[212:219], v252\n"
"v_exp_f32 v196, v196\n"
"v_mfma_f32_16x16x32_bf16 v[160:163], v[216:219], a[160:163], v[160:163]\n"
"ds_read_b64_tr_b16 a[160:161], v246 offset:34816\n"
"ds_read_b64_tr_b16 a[162:163], v246 offset:43008\n"
"v_mfma_f32_16x16x32_bf16 v[160:163], v[212:215], a[164:167], v[160:163]\n"
"v_exp_f32 v197, v197\n"
"v_mfma_f32_16x16x32_bf16 v[164:167], v[212:215], a[168:171], v[164:167]\n"
"ds_read_b64_tr_b16 a[164:165], v246 offset:51200\n"
"v_cvt_pk_bf16_f32 v210, v196, v197\n"
"v_mfma_f32_16x16x32_bf16 v[164:167], v[216:219], a[172:175], v[164:167]\n"
"v_exp_f32 v198, v198\n"
"v_mfma_f32_16x16x32_bf16 v[168:171], v[216:219], a[176:179], v[168:171]\n"
"ds_read_b64_tr_b16 a[166:167], v246 offset:59392\n"
"ds_read_b64_tr_b16 a[168:169], v247 offset:51200\n"
"v_mfma_f32_16x16x32_bf16 v[168:171], v[212:215], a[180:183], v[168:171]\n"
"v_exp_f32 v199, v199\n"
"v_mfma_f32_16x16x32_bf16 v[172:175], v[212:215], a[184:187], v[172:175]\n"
"ds_read_b64_tr_b16 a[170:171], v247 offset:59392\n"
"v_cvt_pk_bf16_f32 v211, v198, v199\n"
"v_mfma_f32_16x16x32_bf16 v[172:175], v[216:219], a[188:191], v[172:175]\n"
"v_exp_f32 v200, v200\n"
"v_mfma_f32_16x16x32_bf16 v[176:179], v[216:219], a[192:195], v[176:179]\n"
"ds_read_b64_tr_b16 a[172:173], v247 offset:34816\n"
"ds_read_b64_tr_b16 a[174:175], v247 offset:43008\n"
"v_mfma_f32_16x16x32_bf16 v[176:179], v[212:215], a[196:199], v[176:179]\n"
"v_exp_f32 v201, v201\n"
"v_mfma_f32_16x16x32_bf16 v[180:183], v[212:215], a[200:203], v[180:183]\n"
"ds_read_b64_tr_b16 a[176:177], v246 offset:35328\n"
"ds_read_b64_tr_b16 a[178:179], v246 offset:43520\n"
"v_mfma_f32_16x16x32_bf16 v[180:183], v[216:219], a[204:207], v[180:183]\n"
"v_exp_f32 v202, v202\n"
"v_mfma_f32_16x16x32_bf16 v[184:187], v[216:219], a[208:211], v[184:187]\n"
"ds_read_b64_tr_b16 a[180:181], v246 offset:51712\n"
"ds_read_b64_tr_b16 a[182:183], v246 offset:59904\n"
"v_mfma_f32_16x16x32_bf16 v[184:187], v[212:215], a[212:215], v[184:187]\n"
"v_exp_f32 v203, v203\n"
"v_mfma_f32_16x16x32_bf16 v[188:191], v[212:215], a[216:219], v[188:191]\n"
"ds_read_b64_tr_b16 a[184:185], v247 offset:51712\n"
"ds_read_b64_tr_b16 a[186:187], v247 offset:59904\n"
"v_mfma_f32_16x16x32_bf16 v[188:191], v[216:219], a[220:223], v[188:191]\n"
"ds_read_b64_tr_b16 a[188:189], v247 offset:35328\n"
"ds_read_b64_tr_b16 a[190:191], v247 offset:43520\n"
"v_mfma_f32_16x16x32_bf16 v[192:195], a[96:99], a[16:19], v[224:227]\n"
"v_exp_f32 v204, v204\n"
"v_mfma_f32_16x16x32_bf16 v[192:195], a[100:103], a[20:23], v[192:195]\n"
"v_cvt_pk_bf16_f32 v212, v200, v201\n"
"v_cvt_pk_bf16_f32 v213, v202, v203\n"
"v_mfma_f32_16x16x32_bf16 v[192:195], a[104:107], a[24:27], v[192:195]\n"
"v_exp_f32 v205, v205\n"
"v_mov_b32 v255, 0\n"
"v_mbcnt_lo_u32_b32 v253, -1, 0\n"
"v_mfma_f32_16x16x32_bf16 v[192:195], a[108:111], a[28:31], v[192:195]\n"
"ds_read_b64_tr_b16 a[192:193], v246 offset:35840\n"
"v_cvt_pk_bf16_f32 v214, v204, v205\n"
"v_mbcnt_hi_u32_b32 v253, -1, v253\n"
"v_mfma_f32_16x16x32_bf16 v[196:199], a[112:115], a[28:31], v[224:227]\n"
"v_exp_f32 v206, v206\n"
"s_mul_i32 s59, %[wid], 24576\n"
"v_mfma_f32_16x16x32_bf16 v[196:199], a[116:119], a[24:27], v[196:199]\n"
"ds_read_b64_tr_b16 a[194:195], v246 offset:44032\n"
"ds_read_b64_tr_b16 a[196:197], v246 offset:52224\n"
"s_lshl_b32 s55, %[wid], 13\n"
"s_add_u32 s55, s55, 131072\n"
"v_mfma_f32_16x16x32_bf16 v[196:199], a[120:123], a[20:23], v[196:199]\n"
"v_exp_f32 v207, v207\n"
"v_and_b32 v254, 3, v253\n"
"v_mfma_f32_16x16x32_bf16 v[196:199], a[124:127], a[16:19], v[196:199]\n"
"ds_read_b64_tr_b16 a[198:199], v246 offset:60416\n"
"v_cvt_pk_bf16_f32 v215, v206, v207\n"
"v_lshlrev_b32 v250, 5, v254\n"
"v_mfma_f32_16x16x32_bf16 v[200:203], a[128:131], a[16:19], v[224:227]\n"
"v_exp_f32 v192, v192\n"
"v_lshlrev_b32 v249, 6, v254\n"
"v_mfma_f32_16x16x32_bf16 v[200:203], a[132:135], a[20:23], v[200:203]\n"
"ds_read_b64_tr_b16 a[200:201], v247 offset:52224\n"
"ds_read_b64_tr_b16 a[202:203], v247 offset:60416\n"
"v_bfe_u32 v254, v253, 2, 1\n"
"v_lshl_add_u32 v250, v254, 4, v250\n"
"v_mfma_f32_16x16x32_bf16 v[200:203], a[136:139], a[24:27], v[200:203]\n"
"v_exp_f32 v193, v193\n"
"v_bfe_u32 v254, v253, 3, 1\n"
"v_mfma_f32_16x16x32_bf16 v[200:203], a[140:143], a[28:31], v[200:203]\n"
"ds_read_b64_tr_b16 a[204:205], v247 offset:35840\n"
"v_cvt_pk_bf16_f32 v216, v192, v193\n"
"v_lshl_add_u32 v250, v254, 7, v250\n"
"v_mfma_f32_16x16x32_bf16 v[204:207], a[144:147], a[28:31], v[224:227]\n"
"v_exp_f32 v194, v194\n"
"v_bfe_u32 v254, v253, 2, 2\n"
"v_mfma_f32_16x16x32_bf16 v[204:207], a[148:151], a[24:27], v[204:207]\n"
"ds_read_b64_tr_b16 a[206:207], v247 offset:44032\n"
"ds_read_b64_tr_b16 a[208:209], v246 offset:36352\n"
"v_lshl_add_u32 v249, v254, 3, v249\n"
"v_lshl_add_u32 v251, v253, 2, s55\n"
"v_mfma_f32_16x16x32_bf16 v[204:207], a[152:155], a[20:23], v[204:207]\n"
"v_exp_f32 v195, v195\n"
"v_and_b32 v254, 15, v253\n"
"v_mfma_f32_16x16x32_bf16 v[204:207], a[156:159], a[16:19], v[204:207]\n"
"v_cvt_pk_bf16_f32 v217, v194, v195\n"
"s_waitcnt lgkmcnt(15)\n"
"v_lshl_add_u32 v248, v254, 3, s55\n"
"v_smfmac_f32_16x16x64_bf16 a[224:227], a[248:251], v[208:215], v252\n"
"v_exp_f32 v196, v196\n"
"v_lshrrev_b32 v254, 4, v253\n"
"v_mfma_f32_16x16x32_bf16 v[0:3], v[208:211], a[160:163], v[0:3]\n"
"ds_read_b64_tr_b16 a[210:211], v246 offset:44544\n"
"ds_read_b64_tr_b16 a[212:213], v246 offset:52736\n"
"v_lshl_add_u32 v250, v254, 8, v250\n"
"v_mul_u32_u24 v253, 288, v254\n"
"v_mfma_f32_16x16x32_bf16 v[0:3], v[212:215], a[164:167], v[0:3]\n"
"v_exp_f32 v197, v197\n"
"v_add3_u32 v249, v249, v253, s55\n"
"v_mfma_f32_16x16x32_bf16 v[4:7], v[212:215], a[168:171], v[4:7]\n"
"v_cvt_pk_bf16_f32 v218, v196, v197\n"
"s_waitcnt lgkmcnt(11)\n"
"v_lshl_add_u32 v248, v253, 2, v248\n"
"v_mfma_f32_16x16x32_bf16 v[4:7], v[208:211], a[172:175], v[4:7]\n"
"v_exp_f32 v198, v198\n"
"v_lshl_add_u32 v254, v254, 4, s55\n"
"v_mfma_f32_16x16x32_bf16 v[8:11], v[208:211], a[176:179], v[8:11]\n"
"ds_read_b64_tr_b16 a[214:215], v246 offset:60928\n"
"ds_read_b64_tr_b16 a[216:217], v247 offset:52736\n"
"ds_read_b64_tr_b16 a[218:219], v247 offset:60928\n"
"ds_read_b64_tr_b16 a[220:221], v247 offset:36352\n"
"ds_read_b64_tr_b16 a[222:223], v247 offset:44544\n"
"v_mfma_f32_16x16x32_bf16 v[8:11], v[212:215], a[180:183], v[8:11]\n"
"v_exp_f32 v199, v199\n"
"v_mfma_f32_16x16x32_bf16 v[12:15], v[212:215], a[184:187], v[12:15]\n"
"v_cvt_pk_bf16_f32 v219, v198, v199\n"
"s_waitcnt lgkmcnt(8)\n"
"v_mfma_f32_16x16x32_bf16 v[12:15], v[208:211], a[188:191], v[12:15]\n"
"v_exp_f32 v200, v200\n"
"v_mfma_f32_16x16x32_bf16 v[16:19], v[208:211], a[192:195], v[16:19]\n"
"v_accvgpr_read_b32 v220, a224\n"
"v_mfma_f32_16x16x32_bf16 v[16:19], v[212:215], a[196:199], v[16:19]\n"
"v_exp_f32 v201, v201\n"
"v_accvgpr_read_b32 v221, a225\n"
"v_mfma_f32_16x16x32_bf16 v[20:23], v[212:215], a[200:203], v[20:23]\n"
"s_waitcnt lgkmcnt(0)\n"
"v_add_f32 v220, v220, v221\n"
"v_mfma_f32_16x16x32_bf16 v[20:23], v[208:211], a[204:207], v[20:23]\n"
"v_exp_f32 v202, v202\n"
"v_add_f32 v255, v255, v220\n"
"v_mfma_f32_16x16x32_bf16 v[24:27], v[208:211], a[208:211], v[24:27]\n"
"v_rcp_f32 v220, v220\n"
"v_mfma_f32_16x16x32_bf16 v[24:27], v[212:215], a[212:215], v[24:27]\n"
"ds_write_b32 v251, v220 offset:5120\n"
"v_exp_f32 v203, v203\n"
"v_mfma_f32_16x16x32_bf16 v[28:31], v[212:215], a[216:219], v[28:31]\n"
"ds_read_b128 v[220:223], v254 offset:5120\n"
"v_mfma_f32_16x16x32_bf16 v[28:31], v[208:211], a[220:223], v[28:31]\n"
"v_mfma_f32_16x16x32_bf16 v[192:195], a[96:99], a[32:35], v[228:231]\n"
"v_exp_f32 v204, v204\n"
"s_waitcnt lgkmcnt(0)\n"
"v_mfma_f32_16x16x32_bf16 v[192:195], a[100:103], a[36:39], v[192:195]\n"
"v_cvt_pk_bf16_f32 v212, v200, v201\n"
"v_cvt_pk_bf16_f32 v213, v202, v203\n"
"v_pk_mul_f32 v[0:1], v[0:1], v[220:221]\n"
"v_pk_mul_f32 v[2:3], v[2:3], v[222:223]\n"
"v_pk_mul_f32 v[4:5], v[4:5], v[220:221]\n"
"v_pk_mul_f32 v[6:7], v[6:7], v[222:223]\n"
"v_pk_mul_f32 v[8:9], v[8:9], v[220:221]\n"
"v_pk_mul_f32 v[10:11], v[10:11], v[222:223]\n"
"v_pk_mul_f32 v[12:13], v[12:13], v[220:221]\n"
"v_pk_mul_f32 v[14:15], v[14:15], v[222:223]\n"
"v_mfma_f32_16x16x32_bf16 v[192:195], a[104:107], a[40:43], v[192:195]\n"
"v_exp_f32 v205, v205\n"
"v_cvt_pk_bf16_f32 v0, v0, v4\n"
"v_cvt_pk_bf16_f32 v8, v8, v12\n"
"v_mfma_f32_16x16x32_bf16 v[192:195], a[108:111], a[44:47], v[192:195]\n"
"v_cvt_pk_bf16_f32 v214, v204, v205\n"
"v_mfma_f32_16x16x32_bf16 v[196:199], a[112:115], a[44:47], v[228:231]\n"
"ds_write2_b32 v248, v0, v8 offset0:0 offset1:1\n"
"v_exp_f32 v206, v206\n"
"v_cvt_pk_bf16_f32 v1, v1, v5\n"
"v_cvt_pk_bf16_f32 v9, v9, v13\n"
"v_mfma_f32_16x16x32_bf16 v[196:199], a[116:119], a[40:43], v[196:199]\n"
"ds_write2_b32 v248, v1, v9 offset0:72 offset1:73\n"
"v_cvt_pk_bf16_f32 v2, v2, v6\n"
"v_cvt_pk_bf16_f32 v10, v10, v14\n"
"v_pk_mul_f32 v[16:17], v[16:17], v[220:221]\n"
"v_pk_mul_f32 v[18:19], v[18:19], v[222:223]\n"
"v_pk_mul_f32 v[20:21], v[20:21], v[220:221]\n"
"v_pk_mul_f32 v[22:23], v[22:23], v[222:223]\n"
"v_pk_mul_f32 v[24:25], v[24:25], v[220:221]\n"
"v_pk_mul_f32 v[26:27], v[26:27], v[222:223]\n"
"v_pk_mul_f32 v[28:29], v[28:29], v[220:221]\n"
"v_pk_mul_f32 v[30:31], v[30:31], v[222:223]\n"
"v_mfma_f32_16x16x32_bf16 v[196:199], a[120:123], a[36:39], v[196:199]\n"
"v_exp_f32 v207, v207\n"
"v_mfma_f32_16x16x32_bf16 v[196:199], a[124:127], a[32:35], v[196:199]\n"
"ds_write2_b32 v248, v2, v10 offset0:144 offset1:145\n"
"v_cvt_pk_bf16_f32 v215, v206, v207\n"
"v_cvt_pk_bf16_f32 v3, v3, v7\n"
"v_cvt_pk_bf16_f32 v11, v11, v15\n"
"v_mfma_f32_16x16x32_bf16 v[200:203], a[128:131], a[32:35], v[228:231]\n"
"ds_write2_b32 v248, v3, v11 offset0:216 offset1:217\n"
"v_exp_f32 v192, v192\n"
"v_cvt_pk_bf16_f32 v16, v16, v20\n"
"v_cvt_pk_bf16_f32 v24, v24, v28\n"
"v_mfma_f32_16x16x32_bf16 v[200:203], a[132:135], a[36:39], v[200:203]\n"
"v_mfma_f32_16x16x32_bf16 v[200:203], a[136:139], a[40:43], v[200:203]\n"
"ds_write2_b32 v248, v16, v24 offset0:32 offset1:33\n"
"v_exp_f32 v193, v193\n"
"v_cvt_pk_bf16_f32 v17, v17, v21\n"
"v_cvt_pk_bf16_f32 v25, v25, v29\n"
"v_mfma_f32_16x16x32_bf16 v[200:203], a[140:143], a[44:47], v[200:203]\n"
"ds_write2_b32 v248, v17, v25 offset0:104 offset1:105\n"
"v_cvt_pk_bf16_f32 v208, v192, v193\n"
"v_cvt_pk_bf16_f32 v18, v18, v22\n"
"v_cvt_pk_bf16_f32 v26, v26, v30\n"
"v_mfma_f32_16x16x32_bf16 v[204:207], a[144:147], a[44:47], v[228:231]\n"
"v_exp_f32 v194, v194\n"
"v_mfma_f32_16x16x32_bf16 v[204:207], a[148:151], a[40:43], v[204:207]\n"
"ds_write2_b32 v248, v18, v26 offset0:176 offset1:177\n"
"v_cvt_pk_bf16_f32 v19, v19, v23\n"
"v_cvt_pk_bf16_f32 v27, v27, v31\n"
"v_mfma_f32_16x16x32_bf16 v[204:207], a[152:155], a[36:39], v[204:207]\n"
"ds_write2_b32 v248, v19, v27 offset0:248 offset1:249\n"
"v_exp_f32 v195, v195\n"
"v_mfma_f32_16x16x32_bf16 v[204:207], a[156:159], a[32:35], v[204:207]\n"
"v_cvt_pk_bf16_f32 v209, v194, v195\n"
"v_smfmac_f32_16x16x64_bf16 a[228:231], a[248:251], v[212:219], v252\n"
"ds_read_b64_tr_b16 v[4:5], v249 offset:0\n"
"ds_read_b64_tr_b16 v[6:7], v249 offset:32\n"
"v_exp_f32 v196, v196\n"
"v_mfma_f32_16x16x32_bf16 v[32:35], v[216:219], a[160:163], v[32:35]\n"
"ds_read_b64_tr_b16 v[12:13], v249 offset:1152\n"
"ds_read_b64_tr_b16 v[14:15], v249 offset:1184\n"
"v_mfma_f32_16x16x32_bf16 v[32:35], v[212:215], a[164:167], v[32:35]\n"
"v_exp_f32 v197, v197\n"
"v_mfma_f32_16x16x32_bf16 v[36:39], v[212:215], a[168:171], v[36:39]\n"
"ds_read_b64_tr_b16 v[20:21], v249 offset:2304\n"
"ds_read_b64_tr_b16 v[22:23], v249 offset:2336\n"
"v_cvt_pk_bf16_f32 v210, v196, v197\n"
"v_mfma_f32_16x16x32_bf16 v[36:39], v[216:219], a[172:175], v[36:39]\n"
"ds_read_b64_tr_b16 v[28:29], v249 offset:3456\n"
"ds_read_b64_tr_b16 v[30:31], v249 offset:3488\n"
"v_exp_f32 v198, v198\n"
"v_mfma_f32_16x16x32_bf16 v[40:43], v[216:219], a[176:179], v[40:43]\n"
"v_mfma_f32_16x16x32_bf16 v[40:43], v[212:215], a[180:183], v[40:43]\n"
"v_exp_f32 v199, v199\n"
"v_mfma_f32_16x16x32_bf16 v[44:47], v[212:215], a[184:187], v[44:47]\n"
"v_cvt_pk_bf16_f32 v211, v198, v199\n"
"v_mfma_f32_16x16x32_bf16 v[44:47], v[216:219], a[188:191], v[44:47]\n"
"v_exp_f32 v200, v200\n"
"v_mfma_f32_16x16x32_bf16 v[48:51], v[216:219], a[192:195], v[48:51]\n"
"v_accvgpr_read_b32 v224, a228\n"
"v_mfma_f32_16x16x32_bf16 v[48:51], v[212:215], a[196:199], v[48:51]\n"
"s_waitcnt lgkmcnt(6)\n"
"v_exp_f32 v201, v201\n"
"s_add_u32 s58, s59, 0\n"
"buffer_store_dwordx4 v[4:7], v250, s[48:51], s58 offen sc0 sc1 nt\n"
"v_accvgpr_read_b32 v225, a229\n"
"v_mfma_f32_16x16x32_bf16 v[52:55], v[212:215], a[200:203], v[52:55]\n"
"s_waitcnt lgkmcnt(4)\n"
"buffer_store_dwordx4 v[12:15], v250, s[48:51], s58 offen offset:1024 sc0 sc1 nt\n"
"v_add_f32 v224, v224, v225\n"
"v_mfma_f32_16x16x32_bf16 v[52:55], v[216:219], a[204:207], v[52:55]\n"
"v_exp_f32 v202, v202\n"
"v_add_f32 v255, v255, v224\n"
"v_mfma_f32_16x16x32_bf16 v[56:59], v[216:219], a[208:211], v[56:59]\n"
"s_waitcnt lgkmcnt(2)\n"
"buffer_store_dwordx4 v[20:23], v250, s[48:51], s58 offen offset:2048 sc0 sc1 nt\n"
"v_rcp_f32 v224, v224\n"
"v_mfma_f32_16x16x32_bf16 v[56:59], v[212:215], a[212:215], v[56:59]\n"
"ds_write_b32 v251, v224 offset:5120\n"
"v_exp_f32 v203, v203\n"
"v_mfma_f32_16x16x32_bf16 v[60:63], v[212:215], a[216:219], v[60:63]\n"
"s_waitcnt lgkmcnt(1)\n"
"buffer_store_dwordx4 v[28:31], v250, s[48:51], s58 offen offset:3072 sc0 sc1 nt\n"
"ds_read_b128 v[224:227], v254 offset:5120\n"
"v_mfma_f32_16x16x32_bf16 v[60:63], v[216:219], a[220:223], v[60:63]\n"
"v_mfma_f32_16x16x32_bf16 v[192:195], a[96:99], a[48:51], v[232:235]\n"
"v_exp_f32 v204, v204\n"
"s_waitcnt lgkmcnt(0)\n"
"v_mfma_f32_16x16x32_bf16 v[192:195], a[100:103], a[52:55], v[192:195]\n"
"v_cvt_pk_bf16_f32 v212, v200, v201\n"
"v_cvt_pk_bf16_f32 v213, v202, v203\n"
"v_pk_mul_f32 v[32:33], v[32:33], v[224:225]\n"
"v_pk_mul_f32 v[34:35], v[34:35], v[226:227]\n"
"v_pk_mul_f32 v[36:37], v[36:37], v[224:225]\n"
"v_pk_mul_f32 v[38:39], v[38:39], v[226:227]\n"
"v_pk_mul_f32 v[40:41], v[40:41], v[224:225]\n"
"v_pk_mul_f32 v[42:43], v[42:43], v[226:227]\n"
"v_pk_mul_f32 v[44:45], v[44:45], v[224:225]\n"
"v_pk_mul_f32 v[46:47], v[46:47], v[226:227]\n"
"v_mfma_f32_16x16x32_bf16 v[192:195], a[104:107], a[56:59], v[192:195]\n"
"v_exp_f32 v205, v205\n"
"v_cvt_pk_bf16_f32 v32, v32, v36\n"
"v_cvt_pk_bf16_f32 v40, v40, v44\n"
"v_mfma_f32_16x16x32_bf16 v[192:195], a[108:111], a[60:63], v[192:195]\n"
"v_cvt_pk_bf16_f32 v214, v204, v205\n"
"v_mfma_f32_16x16x32_bf16 v[196:199], a[112:115], a[60:63], v[232:235]\n"
"ds_write2_b32 v248, v32, v40 offset0:0 offset1:1\n"
"v_exp_f32 v206, v206\n"
"v_cvt_pk_bf16_f32 v33, v33, v37\n"
"v_cvt_pk_bf16_f32 v41, v41, v45\n"
"v_mfma_f32_16x16x32_bf16 v[196:199], a[116:119], a[56:59], v[196:199]\n"
"ds_write2_b32 v248, v33, v41 offset0:72 offset1:73\n"
"v_cvt_pk_bf16_f32 v34, v34, v38\n"
"v_cvt_pk_bf16_f32 v42, v42, v46\n"
"v_pk_mul_f32 v[48:49], v[48:49], v[224:225]\n"
"v_pk_mul_f32 v[50:51], v[50:51], v[226:227]\n"
"v_pk_mul_f32 v[52:53], v[52:53], v[224:225]\n"
"v_pk_mul_f32 v[54:55], v[54:55], v[226:227]\n"
"v_pk_mul_f32 v[56:57], v[56:57], v[224:225]\n"
"v_pk_mul_f32 v[58:59], v[58:59], v[226:227]\n"
"v_pk_mul_f32 v[60:61], v[60:61], v[224:225]\n"
"v_pk_mul_f32 v[62:63], v[62:63], v[226:227]\n"
"v_mfma_f32_16x16x32_bf16 v[196:199], a[120:123], a[52:55], v[196:199]\n"
"v_exp_f32 v207, v207\n"
"v_mfma_f32_16x16x32_bf16 v[196:199], a[124:127], a[48:51], v[196:199]\n"
"ds_write2_b32 v248, v34, v42 offset0:144 offset1:145\n"
"v_cvt_pk_bf16_f32 v215, v206, v207\n"
"v_cvt_pk_bf16_f32 v35, v35, v39\n"
"v_cvt_pk_bf16_f32 v43, v43, v47\n"
"v_mfma_f32_16x16x32_bf16 v[200:203], a[128:131], a[48:51], v[232:235]\n"
"ds_write2_b32 v248, v35, v43 offset0:216 offset1:217\n"
"v_exp_f32 v192, v192\n"
"v_cvt_pk_bf16_f32 v48, v48, v52\n"
"v_cvt_pk_bf16_f32 v56, v56, v60\n"
"v_mfma_f32_16x16x32_bf16 v[200:203], a[132:135], a[52:55], v[200:203]\n"
"v_mfma_f32_16x16x32_bf16 v[200:203], a[136:139], a[56:59], v[200:203]\n"
"ds_write2_b32 v248, v48, v56 offset0:32 offset1:33\n"
"v_exp_f32 v193, v193\n"
"v_cvt_pk_bf16_f32 v49, v49, v53\n"
"v_cvt_pk_bf16_f32 v57, v57, v61\n"
"v_mfma_f32_16x16x32_bf16 v[200:203], a[140:143], a[60:63], v[200:203]\n"
"ds_write2_b32 v248, v49, v57 offset0:104 offset1:105\n"
"v_cvt_pk_bf16_f32 v216, v192, v193\n"
"v_cvt_pk_bf16_f32 v50, v50, v54\n"
"v_cvt_pk_bf16_f32 v58, v58, v62\n"
"v_mfma_f32_16x16x32_bf16 v[204:207], a[144:147], a[60:63], v[232:235]\n"
"v_exp_f32 v194, v194\n"
"v_mfma_f32_16x16x32_bf16 v[204:207], a[148:151], a[56:59], v[204:207]\n"
"ds_write2_b32 v248, v50, v58 offset0:176 offset1:177\n"
"v_cvt_pk_bf16_f32 v51, v51, v55\n"
"v_cvt_pk_bf16_f32 v59, v59, v63\n"
"v_mfma_f32_16x16x32_bf16 v[204:207], a[152:155], a[52:55], v[204:207]\n"
"ds_write2_b32 v248, v51, v59 offset0:248 offset1:249\n"
"v_exp_f32 v195, v195\n"
"v_mfma_f32_16x16x32_bf16 v[204:207], a[156:159], a[48:51], v[204:207]\n"
"v_cvt_pk_bf16_f32 v217, v194, v195\n"
"v_smfmac_f32_16x16x64_bf16 a[232:235], a[248:251], v[208:215], v252\n"
"ds_read_b64_tr_b16 v[36:37], v249 offset:0\n"
"ds_read_b64_tr_b16 v[38:39], v249 offset:32\n"
"v_exp_f32 v196, v196\n"
"v_mfma_f32_16x16x32_bf16 v[64:67], v[208:211], a[160:163], v[64:67]\n"
"ds_read_b64_tr_b16 v[44:45], v249 offset:1152\n"
"ds_read_b64_tr_b16 v[46:47], v249 offset:1184\n"
"v_mfma_f32_16x16x32_bf16 v[64:67], v[212:215], a[164:167], v[64:67]\n"
"v_exp_f32 v197, v197\n"
"v_mfma_f32_16x16x32_bf16 v[68:71], v[212:215], a[168:171], v[68:71]\n"
"ds_read_b64_tr_b16 v[52:53], v249 offset:2304\n"
"ds_read_b64_tr_b16 v[54:55], v249 offset:2336\n"
"v_cvt_pk_bf16_f32 v218, v196, v197\n"
"v_mfma_f32_16x16x32_bf16 v[68:71], v[208:211], a[172:175], v[68:71]\n"
"ds_read_b64_tr_b16 v[60:61], v249 offset:3456\n"
"ds_read_b64_tr_b16 v[62:63], v249 offset:3488\n"
"v_exp_f32 v198, v198\n"
"v_mfma_f32_16x16x32_bf16 v[72:75], v[208:211], a[176:179], v[72:75]\n"
"v_mfma_f32_16x16x32_bf16 v[72:75], v[212:215], a[180:183], v[72:75]\n"
"v_exp_f32 v199, v199\n"
"v_mfma_f32_16x16x32_bf16 v[76:79], v[212:215], a[184:187], v[76:79]\n"
"v_cvt_pk_bf16_f32 v219, v198, v199\n"
"v_mfma_f32_16x16x32_bf16 v[76:79], v[208:211], a[188:191], v[76:79]\n"
"v_exp_f32 v200, v200\n"
"v_mfma_f32_16x16x32_bf16 v[80:83], v[208:211], a[192:195], v[80:83]\n"
"v_accvgpr_read_b32 v228, a232\n"
"v_mfma_f32_16x16x32_bf16 v[80:83], v[212:215], a[196:199], v[80:83]\n"
"s_waitcnt lgkmcnt(6)\n"
"v_exp_f32 v201, v201\n"
"s_add_u32 s58, s59, 4096\n"
"buffer_store_dwordx4 v[36:39], v250, s[48:51], s58 offen sc0 sc1 nt\n"
"v_accvgpr_read_b32 v229, a233\n"
"v_mfma_f32_16x16x32_bf16 v[84:87], v[212:215], a[200:203], v[84:87]\n"
"s_waitcnt lgkmcnt(4)\n"
"buffer_store_dwordx4 v[44:47], v250, s[48:51], s58 offen offset:1024 sc0 sc1 nt\n"
"v_add_f32 v228, v228, v229\n"
"v_mfma_f32_16x16x32_bf16 v[84:87], v[208:211], a[204:207], v[84:87]\n"
"v_exp_f32 v202, v202\n"
"v_add_f32 v255, v255, v228\n"
"v_mfma_f32_16x16x32_bf16 v[88:91], v[208:211], a[208:211], v[88:91]\n"
"s_waitcnt lgkmcnt(2)\n"
"buffer_store_dwordx4 v[52:55], v250, s[48:51], s58 offen offset:2048 sc0 sc1 nt\n"
"v_rcp_f32 v228, v228\n"
"v_mfma_f32_16x16x32_bf16 v[88:91], v[212:215], a[212:215], v[88:91]\n"
"ds_write_b32 v251, v228 offset:5120\n"
"v_exp_f32 v203, v203\n"
"v_mfma_f32_16x16x32_bf16 v[92:95], v[212:215], a[216:219], v[92:95]\n"
"s_waitcnt lgkmcnt(1)\n"
"buffer_store_dwordx4 v[60:63], v250, s[48:51], s58 offen offset:3072 sc0 sc1 nt\n"
"ds_read_b128 v[228:231], v254 offset:5120\n"
"v_mfma_f32_16x16x32_bf16 v[92:95], v[208:211], a[220:223], v[92:95]\n"
"v_mfma_f32_16x16x32_bf16 v[192:195], a[96:99], a[64:67], v[236:239]\n"
"v_exp_f32 v204, v204\n"
"s_waitcnt lgkmcnt(0)\n"
"v_mfma_f32_16x16x32_bf16 v[192:195], a[100:103], a[68:71], v[192:195]\n"
"v_cvt_pk_bf16_f32 v212, v200, v201\n"
"v_cvt_pk_bf16_f32 v213, v202, v203\n"
"v_pk_mul_f32 v[64:65], v[64:65], v[228:229]\n"
"v_pk_mul_f32 v[66:67], v[66:67], v[230:231]\n"
"v_pk_mul_f32 v[68:69], v[68:69], v[228:229]\n"
"v_pk_mul_f32 v[70:71], v[70:71], v[230:231]\n"
"v_pk_mul_f32 v[72:73], v[72:73], v[228:229]\n"
"v_pk_mul_f32 v[74:75], v[74:75], v[230:231]\n"
"v_pk_mul_f32 v[76:77], v[76:77], v[228:229]\n"
"v_pk_mul_f32 v[78:79], v[78:79], v[230:231]\n"
"v_mfma_f32_16x16x32_bf16 v[192:195], a[104:107], a[72:75], v[192:195]\n"
"v_exp_f32 v205, v205\n"
"v_cvt_pk_bf16_f32 v64, v64, v68\n"
"v_cvt_pk_bf16_f32 v72, v72, v76\n"
"v_mfma_f32_16x16x32_bf16 v[192:195], a[108:111], a[76:79], v[192:195]\n"
"v_cvt_pk_bf16_f32 v214, v204, v205\n"
"v_mfma_f32_16x16x32_bf16 v[196:199], a[112:115], a[76:79], v[236:239]\n"
"ds_write2_b32 v248, v64, v72 offset0:0 offset1:1\n"
"v_exp_f32 v206, v206\n"
"v_cvt_pk_bf16_f32 v65, v65, v69\n"
"v_cvt_pk_bf16_f32 v73, v73, v77\n"
"v_mfma_f32_16x16x32_bf16 v[196:199], a[116:119], a[72:75], v[196:199]\n"
"ds_write2_b32 v248, v65, v73 offset0:72 offset1:73\n"
"v_cvt_pk_bf16_f32 v66, v66, v70\n"
"v_cvt_pk_bf16_f32 v74, v74, v78\n"
"v_pk_mul_f32 v[80:81], v[80:81], v[228:229]\n"
"v_pk_mul_f32 v[82:83], v[82:83], v[230:231]\n"
"v_pk_mul_f32 v[84:85], v[84:85], v[228:229]\n"
"v_pk_mul_f32 v[86:87], v[86:87], v[230:231]\n"
"v_pk_mul_f32 v[88:89], v[88:89], v[228:229]\n"
"v_pk_mul_f32 v[90:91], v[90:91], v[230:231]\n"
"v_pk_mul_f32 v[92:93], v[92:93], v[228:229]\n"
"v_pk_mul_f32 v[94:95], v[94:95], v[230:231]\n"
"v_mfma_f32_16x16x32_bf16 v[196:199], a[120:123], a[68:71], v[196:199]\n"
"v_exp_f32 v207, v207\n"
"v_mfma_f32_16x16x32_bf16 v[196:199], a[124:127], a[64:67], v[196:199]\n"
"ds_write2_b32 v248, v66, v74 offset0:144 offset1:145\n"
"v_cvt_pk_bf16_f32 v215, v206, v207\n"
"v_cvt_pk_bf16_f32 v67, v67, v71\n"
"v_cvt_pk_bf16_f32 v75, v75, v79\n"
"v_mfma_f32_16x16x32_bf16 v[200:203], a[128:131], a[64:67], v[236:239]\n"
"ds_write2_b32 v248, v67, v75 offset0:216 offset1:217\n"
"v_exp_f32 v192, v192\n"
"v_cvt_pk_bf16_f32 v80, v80, v84\n"
"v_cvt_pk_bf16_f32 v88, v88, v92\n"
"v_mfma_f32_16x16x32_bf16 v[200:203], a[132:135], a[68:71], v[200:203]\n"
"v_mfma_f32_16x16x32_bf16 v[200:203], a[136:139], a[72:75], v[200:203]\n"
"ds_write2_b32 v248, v80, v88 offset0:32 offset1:33\n"
"v_exp_f32 v193, v193\n"
"v_cvt_pk_bf16_f32 v81, v81, v85\n"
"v_cvt_pk_bf16_f32 v89, v89, v93\n"
"v_mfma_f32_16x16x32_bf16 v[200:203], a[140:143], a[76:79], v[200:203]\n"
"ds_write2_b32 v248, v81, v89 offset0:104 offset1:105\n"
"v_cvt_pk_bf16_f32 v208, v192, v193\n"
"v_cvt_pk_bf16_f32 v82, v82, v86\n"
"v_cvt_pk_bf16_f32 v90, v90, v94\n"
"v_mfma_f32_16x16x32_bf16 v[204:207], a[144:147], a[76:79], v[236:239]\n"
"v_exp_f32 v194, v194\n"
"v_mfma_f32_16x16x32_bf16 v[204:207], a[148:151], a[72:75], v[204:207]\n"
"ds_write2_b32 v248, v82, v90 offset0:176 offset1:177\n"
"v_cvt_pk_bf16_f32 v83, v83, v87\n"
"v_cvt_pk_bf16_f32 v91, v91, v95\n"
"v_mfma_f32_16x16x32_bf16 v[204:207], a[152:155], a[68:71], v[204:207]\n"
"ds_write2_b32 v248, v83, v91 offset0:248 offset1:249\n"
"v_exp_f32 v195, v195\n"
"v_mfma_f32_16x16x32_bf16 v[204:207], a[156:159], a[64:67], v[204:207]\n"
"v_cvt_pk_bf16_f32 v209, v194, v195\n"
"v_smfmac_f32_16x16x64_bf16 a[236:239], a[248:251], v[212:219], v252\n"
"ds_read_b64_tr_b16 v[68:69], v249 offset:0\n"
"ds_read_b64_tr_b16 v[70:71], v249 offset:32\n"
"v_exp_f32 v196, v196\n"
"v_mfma_f32_16x16x32_bf16 v[96:99], v[216:219], a[160:163], v[96:99]\n"
"ds_read_b64_tr_b16 v[76:77], v249 offset:1152\n"
"ds_read_b64_tr_b16 v[78:79], v249 offset:1184\n"
"v_mfma_f32_16x16x32_bf16 v[96:99], v[212:215], a[164:167], v[96:99]\n"
"v_exp_f32 v197, v197\n"
"v_mfma_f32_16x16x32_bf16 v[100:103], v[212:215], a[168:171], v[100:103]\n"
"ds_read_b64_tr_b16 v[84:85], v249 offset:2304\n"
"ds_read_b64_tr_b16 v[86:87], v249 offset:2336\n"
"v_cvt_pk_bf16_f32 v210, v196, v197\n"
"v_mfma_f32_16x16x32_bf16 v[100:103], v[216:219], a[172:175], v[100:103]\n"
"ds_read_b64_tr_b16 v[92:93], v249 offset:3456\n"
"ds_read_b64_tr_b16 v[94:95], v249 offset:3488\n"
"v_exp_f32 v198, v198\n"
"v_mfma_f32_16x16x32_bf16 v[104:107], v[216:219], a[176:179], v[104:107]\n"
"v_mfma_f32_16x16x32_bf16 v[104:107], v[212:215], a[180:183], v[104:107]\n"
"v_exp_f32 v199, v199\n"
"v_mfma_f32_16x16x32_bf16 v[108:111], v[212:215], a[184:187], v[108:111]\n"
"v_cvt_pk_bf16_f32 v211, v198, v199\n"
"v_mfma_f32_16x16x32_bf16 v[108:111], v[216:219], a[188:191], v[108:111]\n"
"v_exp_f32 v200, v200\n"
"v_mfma_f32_16x16x32_bf16 v[112:115], v[216:219], a[192:195], v[112:115]\n"
"v_accvgpr_read_b32 v232, a236\n"
"v_mfma_f32_16x16x32_bf16 v[112:115], v[212:215], a[196:199], v[112:115]\n"
"s_waitcnt lgkmcnt(6)\n"
"v_exp_f32 v201, v201\n"
"s_add_u32 s58, s59, 8192\n"
"buffer_store_dwordx4 v[68:71], v250, s[48:51], s58 offen sc0 sc1 nt\n"
"v_accvgpr_read_b32 v233, a237\n"
"v_mfma_f32_16x16x32_bf16 v[116:119], v[212:215], a[200:203], v[116:119]\n"
"s_waitcnt lgkmcnt(4)\n"
"buffer_store_dwordx4 v[76:79], v250, s[48:51], s58 offen offset:1024 sc0 sc1 nt\n"
"v_add_f32 v232, v232, v233\n"
"v_mfma_f32_16x16x32_bf16 v[116:119], v[216:219], a[204:207], v[116:119]\n"
"v_exp_f32 v202, v202\n"
"v_add_f32 v255, v255, v232\n"
"v_mfma_f32_16x16x32_bf16 v[120:123], v[216:219], a[208:211], v[120:123]\n"
"s_waitcnt lgkmcnt(2)\n"
"buffer_store_dwordx4 v[84:87], v250, s[48:51], s58 offen offset:2048 sc0 sc1 nt\n"
"v_rcp_f32 v232, v232\n"
"v_mfma_f32_16x16x32_bf16 v[120:123], v[212:215], a[212:215], v[120:123]\n"
"ds_write_b32 v251, v232 offset:5120\n"
"v_exp_f32 v203, v203\n"
"v_mfma_f32_16x16x32_bf16 v[124:127], v[212:215], a[216:219], v[124:127]\n"
"s_waitcnt lgkmcnt(1)\n"
"buffer_store_dwordx4 v[92:95], v250, s[48:51], s58 offen offset:3072 sc0 sc1 nt\n"
"ds_read_b128 v[232:235], v254 offset:5120\n"
"v_mfma_f32_16x16x32_bf16 v[124:127], v[216:219], a[220:223], v[124:127]\n"
"v_mfma_f32_16x16x32_bf16 v[192:195], a[96:99], a[80:83], v[240:243]\n"
"v_exp_f32 v204, v204\n"
"s_waitcnt lgkmcnt(0)\n"
"v_mfma_f32_16x16x32_bf16 v[192:195], a[100:103], a[84:87], v[192:195]\n"
"v_cvt_pk_bf16_f32 v212, v200, v201\n"
"v_cvt_pk_bf16_f32 v213, v202, v203\n"
"v_pk_mul_f32 v[96:97], v[96:97], v[232:233]\n"
"v_pk_mul_f32 v[98:99], v[98:99], v[234:235]\n"
"v_pk_mul_f32 v[100:101], v[100:101], v[232:233]\n"
"v_pk_mul_f32 v[102:103], v[102:103], v[234:235]\n"
"v_pk_mul_f32 v[104:105], v[104:105], v[232:233]\n"
"v_pk_mul_f32 v[106:107], v[106:107], v[234:235]\n"
"v_pk_mul_f32 v[108:109], v[108:109], v[232:233]\n"
"v_pk_mul_f32 v[110:111], v[110:111], v[234:235]\n"
"v_mfma_f32_16x16x32_bf16 v[192:195], a[104:107], a[88:91], v[192:195]\n"
"v_exp_f32 v205, v205\n"
"v_cvt_pk_bf16_f32 v96, v96, v100\n"
"v_cvt_pk_bf16_f32 v104, v104, v108\n"
"v_mfma_f32_16x16x32_bf16 v[192:195], a[108:111], a[92:95], v[192:195]\n"
"v_cvt_pk_bf16_f32 v214, v204, v205\n"
"v_mfma_f32_16x16x32_bf16 v[196:199], a[112:115], a[92:95], v[240:243]\n"
"ds_write2_b32 v248, v96, v104 offset0:0 offset1:1\n"
"v_exp_f32 v206, v206\n"
"v_cvt_pk_bf16_f32 v97, v97, v101\n"
"v_cvt_pk_bf16_f32 v105, v105, v109\n"
"v_mfma_f32_16x16x32_bf16 v[196:199], a[116:119], a[88:91], v[196:199]\n"
"ds_write2_b32 v248, v97, v105 offset0:72 offset1:73\n"
"v_cvt_pk_bf16_f32 v98, v98, v102\n"
"v_cvt_pk_bf16_f32 v106, v106, v110\n"
"v_pk_mul_f32 v[112:113], v[112:113], v[232:233]\n"
"v_pk_mul_f32 v[114:115], v[114:115], v[234:235]\n"
"v_pk_mul_f32 v[116:117], v[116:117], v[232:233]\n"
"v_pk_mul_f32 v[118:119], v[118:119], v[234:235]\n"
"v_pk_mul_f32 v[120:121], v[120:121], v[232:233]\n"
"v_pk_mul_f32 v[122:123], v[122:123], v[234:235]\n"
"v_pk_mul_f32 v[124:125], v[124:125], v[232:233]\n"
"v_pk_mul_f32 v[126:127], v[126:127], v[234:235]\n"
"v_mfma_f32_16x16x32_bf16 v[196:199], a[120:123], a[84:87], v[196:199]\n"
"v_exp_f32 v207, v207\n"
"v_mfma_f32_16x16x32_bf16 v[196:199], a[124:127], a[80:83], v[196:199]\n"
"ds_write2_b32 v248, v98, v106 offset0:144 offset1:145\n"
"v_cvt_pk_bf16_f32 v215, v206, v207\n"
"v_cvt_pk_bf16_f32 v99, v99, v103\n"
"v_cvt_pk_bf16_f32 v107, v107, v111\n"
"v_mfma_f32_16x16x32_bf16 v[200:203], a[128:131], a[80:83], v[240:243]\n"
"ds_write2_b32 v248, v99, v107 offset0:216 offset1:217\n"
"v_exp_f32 v192, v192\n"
"v_cvt_pk_bf16_f32 v112, v112, v116\n"
"v_cvt_pk_bf16_f32 v120, v120, v124\n"
"v_mfma_f32_16x16x32_bf16 v[200:203], a[132:135], a[84:87], v[200:203]\n"
"v_mfma_f32_16x16x32_bf16 v[200:203], a[136:139], a[88:91], v[200:203]\n"
"ds_write2_b32 v248, v112, v120 offset0:32 offset1:33\n"
"v_exp_f32 v193, v193\n"
"v_cvt_pk_bf16_f32 v113, v113, v117\n"
"v_cvt_pk_bf16_f32 v121, v121, v125\n"
"v_mfma_f32_16x16x32_bf16 v[200:203], a[140:143], a[92:95], v[200:203]\n"
"ds_write2_b32 v248, v113, v121 offset0:104 offset1:105\n"
"v_cvt_pk_bf16_f32 v216, v192, v193\n"
"v_cvt_pk_bf16_f32 v114, v114, v118\n"
"v_cvt_pk_bf16_f32 v122, v122, v126\n"
"v_mfma_f32_16x16x32_bf16 v[204:207], a[144:147], a[92:95], v[240:243]\n"
"v_exp_f32 v194, v194\n"
"v_mfma_f32_16x16x32_bf16 v[204:207], a[148:151], a[88:91], v[204:207]\n"
"ds_write2_b32 v248, v114, v122 offset0:176 offset1:177\n"
"v_cvt_pk_bf16_f32 v115, v115, v119\n"
"v_cvt_pk_bf16_f32 v123, v123, v127\n"
"v_mfma_f32_16x16x32_bf16 v[204:207], a[152:155], a[84:87], v[204:207]\n"
"ds_write2_b32 v248, v115, v123 offset0:248 offset1:249\n"
"v_exp_f32 v195, v195\n"
"v_mfma_f32_16x16x32_bf16 v[204:207], a[156:159], a[80:83], v[204:207]\n"
"v_cvt_pk_bf16_f32 v217, v194, v195\n"
"v_smfmac_f32_16x16x64_bf16 a[240:243], a[248:251], v[208:215], v252\n"
"ds_read_b64_tr_b16 v[100:101], v249 offset:0\n"
"ds_read_b64_tr_b16 v[102:103], v249 offset:32\n"
"v_exp_f32 v196, v196\n"
"v_mfma_f32_16x16x32_bf16 v[128:131], v[208:211], a[160:163], v[128:131]\n"
"ds_read_b64_tr_b16 v[108:109], v249 offset:1152\n"
"ds_read_b64_tr_b16 v[110:111], v249 offset:1184\n"
"v_mfma_f32_16x16x32_bf16 v[128:131], v[212:215], a[164:167], v[128:131]\n"
"v_exp_f32 v197, v197\n"
"v_mfma_f32_16x16x32_bf16 v[132:135], v[212:215], a[168:171], v[132:135]\n"
"ds_read_b64_tr_b16 v[116:117], v249 offset:2304\n"
"ds_read_b64_tr_b16 v[118:119], v249 offset:2336\n"
"v_cvt_pk_bf16_f32 v218, v196, v197\n"
"v_mfma_f32_16x16x32_bf16 v[132:135], v[208:211], a[172:175], v[132:135]\n"
"ds_read_b64_tr_b16 v[124:125], v249 offset:3456\n"
"ds_read_b64_tr_b16 v[126:127], v249 offset:3488\n"
"v_exp_f32 v198, v198\n"
"v_mfma_f32_16x16x32_bf16 v[136:139], v[208:211], a[176:179], v[136:139]\n"
"v_mfma_f32_16x16x32_bf16 v[136:139], v[212:215], a[180:183], v[136:139]\n"
"v_exp_f32 v199, v199\n"
"v_mfma_f32_16x16x32_bf16 v[140:143], v[212:215], a[184:187], v[140:143]\n"
"v_cvt_pk_bf16_f32 v219, v198, v199\n"
"v_mfma_f32_16x16x32_bf16 v[140:143], v[208:211], a[188:191], v[140:143]\n"
"v_exp_f32 v200, v200\n"
"v_mfma_f32_16x16x32_bf16 v[144:147], v[208:211], a[192:195], v[144:147]\n"
"v_accvgpr_read_b32 v236, a240\n"
"v_mfma_f32_16x16x32_bf16 v[144:147], v[212:215], a[196:199], v[144:147]\n"
"s_waitcnt lgkmcnt(6)\n"
"v_exp_f32 v201, v201\n"
"s_add_u32 s58, s59, 12288\n"
"buffer_store_dwordx4 v[100:103], v250, s[48:51], s58 offen sc0 sc1 nt\n"
"v_accvgpr_read_b32 v237, a241\n"
"v_mfma_f32_16x16x32_bf16 v[148:151], v[212:215], a[200:203], v[148:151]\n"
"s_waitcnt lgkmcnt(4)\n"
"buffer_store_dwordx4 v[108:111], v250, s[48:51], s58 offen offset:1024 sc0 sc1 nt\n"
"v_add_f32 v236, v236, v237\n"
"v_mfma_f32_16x16x32_bf16 v[148:151], v[208:211], a[204:207], v[148:151]\n"
"v_exp_f32 v202, v202\n"
"v_add_f32 v255, v255, v236\n"
"v_mfma_f32_16x16x32_bf16 v[152:155], v[208:211], a[208:211], v[152:155]\n"
"s_waitcnt lgkmcnt(2)\n"
"buffer_store_dwordx4 v[116:119], v250, s[48:51], s58 offen offset:2048 sc0 sc1 nt\n"
"v_rcp_f32 v236, v236\n"
"v_mfma_f32_16x16x32_bf16 v[152:155], v[212:215], a[212:215], v[152:155]\n"
"ds_write_b32 v251, v236 offset:5120\n"
"v_exp_f32 v203, v203\n"
"v_mfma_f32_16x16x32_bf16 v[156:159], v[212:215], a[216:219], v[156:159]\n"
"s_waitcnt lgkmcnt(1)\n"
"buffer_store_dwordx4 v[124:127], v250, s[48:51], s58 offen offset:3072 sc0 sc1 nt\n"
"ds_read_b128 v[236:239], v254 offset:5120\n"
"v_mfma_f32_16x16x32_bf16 v[156:159], v[208:211], a[220:223], v[156:159]\n"
"v_exp_f32 v204, v204\n"
"v_exp_f32 v205, v205\n"
"v_cvt_pk_bf16_f32 v212, v200, v201\n"
"v_cvt_pk_bf16_f32 v213, v202, v203\n"
"v_exp_f32 v206, v206\n"
"v_exp_f32 v207, v207\n"
"v_cvt_pk_bf16_f32 v214, v204, v205\n"
"v_cvt_pk_bf16_f32 v215, v206, v207\n"
"s_nop 1\n"
"v_smfmac_f32_16x16x64_bf16 a[244:247], a[248:251], v[212:219], v252\n"
"s_waitcnt lgkmcnt(0)\n"
"v_pk_mul_f32 v[128:129], v[128:129], v[236:237]\n"
"v_pk_mul_f32 v[130:131], v[130:131], v[238:239]\n"
"v_pk_mul_f32 v[132:133], v[132:133], v[236:237]\n"
"v_pk_mul_f32 v[134:135], v[134:135], v[238:239]\n"
"v_pk_mul_f32 v[136:137], v[136:137], v[236:237]\n"
"v_pk_mul_f32 v[138:139], v[138:139], v[238:239]\n"
"v_pk_mul_f32 v[140:141], v[140:141], v[236:237]\n"
"v_pk_mul_f32 v[142:143], v[142:143], v[238:239]\n"
"v_mfma_f32_16x16x32_bf16 v[160:163], v[216:219], a[160:163], v[160:163]\n"
"v_cvt_pk_bf16_f32 v128, v128, v132\n"
"v_cvt_pk_bf16_f32 v136, v136, v140\n"
"v_mfma_f32_16x16x32_bf16 v[160:163], v[212:215], a[164:167], v[160:163]\n"
"ds_write2_b32 v248, v128, v136 offset0:0 offset1:1\n"
"v_cvt_pk_bf16_f32 v129, v129, v133\n"
"v_cvt_pk_bf16_f32 v137, v137, v141\n"
"v_mfma_f32_16x16x32_bf16 v[164:167], v[212:215], a[168:171], v[164:167]\n"
"ds_write2_b32 v248, v129, v137 offset0:72 offset1:73\n"
"v_cvt_pk_bf16_f32 v130, v130, v134\n"
"v_cvt_pk_bf16_f32 v138, v138, v142\n"
"v_pk_mul_f32 v[144:145], v[144:145], v[236:237]\n"
"v_pk_mul_f32 v[146:147], v[146:147], v[238:239]\n"
"v_pk_mul_f32 v[148:149], v[148:149], v[236:237]\n"
"v_pk_mul_f32 v[150:151], v[150:151], v[238:239]\n"
"v_pk_mul_f32 v[152:153], v[152:153], v[236:237]\n"
"v_pk_mul_f32 v[154:155], v[154:155], v[238:239]\n"
"v_pk_mul_f32 v[156:157], v[156:157], v[236:237]\n"
"v_pk_mul_f32 v[158:159], v[158:159], v[238:239]\n"
"v_mfma_f32_16x16x32_bf16 v[164:167], v[216:219], a[172:175], v[164:167]\n"
"ds_write2_b32 v248, v130, v138 offset0:144 offset1:145\n"
"v_cvt_pk_bf16_f32 v131, v131, v135\n"
"v_cvt_pk_bf16_f32 v139, v139, v143\n"
"ds_write2_b32 v248, v131, v139 offset0:216 offset1:217\n"
"v_cvt_pk_bf16_f32 v144, v144, v148\n"
"v_cvt_pk_bf16_f32 v152, v152, v156\n"
"v_mfma_f32_16x16x32_bf16 v[168:171], v[216:219], a[176:179], v[168:171]\n"
"ds_write2_b32 v248, v144, v152 offset0:32 offset1:33\n"
"v_cvt_pk_bf16_f32 v145, v145, v149\n"
"v_cvt_pk_bf16_f32 v153, v153, v157\n"
"v_mfma_f32_16x16x32_bf16 v[168:171], v[212:215], a[180:183], v[168:171]\n"
"ds_write2_b32 v248, v145, v153 offset0:104 offset1:105\n"
"v_cvt_pk_bf16_f32 v146, v146, v150\n"
"v_cvt_pk_bf16_f32 v154, v154, v158\n"
"v_mfma_f32_16x16x32_bf16 v[172:175], v[212:215], a[184:187], v[172:175]\n"
"ds_write2_b32 v248, v146, v154 offset0:176 offset1:177\n"
"v_cvt_pk_bf16_f32 v147, v147, v151\n"
"v_cvt_pk_bf16_f32 v155, v155, v159\n"
"v_mfma_f32_16x16x32_bf16 v[172:175], v[216:219], a[188:191], v[172:175]\n"
"ds_write2_b32 v248, v147, v155 offset0:248 offset1:249\n"
"ds_read_b64_tr_b16 v[132:133], v249 offset:0\n"
"ds_read_b64_tr_b16 v[134:135], v249 offset:32\n"
"v_mfma_f32_16x16x32_bf16 v[176:179], v[216:219], a[192:195], v[176:179]\n"
"ds_read_b64_tr_b16 v[140:141], v249 offset:1152\n"
"ds_read_b64_tr_b16 v[142:143], v249 offset:1184\n"
"v_accvgpr_read_b32 v240, a244\n"
"v_mfma_f32_16x16x32_bf16 v[176:179], v[212:215], a[196:199], v[176:179]\n"
"ds_read_b64_tr_b16 v[148:149], v249 offset:2304\n"
"ds_read_b64_tr_b16 v[150:151], v249 offset:2336\n"
"v_accvgpr_read_b32 v241, a245\n"
"v_mfma_f32_16x16x32_bf16 v[180:183], v[212:215], a[200:203], v[180:183]\n"
"ds_read_b64_tr_b16 v[156:157], v249 offset:3456\n"
"ds_read_b64_tr_b16 v[158:159], v249 offset:3488\n"
"v_add_f32 v240, v240, v241\n"
"v_mfma_f32_16x16x32_bf16 v[180:183], v[216:219], a[204:207], v[180:183]\n"
"v_add_f32 v255, v255, v240\n"
"v_mfma_f32_16x16x32_bf16 v[184:187], v[216:219], a[208:211], v[184:187]\n"
"v_rcp_f32 v240, v240\n"
"v_mfma_f32_16x16x32_bf16 v[184:187], v[212:215], a[212:215], v[184:187]\n"
"ds_write_b32 v251, v240 offset:5120\n"
"v_mfma_f32_16x16x32_bf16 v[188:191], v[212:215], a[216:219], v[188:191]\n"
"ds_read_b128 v[240:243], v254 offset:5120\n"
"v_mfma_f32_16x16x32_bf16 v[188:191], v[216:219], a[220:223], v[188:191]\n"
"s_add_u32 s58, s59, 16384\n"
"s_waitcnt lgkmcnt(8)\n"
"buffer_store_dwordx4 v[132:135], v250, s[48:51], s58 offen sc0 sc1 nt\n"
"s_waitcnt lgkmcnt(0)\n"
"v_pk_mul_f32 v[160:161], v[160:161], v[240:241]\n"
"v_pk_mul_f32 v[162:163], v[162:163], v[242:243]\n"
"v_pk_mul_f32 v[164:165], v[164:165], v[240:241]\n"
"v_pk_mul_f32 v[166:167], v[166:167], v[242:243]\n"
"v_pk_mul_f32 v[168:169], v[168:169], v[240:241]\n"
"v_pk_mul_f32 v[170:171], v[170:171], v[242:243]\n"
"v_pk_mul_f32 v[172:173], v[172:173], v[240:241]\n"
"v_pk_mul_f32 v[174:175], v[174:175], v[242:243]\n"
"v_cvt_pk_bf16_f32 v160, v160, v164\n"
"v_cvt_pk_bf16_f32 v168, v168, v172\n"
"ds_write2_b32 v248, v160, v168 offset0:0 offset1:1\n"
"buffer_store_dwordx4 v[140:143], v250, s[48:51], s58 offen offset:1024 sc0 sc1 nt\n"
"v_cvt_pk_bf16_f32 v161, v161, v165\n"
"v_cvt_pk_bf16_f32 v169, v169, v173\n"
"ds_write2_b32 v248, v161, v169 offset0:72 offset1:73\n"
"v_cvt_pk_bf16_f32 v162, v162, v166\n"
"v_cvt_pk_bf16_f32 v170, v170, v174\n"
"v_pk_mul_f32 v[176:177], v[176:177], v[240:241]\n"
"v_pk_mul_f32 v[178:179], v[178:179], v[242:243]\n"
"v_pk_mul_f32 v[180:181], v[180:181], v[240:241]\n"
"v_pk_mul_f32 v[182:183], v[182:183], v[242:243]\n"
"v_pk_mul_f32 v[184:185], v[184:185], v[240:241]\n"
"v_pk_mul_f32 v[186:187], v[186:187], v[242:243]\n"
"v_pk_mul_f32 v[188:189], v[188:189], v[240:241]\n"
"buffer_store_dwordx4 v[148:151], v250, s[48:51], s58 offen offset:2048 sc0 sc1 nt\n"
"v_pk_mul_f32 v[190:191], v[190:191], v[242:243]\n"
"ds_write2_b32 v248, v162, v170 offset0:144 offset1:145\n"
"v_cvt_pk_bf16_f32 v163, v163, v167\n"
"v_cvt_pk_bf16_f32 v171, v171, v175\n"
"ds_write2_b32 v248, v163, v171 offset0:216 offset1:217\n"
"v_cvt_pk_bf16_f32 v176, v176, v180\n"
"v_cvt_pk_bf16_f32 v184, v184, v188\n"
"ds_write2_b32 v248, v176, v184 offset0:32 offset1:33\n"
"v_cvt_pk_bf16_f32 v177, v177, v181\n"
"v_cvt_pk_bf16_f32 v185, v185, v189\n"
"ds_write2_b32 v248, v177, v185 offset0:104 offset1:105\n"
"v_cvt_pk_bf16_f32 v178, v178, v182\n"
"buffer_store_dwordx4 v[156:159], v250, s[48:51], s58 offen offset:3072 sc0 sc1 nt\n"
"v_cvt_pk_bf16_f32 v186, v186, v190\n"
"ds_write2_b32 v248, v178, v186 offset0:176 offset1:177\n"
"v_cvt_pk_bf16_f32 v179, v179, v183\n"
"v_cvt_pk_bf16_f32 v187, v187, v191\n"
"ds_write2_b32 v248, v179, v187 offset0:248 offset1:249\n"
"ds_read_b64_tr_b16 v[164:165], v249 offset:0\n"
"ds_read_b64_tr_b16 v[166:167], v249 offset:32\n"
"ds_read_b64_tr_b16 v[172:173], v249 offset:1152\n"
"ds_read_b64_tr_b16 v[174:175], v249 offset:1184\n"
"ds_read_b64_tr_b16 v[180:181], v249 offset:2304\n"
"ds_read_b64_tr_b16 v[182:183], v249 offset:2336\n"
"ds_read_b64_tr_b16 v[188:189], v249 offset:3456\n"
"ds_read_b64_tr_b16 v[190:191], v249 offset:3488\n"
"s_add_u32 s58, s59, 20480\n"
"s_waitcnt lgkmcnt(6)\n"
"buffer_store_dwordx4 v[164:167], v250, s[48:51], s58 offen sc0 sc1 nt\n"
"s_waitcnt lgkmcnt(4)\n"
"buffer_store_dwordx4 v[172:175], v250, s[48:51], s58 offen offset:1024 sc0 sc1 nt\n"
"s_waitcnt lgkmcnt(2)\n"
"buffer_store_dwordx4 v[180:183], v250, s[48:51], s58 offen offset:2048 sc0 sc1 nt\n"
"s_waitcnt lgkmcnt(0)\n"
"buffer_store_dwordx4 v[188:191], v250, s[48:51], s58 offen offset:3072 sc0 sc1 nt\n"
"s_branch L_join_%=\n"
"L_late6_2_%=:\n"
"ds_read_b32 v255, v254\n"
"s_waitcnt lgkmcnt(0)\n"
"v_cmp_gt_u32 vcc, s27, v255\n"
"s_nop 0\n"
"s_cbranch_vccnz L_late6_2_%=\n"
"s_branch L_back6_2_%=\n"
"L_late6_3_%=:\n"
"ds_read_b32 v255, v254\n"
"s_waitcnt lgkmcnt(0)\n"
"v_cmp_gt_u32 vcc, s27, v255\n"
"s_nop 0\n"
"s_cbranch_vccnz L_late6_3_%=\n"
"s_branch L_back6_3_%=\n"
"L_late6_4_%=:\n"
"ds_read_b32 v255, v254\n"
"s_waitcnt lgkmcnt(0)\n"
"v_cmp_gt_u32 vcc, s27, v255\n"
"s_nop 0\n"
"s_cbranch_vccnz L_late6_4_%=\n"
"s_branch L_back6_4_%=\n"
"L_late6_5_%=:\n"
"ds_read_b32 v255, v254\n"
"s_waitcnt lgkmcnt(0)\n"
"v_cmp_gt_u32 vcc, s27, v255\n"
"s_nop 0\n"
"s_cbranch_vccnz L_late6_5_%=\n"
"s_branch L_back6_5_%=\n"
"L_late6_6_%=:\n"
"ds_read_b32 v255, v254\n"
"s_waitcnt lgkmcnt(0)\n"
"v_cmp_gt_u32 vcc, s27, v255\n"
"s_nop 0\n"
"s_cbranch_vccnz L_late6_6_%=\n"
"s_branch L_back6_6_%=\n"
"L_late6_13_%=:\n"
"ds_read_b32 v255, v254\n"
"s_waitcnt lgkmcnt(0)\n"
"v_cmp_gt_u32 vcc, s27, v255\n"
"s_nop 0\n"
"s_cbranch_vccnz L_late6_13_%=\n"
"s_branch L_back6_13_%=\n"
"L_late6_14_%=:\n"
"ds_read_b32 v255, v254\n"
"s_waitcnt lgkmcnt(0)\n"
"v_cmp_gt_u32 vcc, s27, v255\n"
"s_nop 0\n"
"s_cbranch_vccnz L_late6_14_%=\n"
"s_branch L_back6_14_%=\n"
"L_late6_15_%=:\n"
"ds_read_b32 v255, v254\n"
"s_waitcnt lgkmcnt(0)\n"
"v_cmp_gt_u32 vcc, s27, v255\n"
"s_nop 0\n"
"s_cbranch_vccnz L_late6_15_%=\n"
"s_branch L_back6_15_%=\n"
"L_late5_2_%=:\n"
"s_sleep 1\n"
"ds_read_b32 v255, v254\n"
"s_waitcnt lgkmcnt(0)\n"
"v_cmp_gt_u32 vcc, s27, v255\n"
"s_nop 0\n"
"s_cbranch_vccnz L_late5_2_%=\n"
"s_branch L_back5_2_%=\n"
"L_late5_3_%=:\n"
"s_sleep 1\n"
"ds_read_b32 v255, v254\n"
"s_waitcnt lgkmcnt(0)\n"
"v_cmp_gt_u32 vcc, s27, v255\n"
"s_nop 0\n"
"s_cbranch_vccnz L_late5_3_%=\n"
"s_branch L_back5_3_%=\n"
"L_late5_4_%=:\n"
"s_sleep 1\n"
"ds_read_b32 v255, v254\n"
"s_waitcnt lgkmcnt(0)\n"
"v_cmp_gt_u32 vcc, s27, v255\n"
"s_nop 0\n"
"s_cbranch_vccnz L_late5_4_%=\n"
"s_branch L_back5_4_%=\n"
"L_late5_5_%=:\n"
"s_sleep 1\n"
"ds_read_b32 v255, v254\n"
"s_waitcnt lgkmcnt(0)\n"
"v_cmp_gt_u32 vcc, s27, v255\n"
"s_nop 0\n"
"s_cbranch_vccnz L_late5_5_%=\n"
"s_branch L_back5_5_%=\n"
"L_late5_6_%=:\n"
"s_sleep 1\n"
"ds_read_b32 v255, v254\n"
"s_waitcnt lgkmcnt(0)\n"
"v_cmp_gt_u32 vcc, s27, v255\n"
"s_nop 0\n"
"s_cbranch_vccnz L_late5_6_%=\n"
"s_branch L_back5_6_%=\n"
"L_late5_13_%=:\n"
"s_sleep 1\n"
"ds_read_b32 v255, v254\n"
"s_waitcnt lgkmcnt(0)\n"
"v_cmp_gt_u32 vcc, s27, v255\n"
"s_nop 0\n"
"s_cbranch_vccnz L_late5_13_%=\n"
"s_branch L_back5_13_%=\n"
"L_late5_14_%=:\n"
"s_sleep 1\n"
"ds_read_b32 v255, v254\n"
"s_waitcnt lgkmcnt(0)\n"
"v_cmp_gt_u32 vcc, s27, v255\n"
"s_nop 0\n"
"s_cbranch_vccnz L_late5_14_%=\n"
"s_branch L_back5_14_%=\n"
"L_late5_15_%=:\n"
"s_sleep 1\n"
"ds_read_b32 v255, v254\n"
"s_waitcnt lgkmcnt(0)\n"
"v_cmp_gt_u32 vcc, s27, v255\n"
"s_nop 0\n"
"s_cbranch_vccnz L_late5_15_%=\n"
"s_branch L_back5_15_%=\n"
".p2align 6\n"
"L_w3_%=:\n"
"s_waitcnt vmcnt(4)\n"
"s_barrier\n"
"ds_read_b128 a[96:99], v244 offset:0\n"
"ds_read_b128 a[100:103], v245 offset:0\n"
"ds_read_b128 a[104:107], v244 offset:1024\n"
"ds_read_b128 a[108:111], v245 offset:1024\n"
"ds_read_b128 a[112:115], v245 offset:9216\n"
"ds_read_b128 a[116:119], v244 offset:9216\n"
"ds_read_b128 a[120:123], v245 offset:8192\n"
"ds_read_b128 a[124:127], v244 offset:8192\n"
"ds_read_b128 a[128:131], v244 offset:16384\n"
"ds_read_b128 a[132:135], v245 offset:16384\n"
"ds_read_b128 a[136:139], v244 offset:17408\n"
"ds_read_b128 a[140:143], v245 offset:17408\n"
"ds_read_b128 a[144:147], v245 offset:25600\n"
"ds_read_b128 a[148:151], v244 offset:25600\n"
"ds_read_b128 a[152:155], v245 offset:24576\n"
"ds_read_b128 a[156:159], v244 offset:24576\n"
"s_add_u32 m0, s53, 81920\n"
"s_nop 0\n"
"buffer_load_dwordx4 v136, s[44:47], s54 offen nt lds\n"
"buffer_load_dwordx4 v137, s[44:47], s54 offen offset:1024 nt lds\n"
"buffer_load_dwordx4 v138, s[44:47], s54 offen offset:2048 nt lds\n"
"buffer_load_dwordx4 v139, s[44:47], s54 offen offset:3072 nt lds\n"
"s_add_u32 s54, s54, 4096\n"
"s_waitcnt vmcnt(4)\n"
"s_mov_b32 m0, s53\n"
"s_add_u32 s24, s52, 0\n"
"buffer_load_dwordx4 v250, s[40:43], s24 offen offset:2048 nt lds\n"
"buffer_load_dwordx4 v251, s[40:43], s24 offen offset:3072 nt lds\n"
"s_add_u32 m0, s53, 16384\n"
"s_add_u32 s24, s52, 8192\n"
"buffer_load_dwordx4 v250, s[40:43], s24 offen offset:2048 nt lds\n"
"buffer_load_dwordx4 v251, s[40:43], s24 offen offset:3072 nt lds\n"
"ds_read_b128 v[160:163], v134 offset:0\n"
"ds_read_b128 v[164:167], v135 offset:0\n"
"ds_read_b128 v[168:171], v134 offset:1024\n"
"ds_read_b128 v[172:175], v135 offset:1024\n"
"s_waitcnt lgkmcnt(3)\n"
"v_lshlrev_b32 v176, 16, v160\n"
"v_and_b32 v177, 0xffff0000, v160\n"
"v_lshlrev_b32 v178, 16, v161\n"
"v_and_b32 v179, 0xffff0000, v161\n"
"v_lshlrev_b32 v180, 16, v162\n"
"v_and_b32 v181, 0xffff0000, v162\n"
"v_lshlrev_b32 v182, 16, v163\n"
"v_and_b32 v183, 0xffff0000, v163\n"
"v_pk_mul_f32 v[176:177], v[176:177], s[56:57]\n"
"v_pk_mul_f32 v[178:179], v[178:179], s[56:57]\n"
"v_pk_mul_f32 v[180:181], v[180:181], s[56:57]\n"
"v_pk_mul_f32 v[182:183], v[182:183], s[56:57]\n"
"v_cvt_pk_bf16_f32 v160, v176, v177\n"
"v_cvt_pk_bf16_f32 v161, v178, v179\n"
"v_cvt_pk_bf16_f32 v162, v180, v181\n"
"v_cvt_pk_bf16_f32 v163, v182, v183\n"
"v_accvgpr_write_b32 a0, v160\n"
"v_accvgpr_write_b32 a1, v161\n"
"v_accvgpr_write_b32 a2, v162\n"
"v_accvgpr_write_b32 a3, v163\n"
"s_waitcnt lgkmcnt(2)\n"
"v_lshlrev_b32 v184, 16, v164\n"
"v_and_b32 v185, 0xffff0000, v164\n"
"v_lshlrev_b32 v186, 16, v165\n"
"v_and_b32 v187, 0xffff0000, v165\n"
"v_lshlrev_b32 v188, 16, v166\n"
"v_and_b32 v189, 0xffff0000, v166\n"
"v_lshlrev_b32 v190, 16, v167\n"
"v_and_b32 v191, 0xffff0000, v167\n"
"v_pk_mul_f32 v[184:185], v[184:185], s[56:57]\n"
"v_pk_mul_f32 v[186:187], v[186:187], s[56:57]\n"
"v_pk_mul_f32 v[188:189], v[188:189], s[56:57]\n"
"v_pk_mul_f32 v[190:191], v[190:191], s[56:57]\n"
"v_cvt_pk_bf16_f32 v164, v184, v185\n"
"v_cvt_pk_bf16_f32 v165, v186, v187\n"
"v_cvt_pk_bf16_f32 v166, v188, v189\n"
"v_cvt_pk_bf16_f32 v167, v190, v191\n"
"v_accvgpr_write_b32 a4, v164\n"
"v_accvgpr_write_b32 a5, v165\n"
"v_accvgpr_write_b32 a6, v166\n"
"v_accvgpr_write_b32 a7, v167\n"
"s_waitcnt lgkmcnt(1)\n"
"v_lshlrev_b32 v176, 16, v168\n"
"v_and_b32 v177, 0xffff0000, v168\n"
"v_lshlrev_b32 v178, 16, v169\n"
"v_and_b32 v179, 0xffff0000, v169\n"
"v_lshlrev_b32 v180, 16, v170\n"
"v_and_b32 v181, 0xffff0000, v170\n"
"v_lshlrev_b32 v182, 16, v171\n"
"v_and_b32 v183, 0xffff0000, v171\n"
"v_pk_mul_f32 v[176:177], v[176:177], s[56:57]\n"
"v_pk_mul_f32 v[178:179], v[178:179], s[56:57]\n"
"v_pk_mul_f32 v[180:181], v[180:181], s[56:57]\n"
"v_pk_mul_f32 v[182:183], v[182:183], s[56:57]\n"
"v_cvt_pk_bf16_f32 v168, v176, v177\n"
"v_cvt_pk_bf16_f32 v169, v178, v179\n"
"v_cvt_pk_bf16_f32 v170, v180, v181\n"
"v_cvt_pk_bf16_f32 v171, v182, v183\n"
"v_accvgpr_write_b32 a8, v168\n"
"v_accvgpr_write_b32 a9, v169\n"
"v_accvgpr_write_b32 a10, v170\n"
"v_accvgpr_write_b32 a11, v171\n"
"s_waitcnt lgkmcnt(0)\n"
"v_lshlrev_b32 v184, 16, v172\n"
"v_and_b32 v185, 0xffff0000, v172\n"
"v_lshlrev_b32 v186, 16, v173\n"
"v_and_b32 v187, 0xffff0000, v173\n"
"v_lshlrev_b32 v188, 16, v174\n"
"v_and_b32 v189, 0xffff0000, v174\n"
"v_lshlrev_b32 v190, 16, v175\n"
"v_and_b32 v191, 0xffff0000, v175\n"
"v_pk_mul_f32 v[184:185], v[184:185], s[56:57]\n"
"v_pk_mul_f32 v[186:187], v[186:187], s[56:57]\n"
"v_pk_mul_f32 v[188:189], v[188:189], s[56:57]\n"
"v_pk_mul_f32 v[190:191], v[190:191], s[56:57]\n"
"v_cvt_pk_bf16_f32 v172, v184, v185\n"
"v_cvt_pk_bf16_f32 v173, v186, v187\n"
"v_cvt_pk_bf16_f32 v174, v188, v189\n"
"v_cvt_pk_bf16_f32 v175, v190, v191\n"
"v_accvgpr_write_b32 a12, v172\n"
"v_accvgpr_write_b32 a13, v173\n"
"v_accvgpr_write_b32 a14, v174\n"
"v_accvgpr_write_b32 a15, v175\n"
"v_mfma_f32_16x16x32_bf16 v[192:195], a[96:99], a[0:3], 0\n"
"v_mfma_f32_16x16x32_bf16 v[192:195], a[100:103], a[4:7], v[192:195]\n"
"v_mfma_f32_16x16x32_bf16 v[192:195], a[104:107], a[8:11], v[192:195]\n"
"v_mfma_f32_16x16x32_bf16 v[192:195], a[108:111], a[12:15], v[192:195]\n"
"v_mfma_f32_16x16x32_bf16 v[196:199], a[112:115], a[12:15], 0\n"
"v_mfma_f32_16x16x32_bf16 v[196:199], a[116:119], a[8:11], v[196:199]\n"
"v_mfma_f32_16x16x32_bf16 v[196:199], a[120:123], a[4:7], v[196:199]\n"
"v_mfma_f32_16x16x32_bf16 v[196:199], a[124:127], a[0:3], v[196:199]\n"
"v_mfma_f32_16x16x32_bf16 v[200:203], a[128:131], a[0:3], 0\n"
"v_mfma_f32_16x16x32_bf16 v[200:203], a[132:135], a[4:7], v[200:203]\n"
"v_mfma_f32_16x16x32_bf16 v[200:203], a[136:139], a[8:11], v[200:203]\n"
"v_mfma_f32_16x16x32_bf16 v[200:203], a[140:143], a[12:15], v[200:203]\n"
"v_mfma_f32_16x16x32_bf16 v[204:207], a[144:147], a[12:15], 0\n"
"v_mfma_f32_16x16x32_bf16 v[204:207], a[148:151], a[8:11], v[204:207]\n"
"v_mfma_f32_16x16x32_bf16 v[204:207], a[152:155], a[4:7], v[204:207]\n"
"v_mfma_f32_16x16x32_bf16 v[204:207], a[156:159], a[0:3], v[204:207]\n"
"v_max3_f32 v253, v192, v193, v220\n"
"v_max3_f32 v254, v194, v195, v196\n"
"v_max3_f32 v253, v253, v197, v198\n"
"s_nop 0\n"
"v_max3_f32 v254, v254, v199, v200\n"
"v_max3_f32 v253, v253, v201, v202\n"
"s_nop 1\n"
"v_max3_f32 v254, v254, v203, v204\n"
"v_max3_f32 v253, v253, v205, v206\n"
"v_max3_f32 v253, v253, v254, v207\n"
"v_mov_b32 v254, v253\n"
"s_nop 1\n"
"v_permlane32_swap_b32 v253, v254\n"
"v_max_f32 v253, v253, v254\n"
"v_mov_b32 v254, v253\n"
"s_nop 1\n"
"v_permlane16_swap_b32 v253, v254\n"
"v_max_f32 v253, v253, v254\n"
"v_sub_f32 v192, v192, v253\n"
"v_sub_f32 v193, v193, v253\n"
"v_sub_f32 v194, v194, v253\n"
"v_sub_f32 v195, v195, v253\n"
"v_sub_f32 v196, v196, v253\n"
"v_sub_f32 v197, v197, v253\n"
"v_sub_f32 v198, v198, v253\n"
"v_sub_f32 v199, v199, v253\n"
"v_sub_f32 v200, v200, v253\n"
"v_sub_f32 v201, v201, v253\n"
"v_sub_f32 v202, v202, v253\n"
"v_sub_f32 v203, v203, v253\n"
"v_sub_f32 v204, v204, v253\n"
"v_sub_f32 v205, v205, v253\n"
"v_sub_f32 v206, v206, v253\n"
"v_sub_f32 v207, v207, v253\n"
"v_sub_f32 v220, 0, v253\n"
"v_sub_f32 v221, 0, v253\n"
"v_sub_f32 v222, 0, v253\n"
"v_sub_f32 v223, 0, v253\n"
"v_exp_f32 v192, v192\n"
"v_exp_f32 v193, v193\n"
"v_exp_f32 v194, v194\n"
"v_exp_f32 v195, v195\n"
"v_cvt_pk_bf16_f32 v208, v192, v193\n"
"v_exp_f32 v196, v196\n"
"v_exp_f32 v197, v197\n"
"v_cvt_pk_bf16_f32 v209, v194, v195\n"
"v_exp_f32 v198, v198\n"
"v_exp_f32 v199, v199\n"
"v_cvt_pk_bf16_f32 v210, v196, v197\n"
"v_exp_f32 v200, v200\n"
"v_cvt_pk_bf16_f32 v211, v198, v199\n"
"v_exp_f32 v201, v201\n"
"v_exp_f32 v202, v202\n"
"v_exp_f32 v203, v203\n"
"s_waitcnt vmcnt(4)\n"
"s_add_u32 m0, s53, 98304\n"
"s_nop 0\n"
"buffer_load_dwordx4 v136, s[44:47], s54 offen nt lds\n"
"buffer_load_dwordx4 v137, s[44:47], s54 offen offset:1024 nt lds\n"
"buffer_load_dwordx4 v138, s[44:47], s54 offen offset:2048 nt lds\n"
"buffer_load_dwordx4 v139, s[44:47], s54 offen offset:3072 nt lds\n"
"s_add_u32 s54, s54, 4096\n"
"ds_read_b128 v[160:163], v134 offset:16384\n"
"ds_read_b128 v[164:167], v135 offset:16384\n"
"ds_read_b128 v[168:171], v134 offset:17408\n"
"ds_read_b128 v[172:175], v135 offset:17408\n"
"s_waitcnt lgkmcnt(3)\n"
"v_lshlrev_b32 v176, 16, v160\n"
"v_and_b32 v177, 0xffff0000, v160\n"
"v_lshlrev_b32 v178, 16, v161\n"
"v_and_b32 v179, 0xffff0000, v161\n"
"v_lshlrev_b32 v180, 16, v162\n"
"v_and_b32 v181, 0xffff0000, v162\n"
"v_lshlrev_b32 v182, 16, v163\n"
"v_and_b32 v183, 0xffff0000, v163\n"
"v_pk_mul_f32 v[176:177], v[176:177], s[56:57]\n"
"v_pk_mul_f32 v[178:179], v[178:179], s[56:57]\n"
"v_pk_mul_f32 v[180:181], v[180:181], s[56:57]\n"
"v_pk_mul_f32 v[182:183], v[182:183], s[56:57]\n"
"v_cvt_pk_bf16_f32 v160, v176, v177\n"
"v_cvt_pk_bf16_f32 v161, v178, v179\n"
"v_cvt_pk_bf16_f32 v162, v180, v181\n"
"v_cvt_pk_bf16_f32 v163, v182, v183\n"
"v_accvgpr_write_b32 a16, v160\n"
"v_accvgpr_write_b32 a17, v161\n"
"v_accvgpr_write_b32 a18, v162\n"
"v_accvgpr_write_b32 a19, v163\n"
"s_waitcnt lgkmcnt(2)\n"
"v_lshlrev_b32 v184, 16, v164\n"
"v_and_b32 v185, 0xffff0000, v164\n"
"v_lshlrev_b32 v186, 16, v165\n"
"v_and_b32 v187, 0xffff0000, v165\n"
"v_lshlrev_b32 v188, 16, v166\n"
"v_and_b32 v189, 0xffff0000, v166\n"
"v_lshlrev_b32 v190, 16, v167\n"
"v_and_b32 v191, 0xffff0000, v167\n"
"v_pk_mul_f32 v[184:185], v[184:185], s[56:57]\n"
"v_pk_mul_f32 v[186:187], v[186:187], s[56:57]\n"
"v_pk_mul_f32 v[188:189], v[188:189], s[56:57]\n"
"v_pk_mul_f32 v[190:191], v[190:191], s[56:57]\n"
"v_cvt_pk_bf16_f32 v164, v184, v185\n"
"v_cvt_pk_bf16_f32 v165, v186, v187\n"
"v_cvt_pk_bf16_f32 v166, v188, v189\n"
"v_cvt_pk_bf16_f32 v167, v190, v191\n"
"v_accvgpr_write_b32 a20, v164\n"
"v_accvgpr_write_b32 a21, v165\n"
"v_accvgpr_write_b32 a22, v166\n"
"v_accvgpr_write_b32 a23, v167\n"
"s_waitcnt lgkmcnt(1)\n"
"v_lshlrev_b32 v176, 16, v168\n"
"v_and_b32 v177, 0xffff0000, v168\n"
"v_lshlrev_b32 v178, 16, v169\n"
"v_and_b32 v179, 0xffff0000, v169\n"
"v_lshlrev_b32 v180, 16, v170\n"
"v_and_b32 v181, 0xffff0000, v170\n"
"v_lshlrev_b32 v182, 16, v171\n"
"v_and_b32 v183, 0xffff0000, v171\n"
"v_pk_mul_f32 v[176:177], v[176:177], s[56:57]\n"
"v_pk_mul_f32 v[178:179], v[178:179], s[56:57]\n"
"v_pk_mul_f32 v[180:181], v[180:181], s[56:57]\n"
"v_pk_mul_f32 v[182:183], v[182:183], s[56:57]\n"
"v_cvt_pk_bf16_f32 v168, v176, v177\n"
"v_cvt_pk_bf16_f32 v169, v178, v179\n"
"v_cvt_pk_bf16_f32 v170, v180, v181\n"
"v_cvt_pk_bf16_f32 v171, v182, v183\n"
"v_accvgpr_write_b32 a24, v168\n"
"v_accvgpr_write_b32 a25, v169\n"
"v_accvgpr_write_b32 a26, v170\n"
"v_accvgpr_write_b32 a27, v171\n"
"s_waitcnt lgkmcnt(0)\n"
"v_lshlrev_b32 v184, 16, v172\n"
"v_and_b32 v185, 0xffff0000, v172\n"
"v_lshlrev_b32 v186, 16, v173\n"
"v_and_b32 v187, 0xffff0000, v173\n"
"v_lshlrev_b32 v188, 16, v174\n"
"v_and_b32 v189, 0xffff0000, v174\n"
"v_lshlrev_b32 v190, 16, v175\n"
"v_and_b32 v191, 0xffff0000, v175\n"
"v_pk_mul_f32 v[184:185], v[184:185], s[56:57]\n"
"v_pk_mul_f32 v[186:187], v[186:187], s[56:57]\n"
"v_pk_mul_f32 v[188:189], v[188:189], s[56:57]\n"
"v_pk_mul_f32 v[190:191], v[190:191], s[56:57]\n"
"v_cvt_pk_bf16_f32 v172, v184, v185\n"
"v_cvt_pk_bf16_f32 v173, v186, v187\n"
"v_cvt_pk_bf16_f32 v174, v188, v189\n"
"v_cvt_pk_bf16_f32 v175, v190, v191\n"
"v_accvgpr_write_b32 a28, v172\n"
"v_accvgpr_write_b32 a29, v173\n"
"v_accvgpr_write_b32 a30, v174\n"
"v_accvgpr_write_b32 a31, v175\n"
"v_mfma_f32_16x16x32_bf16 v[192:195], a[96:99], a[16:19], 0\n"
"v_exp_f32 v204, v204\n"
"v_mfma_f32_16x16x32_bf16 v[192:195], a[100:103], a[20:23], v[192:195]\n"
"v_cvt_pk_bf16_f32 v212, v200, v201\n"
"v_cvt_pk_bf16_f32 v213, v202, v203\n"
"v_mfma_f32_16x16x32_bf16 v[192:195], a[104:107], a[24:27], v[192:195]\n"
"v_exp_f32 v205, v205\n"
"v_mfma_f32_16x16x32_bf16 v[192:195], a[108:111], a[28:31], v[192:195]\n"
"v_cvt_pk_bf16_f32 v214, v204, v205\n"
"v_mfma_f32_16x16x32_bf16 v[196:199], a[112:115], a[28:31], 0\n"
"v_exp_f32 v206, v206\n"
"v_mfma_f32_16x16x32_bf16 v[196:199], a[116:119], a[24:27], v[196:199]\n"
"v_mfma_f32_16x16x32_bf16 v[196:199], a[120:123], a[20:23], v[196:199]\n"
"v_exp_f32 v207, v207\n"
"v_mfma_f32_16x16x32_bf16 v[196:199], a[124:127], a[16:19], v[196:199]\n"
"v_cvt_pk_bf16_f32 v215, v206, v207\n"
"s_waitcnt vmcnt(4)\n"
"s_barrier\n"
"ds_read_b64_tr_b16 a[160:161], v246 offset:2048\n"
"ds_read_b64_tr_b16 a[162:163], v246 offset:10240\n"
"ds_read_b64_tr_b16 a[164:165], v246 offset:18432\n"
"ds_read_b64_tr_b16 a[166:167], v246 offset:26624\n"
"ds_read_b64_tr_b16 a[168:169], v247 offset:18432\n"
"ds_read_b64_tr_b16 a[170:171], v247 offset:26624\n"
"ds_read_b64_tr_b16 a[172:173], v247 offset:2048\n"
"ds_read_b64_tr_b16 a[174:175], v247 offset:10240\n"
"ds_read_b64_tr_b16 a[176:177], v246 offset:2560\n"
"ds_read_b64_tr_b16 a[178:179], v246 offset:10752\n"
"ds_read_b64_tr_b16 a[180:181], v246 offset:18944\n"
"ds_read_b64_tr_b16 a[182:183], v246 offset:27136\n"
"ds_read_b64_tr_b16 a[184:185], v247 offset:18944\n"
"ds_read_b64_tr_b16 a[186:187], v247 offset:27136\n"
"ds_read_b64_tr_b16 a[188:189], v247 offset:2560\n"
"ds_read_b64_tr_b16 a[190:191], v247 offset:10752\n"
"ds_read_b64_tr_b16 a[192:193], v246 offset:3072\n"
"ds_read_b64_tr_b16 a[194:195], v246 offset:11264\n"
"ds_read_b64_tr_b16 a[196:197], v246 offset:19456\n"
"ds_read_b64_tr_b16 a[198:199], v246 offset:27648\n"
"ds_read_b64_tr_b16 a[200:201], v247 offset:19456\n"
"ds_read_b64_tr_b16 a[202:203], v247 offset:27648\n"
"ds_read_b64_tr_b16 a[204:205], v247 offset:3072\n"
"ds_read_b64_tr_b16 a[206:207], v247 offset:11264\n"
"ds_read_b64_tr_b16 a[208:209], v246 offset:3584\n"
"ds_read_b64_tr_b16 a[210:211], v246 offset:11776\n"
"ds_read_b64_tr_b16 a[212:213], v246 offset:19968\n"
"ds_read_b64_tr_b16 a[214:215], v246 offset:28160\n"
"ds_read_b64_tr_b16 a[216:217], v247 offset:19968\n"
"ds_read_b64_tr_b16 a[218:219], v247 offset:28160\n"
"ds_read_b64_tr_b16 a[220:221], v247 offset:3584\n"
"ds_read_b64_tr_b16 a[222:223], v247 offset:11776\n"
"s_add_u32 m0, s53, 114688\n"
"s_nop 0\n"
"buffer_load_dwordx4 v136, s[44:47], s54 offen nt lds\n"
"buffer_load_dwordx4 v137, s[44:47], s54 offen offset:1024 nt lds\n"
"buffer_load_dwordx4 v138, s[44:47], s54 offen offset:2048 nt lds\n"
"buffer_load_dwordx4 v139, s[44:47], s54 offen offset:3072 nt lds\n"
"s_add_u32 s54, s54, 4096\n"
"v_mfma_f32_16x16x32_bf16 v[200:203], a[128:131], a[16:19], 0\n"
"v_mfma_f32_16x16x32_bf16 v[200:203], a[132:135], a[20:23], v[200:203]\n"
"v_mfma_f32_16x16x32_bf16 v[200:203], a[136:139], a[24:27], v[200:203]\n"
"v_mfma_f32_16x16x32_bf16 v[200:203], a[140:143], a[28:31], v[200:203]\n"
"v_mfma_f32_16x16x32_bf16 v[204:207], a[144:147], a[28:31], 0\n"
"v_mfma_f32_16x16x32_bf16 v[204:207], a[148:151], a[24:27], v[204:207]\n"
"v_mfma_f32_16x16x32_bf16 v[204:207], a[152:155], a[20:23], v[204:207]\n"
"v_max3_f32 v253, v192, v193, v224\n"
"v_max3_f32 v254, v194, v195, v196\n"
"v_mfma_f32_16x16x32_bf16 v[204:207], a[156:159], a[16:19], v[204:207]\n"
"v_max3_f32 v253, v253, v197, v198\n"
"s_waitcnt lgkmcnt(15)\n"
"v_smfmac_f32_16x16x64_bf16 a[224:227], a[248:251], v[208:215], v252\n"
"v_max3_f32 v254, v254, v199, v200\n"
"v_mfma_f32_16x16x32_bf16 v[0:3], v[208:211], a[160:163], 0\n"
"v_max3_f32 v253, v253, v201, v202\n"
"v_mfma_f32_16x16x32_bf16 v[0:3], v[212:215], a[164:167], v[0:3]\n"
"v_mfma_f32_16x16x32_bf16 v[4:7], v[212:215], a[168:171], 0\n"
"v_max3_f32 v254, v254, v203, v204\n"
"v_max3_f32 v253, v253, v205, v206\n"
"v_max3_f32 v253, v253, v254, v207\n"
"v_mov_b32 v254, v253\n"
"v_mfma_f32_16x16x32_bf16 v[4:7], v[208:211], a[172:175], v[4:7]\n"
"s_nop 0\n"
"v_permlane32_swap_b32 v253, v254\n"
"v_max_f32 v253, v253, v254\n"
"v_mov_b32 v254, v253\n"
"v_mfma_f32_16x16x32_bf16 v[8:11], v[208:211], a[176:179], 0\n"
"s_nop 0\n"
"v_permlane16_swap_b32 v253, v254\n"
"v_max_f32 v253, v253, v254\n"
"v_sub_f32 v192, v192, v253\n"
"v_mfma_f32_16x16x32_bf16 v[8:11], v[212:215], a[180:183], v[8:11]\n"
"v_sub_f32 v193, v193, v253\n"
"v_sub_f32 v194, v194, v253\n"
"v_sub_f32 v195, v195, v253\n"
"v_mfma_f32_16x16x32_bf16 v[12:15], v[212:215], a[184:187], 0\n"
"v_sub_f32 v196, v196, v253\n"
"v_sub_f32 v197, v197, v253\n"
"v_sub_f32 v198, v198, v253\n"
"v_sub_f32 v199, v199, v253\n"
"s_waitcnt lgkmcnt(8)\n"
"v_mfma_f32_16x16x32_bf16 v[12:15], v[208:211], a[188:191], v[12:15]\n"
"v_sub_f32 v200, v200, v253\n"
"v_sub_f32 v201, v201, v253\n"
"v_sub_f32 v202, v202, v253\n"
"v_mfma_f32_16x16x32_bf16 v[16:19], v[208:211], a[192:195], 0\n"
"v_sub_f32 v203, v203, v253\n"
"v_sub_f32 v204, v204, v253\n"
"v_sub_f32 v205, v205, v253\n"
"v_mfma_f32_16x16x32_bf16 v[16:19], v[212:215], a[196:199], v[16:19]\n"
"v_sub_f32 v206, v206, v253\n"
"v_sub_f32 v207, v207, v253\n"
"v_sub_f32 v224, 0, v253\n"
"v_mfma_f32_16x16x32_bf16 v[20:23], v[212:215], a[200:203], 0\n"
"v_sub_f32 v225, 0, v253\n"
"v_sub_f32 v226, 0, v253\n"
"v_sub_f32 v227, 0, v253\n"
"s_waitcnt lgkmcnt(0)\n"
"v_mfma_f32_16x16x32_bf16 v[20:23], v[208:211], a[204:207], v[20:23]\n"
"v_exp_f32 v192, v192\n"
"v_exp_f32 v193, v193\n"
"v_exp_f32 v194, v194\n"
"v_exp_f32 v195, v195\n"
"v_mfma_f32_16x16x32_bf16 v[24:27], v[208:211], a[208:211], 0\n"
"v_cvt_pk_bf16_f32 v216, v192, v193\n"
"v_exp_f32 v196, v196\n"
"v_exp_f32 v197, v197\n"
"v_mfma_f32_16x16x32_bf16 v[24:27], v[212:215], a[212:215], v[24:27]\n"
"v_cvt_pk_bf16_f32 v217, v194, v195\n"
"v_exp_f32 v198, v198\n"
"v_exp_f32 v199, v199\n"
"v_mfma_f32_16x16x32_bf16 v[28:31], v[212:215], a[216:219], 0\n"
"v_cvt_pk_bf16_f32 v218, v196, v197\n"
"v_exp_f32 v200, v200\n"
"v_cvt_pk_bf16_f32 v219, v198, v199\n"
"v_mfma_f32_16x16x32_bf16 v[28:31], v[208:211], a[220:223], v[28:31]\n"
"v_exp_f32 v201, v201\n"
"v_exp_f32 v202, v202\n"
"v_exp_f32 v203, v203\n"
"s_waitcnt vmcnt(4)\n"
"s_add_u32 m0, s53, 131072\n"
"s_nop 0\n"
"buffer_load_dwordx4 v136, s[44:47], s54 offen nt lds\n"
"buffer_load_dwordx4 v137, s[44:47], s54 offen offset:1024 nt lds\n"
"buffer_load_dwordx4 v138, s[44:47], s54 offen offset:2048 nt lds\n"
"buffer_load_dwordx4 v139, s[44:47], s54 offen offset:3072 nt lds\n"
"s_add_u32 s54, s54, 4096\n"
"ds_read_b128 v[160:163], v134 offset:32768\n"
"ds_read_b128 v[164:167], v135 offset:32768\n"
"ds_read_b128 v[168:171], v134 offset:33792\n"
"ds_read_b128 v[172:175], v135 offset:33792\n"
"s_waitcnt lgkmcnt(3)\n"
"v_lshlrev_b32 v176, 16, v160\n"
"v_and_b32 v177, 0xffff0000, v160\n"
"v_lshlrev_b32 v178, 16, v161\n"
"v_and_b32 v179, 0xffff0000, v161\n"
"v_lshlrev_b32 v180, 16, v162\n"
"v_and_b32 v181, 0xffff0000, v162\n"
"v_lshlrev_b32 v182, 16, v163\n"
"v_and_b32 v183, 0xffff0000, v163\n"
"v_pk_mul_f32 v[176:177], v[176:177], s[56:57]\n"
"v_pk_mul_f32 v[178:179], v[178:179], s[56:57]\n"
"v_pk_mul_f32 v[180:181], v[180:181], s[56:57]\n"
"v_pk_mul_f32 v[182:183], v[182:183], s[56:57]\n"
"v_cvt_pk_bf16_f32 v160, v176, v177\n"
"v_cvt_pk_bf16_f32 v161, v178, v179\n"
"v_cvt_pk_bf16_f32 v162, v180, v181\n"
"v_cvt_pk_bf16_f32 v163, v182, v183\n"
"v_accvgpr_write_b32 a32, v160\n"
"v_accvgpr_write_b32 a33, v161\n"
"v_accvgpr_write_b32 a34, v162\n"
"v_accvgpr_write_b32 a35, v163\n"
"s_waitcnt lgkmcnt(2)\n"
"v_lshlrev_b32 v184, 16, v164\n"
"v_and_b32 v185, 0xffff0000, v164\n"
"v_lshlrev_b32 v186, 16, v165\n"
"v_and_b32 v187, 0xffff0000, v165\n"
"v_lshlrev_b32 v188, 16, v166\n"
"v_and_b32 v189, 0xffff0000, v166\n"
"v_lshlrev_b32 v190, 16, v167\n"
"v_and_b32 v191, 0xffff0000, v167\n"
"v_pk_mul_f32 v[184:185], v[184:185], s[56:57]\n"
"v_pk_mul_f32 v[186:187], v[186:187], s[56:57]\n"
"v_pk_mul_f32 v[188:189], v[188:189], s[56:57]\n"
"v_pk_mul_f32 v[190:191], v[190:191], s[56:57]\n"
"v_cvt_pk_bf16_f32 v164, v184, v185\n"
"v_cvt_pk_bf16_f32 v165, v186, v187\n"
"v_cvt_pk_bf16_f32 v166, v188, v189\n"
"v_cvt_pk_bf16_f32 v167, v190, v191\n"
"v_accvgpr_write_b32 a36, v164\n"
"v_accvgpr_write_b32 a37, v165\n"
"v_accvgpr_write_b32 a38, v166\n"
"v_accvgpr_write_b32 a39, v167\n"
"s_waitcnt lgkmcnt(1)\n"
"v_lshlrev_b32 v176, 16, v168\n"
"v_and_b32 v177, 0xffff0000, v168\n"
"v_lshlrev_b32 v178, 16, v169\n"
"v_and_b32 v179, 0xffff0000, v169\n"
"v_lshlrev_b32 v180, 16, v170\n"
"v_and_b32 v181, 0xffff0000, v170\n"
"v_lshlrev_b32 v182, 16, v171\n"
"v_and_b32 v183, 0xffff0000, v171\n"
"v_pk_mul_f32 v[176:177], v[176:177], s[56:57]\n"
"v_pk_mul_f32 v[178:179], v[178:179], s[56:57]\n"
"v_pk_mul_f32 v[180:181], v[180:181], s[56:57]\n"
"v_pk_mul_f32 v[182:183], v[182:183], s[56:57]\n"
"v_cvt_pk_bf16_f32 v168, v176, v177\n"
"v_cvt_pk_bf16_f32 v169, v178, v179\n"
"v_cvt_pk_bf16_f32 v170, v180, v181\n"
"v_cvt_pk_bf16_f32 v171, v182, v183\n"
"v_accvgpr_write_b32 a40, v168\n"
"v_accvgpr_write_b32 a41, v169\n"
"v_accvgpr_write_b32 a42, v170\n"
"v_accvgpr_write_b32 a43, v171\n"
"s_waitcnt lgkmcnt(0)\n"
"v_lshlrev_b32 v184, 16, v172\n"
"v_and_b32 v185, 0xffff0000, v172\n"
"v_lshlrev_b32 v186, 16, v173\n"
"v_and_b32 v187, 0xffff0000, v173\n"
"v_lshlrev_b32 v188, 16, v174\n"
"v_and_b32 v189, 0xffff0000, v174\n"
"v_lshlrev_b32 v190, 16, v175\n"
"v_and_b32 v191, 0xffff0000, v175\n"
"v_pk_mul_f32 v[184:185], v[184:185], s[56:57]\n"
"v_pk_mul_f32 v[186:187], v[186:187], s[56:57]\n"
"v_pk_mul_f32 v[188:189], v[188:189], s[56:57]\n"
"v_pk_mul_f32 v[190:191], v[190:191], s[56:57]\n"
"v_cvt_pk_bf16_f32 v172, v184, v185\n"
"v_cvt_pk_bf16_f32 v173, v186, v187\n"
"v_cvt_pk_bf16_f32 v174, v188, v189\n"
"v_cvt_pk_bf16_f32 v175, v190, v191\n"
"v_accvgpr_write_b32 a44, v172\n"
"v_accvgpr_write_b32 a45, v173\n"
"v_accvgpr_write_b32 a46, v174\n"
"v_accvgpr_write_b32 a47, v175\n"
"v_mfma_f32_16x16x32_bf16 v[192:195], a[96:99], a[32:35], 0\n"
"v_exp_f32 v204, v204\n"
"v_mfma_f32_16x16x32_bf16 v[192:195], a[100:103], a[36:39], v[192:195]\n"
"v_cvt_pk_bf16_f32 v212, v200, v201\n"
"v_cvt_pk_bf16_f32 v213, v202, v203\n"
"v_mfma_f32_16x16x32_bf16 v[192:195], a[104:107], a[40:43], v[192:195]\n"
"v_exp_f32 v205, v205\n"
"v_mfma_f32_16x16x32_bf16 v[192:195], a[108:111], a[44:47], v[192:195]\n"
"v_cvt_pk_bf16_f32 v214, v204, v205\n"
"v_mfma_f32_16x16x32_bf16 v[196:199], a[112:115], a[44:47], 0\n"
"v_exp_f32 v206, v206\n"
"v_mfma_f32_16x16x32_bf16 v[196:199], a[116:119], a[40:43], v[196:199]\n"
"v_mfma_f32_16x16x32_bf16 v[196:199], a[120:123], a[36:39], v[196:199]\n"
"v_exp_f32 v207, v207\n"
"v_mfma_f32_16x16x32_bf16 v[196:199], a[124:127], a[32:35], v[196:199]\n"
"v_cvt_pk_bf16_f32 v215, v206, v207\n"
"v_mfma_f32_16x16x32_bf16 v[200:203], a[128:131], a[32:35], 0\n"
"v_mfma_f32_16x16x32_bf16 v[200:203], a[132:135], a[36:39], v[200:203]\n"
"v_mfma_f32_16x16x32_bf16 v[200:203], a[136:139], a[40:43], v[200:203]\n"
"v_mfma_f32_16x16x32_bf16 v[200:203], a[140:143], a[44:47], v[200:203]\n"
"v_mfma_f32_16x16x32_bf16 v[204:207], a[144:147], a[44:47], 0\n"
"v_mfma_f32_16x16x32_bf16 v[204:207], a[148:151], a[40:43], v[204:207]\n"
"v_mfma_f32_16x16x32_bf16 v[204:207], a[152:155], a[36:39], v[204:207]\n"
"v_max3_f32 v253, v192, v193, v228\n"
"v_max3_f32 v254, v194, v195, v196\n"
"v_mfma_f32_16x16x32_bf16 v[204:207], a[156:159], a[32:35], v[204:207]\n"
"v_max3_f32 v253, v253, v197, v198\n"
"v_smfmac_f32_16x16x64_bf16 a[228:231], a[248:251], v[212:219], v252\n"
"v_max3_f32 v254, v254, v199, v200\n"
"v_mfma_f32_16x16x32_bf16 v[32:35], v[216:219], a[160:163], 0\n"
"v_max3_f32 v253, v253, v201, v202\n"
"v_mfma_f32_16x16x32_bf16 v[32:35], v[212:215], a[164:167], v[32:35]\n"
"v_mfma_f32_16x16x32_bf16 v[36:39], v[212:215], a[168:171], 0\n"
"s_nop 0\n"
"v_max3_f32 v254, v254, v203, v204\n"
"v_max3_f32 v253, v253, v205, v206\n"
"v_max3_f32 v253, v253, v254, v207\n"
"v_mov_b32 v254, v253\n"
"v_mfma_f32_16x16x32_bf16 v[36:39], v[216:219], a[172:175], v[36:39]\n"
"s_nop 0\n"
"v_permlane32_swap_b32 v253, v254\n"
"v_max_f32 v253, v253, v254\n"
"v_mov_b32 v254, v253\n"
"v_mfma_f32_16x16x32_bf16 v[40:43], v[216:219], a[176:179], 0\n"
"s_nop 0\n"
"v_permlane16_swap_b32 v253, v254\n"
"v_max_f32 v253, v253, v254\n"
"v_sub_f32 v192, v192, v253\n"
"v_mfma_f32_16x16x32_bf16 v[40:43], v[212:215], a[180:183], v[40:43]\n"
"v_sub_f32 v193, v193, v253\n"
"v_sub_f32 v194, v194, v253\n"
"v_sub_f32 v195, v195, v253\n"
"v_mfma_f32_16x16x32_bf16 v[44:47], v[212:215], a[184:187], 0\n"
"v_sub_f32 v196, v196, v253\n"
"v_sub_f32 v197, v197, v253\n"
"v_sub_f32 v198, v198, v253\n"
"v_sub_f32 v199, v199, v253\n"
"v_mfma_f32_16x16x32_bf16 v[44:47], v[216:219], a[188:191], v[44:47]\n"
"v_sub_f32 v200, v200, v253\n"
"v_sub_f32 v201, v201, v253\n"
"v_sub_f32 v202, v202, v253\n"
"v_mfma_f32_16x16x32_bf16 v[48:51], v[216:219], a[192:195], 0\n"
"v_sub_f32 v203, v203, v253\n"
"v_sub_f32 v204, v204, v253\n"
"v_sub_f32 v205, v205, v253\n"
"v_mfma_f32_16x16x32_bf16 v[48:51], v[212:215], a[196:199], v[48:51]\n"
"v_sub_f32 v206, v206, v253\n"
"v_sub_f32 v207, v207, v253\n"
"v_sub_f32 v228, 0, v253\n"
"v_mfma_f32_16x16x32_bf16 v[52:55], v[212:215], a[200:203], 0\n"
"v_sub_f32 v229, 0, v253\n"
"v_sub_f32 v230, 0, v253\n"
"v_sub_f32 v231, 0, v253\n"
"v_mfma_f32_16x16x32_bf16 v[52:55], v[216:219], a[204:207], v[52:55]\n"
"v_exp_f32 v192, v192\n"
"v_exp_f32 v193, v193\n"
"v_exp_f32 v194, v194\n"
"v_exp_f32 v195, v195\n"
"v_mfma_f32_16x16x32_bf16 v[56:59], v[216:219], a[208:211], 0\n"
"v_cvt_pk_bf16_f32 v208, v192, v193\n"
"v_exp_f32 v196, v196\n"
"v_exp_f32 v197, v197\n"
"v_mfma_f32_16x16x32_bf16 v[56:59], v[212:215], a[212:215], v[56:59]\n"
"v_cvt_pk_bf16_f32 v209, v194, v195\n"
"v_exp_f32 v198, v198\n"
"v_exp_f32 v199, v199\n"
"v_mfma_f32_16x16x32_bf16 v[60:63], v[212:215], a[216:219], 0\n"
"v_cvt_pk_bf16_f32 v210, v196, v197\n"
"v_exp_f32 v200, v200\n"
"v_cvt_pk_bf16_f32 v211, v198, v199\n"
"v_mfma_f32_16x16x32_bf16 v[60:63], v[216:219], a[220:223], v[60:63]\n"
"v_exp_f32 v201, v201\n"
"v_exp_f32 v202, v202\n"
"v_exp_f32 v203, v203\n"
"s_waitcnt vmcnt(4)\n"
"s_add_u32 m0, s53, 32768\n"
"s_add_u32 s24, s52, 16384\n"
"buffer_load_dwordx4 v248, s[36:39], s24 offen nt lds\n"
"buffer_load_dwordx4 v249, s[36:39], s24 offen offset:1024 nt lds\n"
"s_add_u32 m0, s53, 49152\n"
"s_add_u32 s24, s52, 24576\n"
"buffer_load_dwordx4 v248, s[36:39], s24 offen nt lds\n"
"buffer_load_dwordx4 v249, s[36:39], s24 offen offset:1024 nt lds\n"
"ds_read_b128 v[160:163], v134 offset:49152\n"
"ds_read_b128 v[164:167], v135 offset:49152\n"
"ds_read_b128 v[168:171], v134 offset:50176\n"
"ds_read_b128 v[172:175], v135 offset:50176\n"
"s_waitcnt lgkmcnt(3)\n"
"v_lshlrev_b32 v176, 16, v160\n"
"v_and_b32 v177, 0xffff0000, v160\n"
"v_lshlrev_b32 v178, 16, v161\n"
"v_and_b32 v179, 0xffff0000, v161\n"
"v_lshlrev_b32 v180, 16, v162\n"
"v_and_b32 v181, 0xffff0000, v162\n"
"v_lshlrev_b32 v182, 16, v163\n"
"v_and_b32 v183, 0xffff0000, v163\n"
"v_pk_mul_f32 v[176:177], v[176:177], s[56:57]\n"
"v_pk_mul_f32 v[178:179], v[178:179], s[56:57]\n"
"v_pk_mul_f32 v[180:181], v[180:181], s[56:57]\n"
"v_pk_mul_f32 v[182:183], v[182:183], s[56:57]\n"
"v_cvt_pk_bf16_f32 v160, v176, v177\n"
"v_cvt_pk_bf16_f32 v161, v178, v179\n"
"v_cvt_pk_bf16_f32 v162, v180, v181\n"
"v_cvt_pk_bf16_f32 v163, v182, v183\n"
"v_accvgpr_write_b32 a48, v160\n"
"v_accvgpr_write_b32 a49, v161\n"
"v_accvgpr_write_b32 a50, v162\n"
"v_accvgpr_write_b32 a51, v163\n"
"s_waitcnt lgkmcnt(2)\n"
"v_lshlrev_b32 v184, 16, v164\n"
"v_and_b32 v185, 0xffff0000, v164\n"
"v_lshlrev_b32 v186, 16, v165\n"
"v_and_b32 v187, 0xffff0000, v165\n"
"v_lshlrev_b32 v188, 16, v166\n"
"v_and_b32 v189, 0xffff0000, v166\n"
"v_lshlrev_b32 v190, 16, v167\n"
"v_and_b32 v191, 0xffff0000, v167\n"
"v_pk_mul_f32 v[184:185], v[184:185], s[56:57]\n"
"v_pk_mul_f32 v[186:187], v[186:187], s[56:57]\n"
"v_pk_mul_f32 v[188:189], v[188:189], s[56:57]\n"
"v_pk_mul_f32 v[190:191], v[190:191], s[56:57]\n"
"v_cvt_pk_bf16_f32 v164, v184, v185\n"
"v_cvt_pk_bf16_f32 v165, v186, v187\n"
"v_cvt_pk_bf16_f32 v166, v188, v189\n"
"v_cvt_pk_bf16_f32 v167, v190, v191\n"
"v_accvgpr_write_b32 a52, v164\n"
"v_accvgpr_write_b32 a53, v165\n"
"v_accvgpr_write_b32 a54, v166\n"
"v_accvgpr_write_b32 a55, v167\n"
"s_waitcnt lgkmcnt(1)\n"
"v_lshlrev_b32 v176, 16, v168\n"
"v_and_b32 v177, 0xffff0000, v168\n"
"v_lshlrev_b32 v178, 16, v169\n"
"v_and_b32 v179, 0xffff0000, v169\n"
"v_lshlrev_b32 v180, 16, v170\n"
"v_and_b32 v181, 0xffff0000, v170\n"
"v_lshlrev_b32 v182, 16, v171\n"
"v_and_b32 v183, 0xffff0000, v171\n"
"v_pk_mul_f32 v[176:177], v[176:177], s[56:57]\n"
"v_pk_mul_f32 v[178:179], v[178:179], s[56:57]\n"
"v_pk_mul_f32 v[180:181], v[180:181], s[56:57]\n"
"v_pk_mul_f32 v[182:183], v[182:183], s[56:57]\n"
"v_cvt_pk_bf16_f32 v168, v176, v177\n"
"v_cvt_pk_bf16_f32 v169, v178, v179\n"
"v_cvt_pk_bf16_f32 v170, v180, v181\n"
"v_cvt_pk_bf16_f32 v171, v182, v183\n"
"v_accvgpr_write_b32 a56, v168\n"
"v_accvgpr_write_b32 a57, v169\n"
"v_accvgpr_write_b32 a58, v170\n"
"v_accvgpr_write_b32 a59, v171\n"
"s_waitcnt lgkmcnt(0)\n"
"v_lshlrev_b32 v184, 16, v172\n"
"v_and_b32 v185, 0xffff0000, v172\n"
"v_lshlrev_b32 v186, 16, v173\n"
"v_and_b32 v187, 0xffff0000, v173\n"
"v_lshlrev_b32 v188, 16, v174\n"
"v_and_b32 v189, 0xffff0000, v174\n"
"v_lshlrev_b32 v190, 16, v175\n"
"v_and_b32 v191, 0xffff0000, v175\n"
"v_pk_mul_f32 v[184:185], v[184:185], s[56:57]\n"
"v_pk_mul_f32 v[186:187], v[186:187], s[56:57]\n"
"v_pk_mul_f32 v[188:189], v[188:189], s[56:57]\n"
"v_pk_mul_f32 v[190:191], v[190:191], s[56:57]\n"
"v_cvt_pk_bf16_f32 v172, v184, v185\n"
"v_cvt_pk_bf16_f32 v173, v186, v187\n"
"v_cvt_pk_bf16_f32 v174, v188, v189\n"
"v_cvt_pk_bf16_f32 v175, v190, v191\n"
"v_accvgpr_write_b32 a60, v172\n"
"v_accvgpr_write_b32 a61, v173\n"
"v_accvgpr_write_b32 a62, v174\n"
"v_accvgpr_write_b32 a63, v175\n"
"v_mfma_f32_16x16x32_bf16 v[192:195], a[96:99], a[48:51], 0\n"
"v_exp_f32 v204, v204\n"
"v_mfma_f32_16x16x32_bf16 v[192:195], a[100:103], a[52:55], v[192:195]\n"
"v_cvt_pk_bf16_f32 v212, v200, v201\n"
"v_cvt_pk_bf16_f32 v213, v202, v203\n"
"v_mfma_f32_16x16x32_bf16 v[192:195], a[104:107], a[56:59], v[192:195]\n"
"v_exp_f32 v205, v205\n"
"v_mfma_f32_16x16x32_bf16 v[192:195], a[108:111], a[60:63], v[192:195]\n"
"v_cvt_pk_bf16_f32 v214, v204, v205\n"
"v_mfma_f32_16x16x32_bf16 v[196:199], a[112:115], a[60:63], 0\n"
"v_exp_f32 v206, v206\n"
"v_mfma_f32_16x16x32_bf16 v[196:199], a[116:119], a[56:59], v[196:199]\n"
"v_mfma_f32_16x16x32_bf16 v[196:199], a[120:123], a[52:55], v[196:199]\n"
"v_exp_f32 v207, v207\n"
"v_mfma_f32_16x16x32_bf16 v[196:199], a[124:127], a[48:51], v[196:199]\n"
"v_cvt_pk_bf16_f32 v215, v206, v207\n"
"v_mfma_f32_16x16x32_bf16 v[200:203], a[128:131], a[48:51], 0\n"
"v_mfma_f32_16x16x32_bf16 v[200:203], a[132:135], a[52:55], v[200:203]\n"
"v_mfma_f32_16x16x32_bf16 v[200:203], a[136:139], a[56:59], v[200:203]\n"
"v_mfma_f32_16x16x32_bf16 v[200:203], a[140:143], a[60:63], v[200:203]\n"
"v_mfma_f32_16x16x32_bf16 v[204:207], a[144:147], a[60:63], 0\n"
"v_mfma_f32_16x16x32_bf16 v[204:207], a[148:151], a[56:59], v[204:207]\n"
"v_mfma_f32_16x16x32_bf16 v[204:207], a[152:155], a[52:55], v[204:207]\n"
"v_max3_f32 v253, v192, v193, v232\n"
"v_max3_f32 v254, v194, v195, v196\n"
"v_mfma_f32_16x16x32_bf16 v[204:207], a[156:159], a[48:51], v[204:207]\n"
"v_max3_f32 v253, v253, v197, v198\n"
"v_smfmac_f32_16x16x64_bf16 a[232:235], a[248:251], v[208:215], v252\n"
"v_max3_f32 v254, v254, v199, v200\n"
"v_mfma_f32_16x16x32_bf16 v[64:67], v[208:211], a[160:163], 0\n"
"v_max3_f32 v253, v253, v201, v202\n"
"v_mfma_f32_16x16x32_bf16 v[64:67], v[212:215], a[164:167], v[64:67]\n"
"v_mfma_f32_16x16x32_bf16 v[68:71], v[212:215], a[168:171], 0\n"
"s_nop 0\n"
"v_max3_f32 v254, v254, v203, v204\n"
"v_max3_f32 v253, v253, v205, v206\n"
"v_max3_f32 v253, v253, v254, v207\n"
"v_mov_b32 v254, v253\n"
"v_mfma_f32_16x16x32_bf16 v[68:71], v[208:211], a[172:175], v[68:71]\n"
"s_nop 0\n"
"v_permlane32_swap_b32 v253, v254\n"
"v_max_f32 v253, v253, v254\n"
"v_mov_b32 v254, v253\n"
"v_mfma_f32_16x16x32_bf16 v[72:75], v[208:211], a[176:179], 0\n"
"s_nop 0\n"
"v_permlane16_swap_b32 v253, v254\n"
"v_max_f32 v253, v253, v254\n"
"v_sub_f32 v192, v192, v253\n"
"v_mfma_f32_16x16x32_bf16 v[72:75], v[212:215], a[180:183], v[72:75]\n"
"v_sub_f32 v193, v193, v253\n"
"v_sub_f32 v194, v194, v253\n"
"v_sub_f32 v195, v195, v253\n"
"v_mfma_f32_16x16x32_bf16 v[76:79], v[212:215], a[184:187], 0\n"
"v_sub_f32 v196, v196, v253\n"
"v_sub_f32 v197, v197, v253\n"
"v_sub_f32 v198, v198, v253\n"
"v_sub_f32 v199, v199, v253\n"
"v_mfma_f32_16x16x32_bf16 v[76:79], v[208:211], a[188:191], v[76:79]\n"
"v_sub_f32 v200, v200, v253\n"
"v_sub_f32 v201, v201, v253\n"
"v_sub_f32 v202, v202, v253\n"
"v_mfma_f32_16x16x32_bf16 v[80:83], v[208:211], a[192:195], 0\n"
"v_sub_f32 v203, v203, v253\n"
"v_sub_f32 v204, v204, v253\n"
"v_sub_f32 v205, v205, v253\n"
"v_mfma_f32_16x16x32_bf16 v[80:83], v[212:215], a[196:199], v[80:83]\n"
"v_sub_f32 v206, v206, v253\n"
"v_sub_f32 v207, v207, v253\n"
"v_sub_f32 v232, 0, v253\n"
"v_mfma_f32_16x16x32_bf16 v[84:87], v[212:215], a[200:203], 0\n"
"v_sub_f32 v233, 0, v253\n"
"v_sub_f32 v234, 0, v253\n"
"v_sub_f32 v235, 0, v253\n"
"v_mfma_f32_16x16x32_bf16 v[84:87], v[208:211], a[204:207], v[84:87]\n"
"v_exp_f32 v192, v192\n"
"v_exp_f32 v193, v193\n"
"v_exp_f32 v194, v194\n"
"v_exp_f32 v195, v195\n"
"v_mfma_f32_16x16x32_bf16 v[88:91], v[208:211], a[208:211], 0\n"
"v_cvt_pk_bf16_f32 v216, v192, v193\n"
"v_exp_f32 v196, v196\n"
"v_exp_f32 v197, v197\n"
"v_mfma_f32_16x16x32_bf16 v[88:91], v[212:215], a[212:215], v[88:91]\n"
"v_cvt_pk_bf16_f32 v217, v194, v195\n"
"v_exp_f32 v198, v198\n"
"v_exp_f32 v199, v199\n"
"v_mfma_f32_16x16x32_bf16 v[92:95], v[212:215], a[216:219], 0\n"
"v_cvt_pk_bf16_f32 v218, v196, v197\n"
"v_exp_f32 v200, v200\n"
"v_cvt_pk_bf16_f32 v219, v198, v199\n"
"v_mfma_f32_16x16x32_bf16 v[92:95], v[208:211], a[220:223], v[92:95]\n"
"v_exp_f32 v201, v201\n"
"v_exp_f32 v202, v202\n"
"v_exp_f32 v203, v203\n"
"s_waitcnt vmcnt(4)\n"
"s_add_u32 m0, s53, 32768\n"
"s_add_u32 s24, s52, 16384\n"
"buffer_load_dwordx4 v250, s[40:43], s24 offen offset:2048 nt lds\n"
"buffer_load_dwordx4 v251, s[40:43], s24 offen offset:3072 nt lds\n"
"s_add_u32 m0, s53, 49152\n"
"s_add_u32 s24, s52, 24576\n"
"buffer_load_dwordx4 v250, s[40:43], s24 offen offset:2048 nt lds\n"
"buffer_load_dwordx4 v251, s[40:43], s24 offen offset:3072 nt lds\n"
"v_add_u32 v134, 65536, v134\n"
"v_add_u32 v135, 65536, v135\n"
"ds_read_b128 v[160:163], v134 offset:0\n"
"ds_read_b128 v[164:167], v135 offset:0\n"
"ds_read_b128 v[168:171], v134 offset:1024\n"
"ds_read_b128 v[172:175], v135 offset:1024\n"
"s_waitcnt lgkmcnt(3)\n"
"v_lshlrev_b32 v176, 16, v160\n"
"v_and_b32 v177, 0xffff0000, v160\n"
"v_lshlrev_b32 v178, 16, v161\n"
"v_and_b32 v179, 0xffff0000, v161\n"
"v_lshlrev_b32 v180, 16, v162\n"
"v_and_b32 v181, 0xffff0000, v162\n"
"v_lshlrev_b32 v182, 16, v163\n"
"v_and_b32 v183, 0xffff0000, v163\n"
"v_pk_mul_f32 v[176:177], v[176:177], s[56:57]\n"
"v_pk_mul_f32 v[178:179], v[178:179], s[56:57]\n"
"v_pk_mul_f32 v[180:181], v[180:181], s[56:57]\n"
"v_pk_mul_f32 v[182:183], v[182:183], s[56:57]\n"
"v_cvt_pk_bf16_f32 v160, v176, v177\n"
"v_cvt_pk_bf16_f32 v161, v178, v179\n"
"v_cvt_pk_bf16_f32 v162, v180, v181\n"
"v_cvt_pk_bf16_f32 v163, v182, v183\n"
"v_accvgpr_write_b32 a64, v160\n"
"v_accvgpr_write_b32 a65, v161\n"
"v_accvgpr_write_b32 a66, v162\n"
"v_accvgpr_write_b32 a67, v163\n"
"s_waitcnt lgkmcnt(2)\n"
"v_lshlrev_b32 v184, 16, v164\n"
"v_and_b32 v185, 0xffff0000, v164\n"
"v_lshlrev_b32 v186, 16, v165\n"
"v_and_b32 v187, 0xffff0000, v165\n"
"v_lshlrev_b32 v188, 16, v166\n"
"v_and_b32 v189, 0xffff0000, v166\n"
"v_lshlrev_b32 v190, 16, v167\n"
"v_and_b32 v191, 0xffff0000, v167\n"
"v_pk_mul_f32 v[184:185], v[184:185], s[56:57]\n"
"v_pk_mul_f32 v[186:187], v[186:187], s[56:57]\n"
"v_pk_mul_f32 v[188:189], v[188:189], s[56:57]\n"
"v_pk_mul_f32 v[190:191], v[190:191], s[56:57]\n"
"v_cvt_pk_bf16_f32 v164, v184, v185\n"
"v_cvt_pk_bf16_f32 v165, v186, v187\n"
"v_cvt_pk_bf16_f32 v166, v188, v189\n"
"v_cvt_pk_bf16_f32 v167, v190, v191\n"
"v_accvgpr_write_b32 a68, v164\n"
"v_accvgpr_write_b32 a69, v165\n"
"v_accvgpr_write_b32 a70, v166\n"
"v_accvgpr_write_b32 a71, v167\n"
"s_waitcnt lgkmcnt(1)\n"
"v_lshlrev_b32 v176, 16, v168\n"
"v_and_b32 v177, 0xffff0000, v168\n"
"v_lshlrev_b32 v178, 16, v169\n"
"v_and_b32 v179, 0xffff0000, v169\n"
"v_lshlrev_b32 v180, 16, v170\n"
"v_and_b32 v181, 0xffff0000, v170\n"
"v_lshlrev_b32 v182, 16, v171\n"
"v_and_b32 v183, 0xffff0000, v171\n"
"v_pk_mul_f32 v[176:177], v[176:177], s[56:57]\n"
"v_pk_mul_f32 v[178:179], v[178:179], s[56:57]\n"
"v_pk_mul_f32 v[180:181], v[180:181], s[56:57]\n"
"v_pk_mul_f32 v[182:183], v[182:183], s[56:57]\n"
"v_cvt_pk_bf16_f32 v168, v176, v177\n"
"v_cvt_pk_bf16_f32 v169, v178, v179\n"
"v_cvt_pk_bf16_f32 v170, v180, v181\n"
"v_cvt_pk_bf16_f32 v171, v182, v183\n"
"v_accvgpr_write_b32 a72, v168\n"
"v_accvgpr_write_b32 a73, v169\n"
"v_accvgpr_write_b32 a74, v170\n"
"v_accvgpr_write_b32 a75, v171\n"
"s_waitcnt lgkmcnt(0)\n"
"v_lshlrev_b32 v184, 16, v172\n"
"v_and_b32 v185, 0xffff0000, v172\n"
"v_lshlrev_b32 v186, 16, v173\n"
"v_and_b32 v187, 0xffff0000, v173\n"
"v_lshlrev_b32 v188, 16, v174\n"
"v_and_b32 v189, 0xffff0000, v174\n"
"v_lshlrev_b32 v190, 16, v175\n"
"v_and_b32 v191, 0xffff0000, v175\n"
"v_pk_mul_f32 v[184:185], v[184:185], s[56:57]\n"
"v_pk_mul_f32 v[186:187], v[186:187], s[56:57]\n"
"v_pk_mul_f32 v[188:189], v[188:189], s[56:57]\n"
"v_pk_mul_f32 v[190:191], v[190:191], s[56:57]\n"
"v_cvt_pk_bf16_f32 v172, v184, v185\n"
"v_cvt_pk_bf16_f32 v173, v186, v187\n"
"v_cvt_pk_bf16_f32 v174, v188, v189\n"
"v_cvt_pk_bf16_f32 v175, v190, v191\n"
"v_accvgpr_write_b32 a76, v172\n"
"v_accvgpr_write_b32 a77, v173\n"
"v_accvgpr_write_b32 a78, v174\n"
"v_accvgpr_write_b32 a79, v175\n"
"v_mfma_f32_16x16x32_bf16 v[192:195], a[96:99], a[64:67], 0\n"
"v_exp_f32 v204, v204\n"
"v_mfma_f32_16x16x32_bf16 v[192:195], a[100:103], a[68:71], v[192:195]\n"
"v_cvt_pk_bf16_f32 v212, v200, v201\n"
"v_cvt_pk_bf16_f32 v213, v202, v203\n"
"v_mfma_f32_16x16x32_bf16 v[192:195], a[104:107], a[72:75], v[192:195]\n"
"v_exp_f32 v205, v205\n"
"v_mfma_f32_16x16x32_bf16 v[192:195], a[108:111], a[76:79], v[192:195]\n"
"v_cvt_pk_bf16_f32 v214, v204, v205\n"
"v_mfma_f32_16x16x32_bf16 v[196:199], a[112:115], a[76:79], 0\n"
"v_exp_f32 v206, v206\n"
"v_mfma_f32_16x16x32_bf16 v[196:199], a[116:119], a[72:75], v[196:199]\n"
"v_mfma_f32_16x16x32_bf16 v[196:199], a[120:123], a[68:71], v[196:199]\n"
"v_exp_f32 v207, v207\n"
"v_mfma_f32_16x16x32_bf16 v[196:199], a[124:127], a[64:67], v[196:199]\n"
"v_cvt_pk_bf16_f32 v215, v206, v207\n"
"v_mfma_f32_16x16x32_bf16 v[200:203], a[128:131], a[64:67], 0\n"
"v_mfma_f32_16x16x32_bf16 v[200:203], a[132:135], a[68:71], v[200:203]\n"
"v_mfma_f32_16x16x32_bf16 v[200:203], a[136:139], a[72:75], v[200:203]\n"
"v_mfma_f32_16x16x32_bf16 v[200:203], a[140:143], a[76:79], v[200:203]\n"
"v_mfma_f32_16x16x32_bf16 v[204:207], a[144:147], a[76:79], 0\n"
"v_mfma_f32_16x16x32_bf16 v[204:207], a[148:151], a[72:75], v[204:207]\n"
"v_mfma_f32_16x16x32_bf16 v[204:207], a[152:155], a[68:71], v[204:207]\n"
"v_max3_f32 v253, v192, v193, v236\n"
"v_max3_f32 v254, v194, v195, v196\n"
"v_mfma_f32_16x16x32_bf16 v[204:207], a[156:159], a[64:67], v[204:207]\n"
"v_max3_f32 v253, v253, v197, v198\n"
"v_smfmac_f32_16x16x64_bf16 a[236:239], a[248:251], v[212:219], v252\n"
"v_max3_f32 v254, v254, v199, v200\n"
"v_mfma_f32_16x16x32_bf16 v[96:99], v[216:219], a[160:163], 0\n"
"v_max3_f32 v253, v253, v201, v202\n"
"v_mfma_f32_16x16x32_bf16 v[96:99], v[212:215], a[164:167], v[96:99]\n"
"v_mfma_f32_16x16x32_bf16 v[100:103], v[212:215], a[168:171], 0\n"
"s_nop 0\n"
"v_max3_f32 v254, v254, v203, v204\n"
"v_max3_f32 v253, v253, v205, v206\n"
"v_max3_f32 v253, v253, v254, v207\n"
"v_mov_b32 v254, v253\n"
"v_mfma_f32_16x16x32_bf16 v[100:103], v[216:219], a[172:175], v[100:103]\n"
"s_nop 0\n"
"v_permlane32_swap_b32 v253, v254\n"
"v_max_f32 v253, v253, v254\n"
"v_mov_b32 v254, v253\n"
"v_mfma_f32_16x16x32_bf16 v[104:107], v[216:219], a[176:179], 0\n"
"s_nop 0\n"
"v_permlane16_swap_b32 v253, v254\n"
"v_max_f32 v253, v253, v254\n"
"v_sub_f32 v192, v192, v253\n"
"v_mfma_f32_16x16x32_bf16 v[104:107], v[212:215], a[180:183], v[104:107]\n"
"v_sub_f32 v193, v193, v253\n"
"v_sub_f32 v194, v194, v253\n"
"v_sub_f32 v195, v195, v253\n"
"v_mfma_f32_16x16x32_bf16 v[108:111], v[212:215], a[184:187], 0\n"
"v_sub_f32 v196, v196, v253\n"
"v_sub_f32 v197, v197, v253\n"
"v_sub_f32 v198, v198, v253\n"
"v_sub_f32 v199, v199, v253\n"
"v_mfma_f32_16x16x32_bf16 v[108:111], v[216:219], a[188:191], v[108:111]\n"
"v_sub_f32 v200, v200, v253\n"
"v_sub_f32 v201, v201, v253\n"
"v_sub_f32 v202, v202, v253\n"
"v_mfma_f32_16x16x32_bf16 v[112:115], v[216:219], a[192:195], 0\n"
"v_sub_f32 v203, v203, v253\n"
"v_sub_f32 v204, v204, v253\n"
"v_sub_f32 v205, v205, v253\n"
"v_mfma_f32_16x16x32_bf16 v[112:115], v[212:215], a[196:199], v[112:115]\n"
"v_sub_f32 v206, v206, v253\n"
"v_sub_f32 v207, v207, v253\n"
"v_sub_f32 v236, 0, v253\n"
"v_mfma_f32_16x16x32_bf16 v[116:119], v[212:215], a[200:203], 0\n"
"v_sub_f32 v237, 0, v253\n"
"v_sub_f32 v238, 0, v253\n"
"v_sub_f32 v239, 0, v253\n"
"v_mfma_f32_16x16x32_bf16 v[116:119], v[216:219], a[204:207], v[116:119]\n"
"v_exp_f32 v192, v192\n"
"v_exp_f32 v193, v193\n"
"v_exp_f32 v194, v194\n"
"v_exp_f32 v195, v195\n"
"v_mfma_f32_16x16x32_bf16 v[120:123], v[216:219], a[208:211], 0\n"
"v_cvt_pk_bf16_f32 v208, v192, v193\n"
"v_exp_f32 v196, v196\n"
"v_exp_f32 v197, v197\n"
"v_mfma_f32_16x16x32_bf16 v[120:123], v[212:215], a[212:215], v[120:123]\n"
"v_cvt_pk_bf16_f32 v209, v194, v195\n"
"v_exp_f32 v198, v198\n"
"v_exp_f32 v199, v199\n"
"v_mfma_f32_16x16x32_bf16 v[124:127], v[212:215], a[216:219], 0\n"
"v_cvt_pk_bf16_f32 v210, v196, v197\n"
"v_exp_f32 v200, v200\n"
"v_cvt_pk_bf16_f32 v211, v198, v199\n"
"v_mfma_f32_16x16x32_bf16 v[124:127], v[216:219], a[220:223], v[124:127]\n"
"v_exp_f32 v201, v201\n"
"v_exp_f32 v202, v202\n"
"v_exp_f32 v203, v203\n"
"s_waitcnt vmcnt(4)\n"
"s_barrier\n"
"ds_read_b128 a[96:99], v244 offset:32768\n"
"ds_read_b128 a[100:103], v245 offset:32768\n"
"ds_read_b128 a[104:107], v244 offset:33792\n"
"ds_read_b128 a[108:111], v245 offset:33792\n"
"ds_read_b128 a[112:115], v245 offset:41984\n"
"ds_read_b128 a[116:119], v244 offset:41984\n"
"ds_read_b128 a[120:123], v245 offset:40960\n"
"ds_read_b128 a[124:127], v244 offset:40960\n"
"ds_read_b128 a[128:131], v244 offset:49152\n"
"ds_read_b128 a[132:135], v245 offset:49152\n"
"ds_read_b128 a[136:139], v244 offset:50176\n"
"ds_read_b128 a[140:143], v245 offset:50176\n"
"ds_read_b128 a[144:147], v245 offset:58368\n"
"ds_read_b128 a[148:151], v244 offset:58368\n"
"ds_read_b128 a[152:155], v245 offset:57344\n"
"ds_read_b128 a[156:159], v244 offset:57344\n"
"s_add_u32 m0, s53, 65536\n"
"s_add_u32 s24, s52, 32768\n"
"buffer_load_dwordx4 v248, s[36:39], s24 offen nt lds\n"
"buffer_load_dwordx4 v249, s[36:39], s24 offen offset:1024 nt lds\n"
"buffer_load_dwordx4 v250, s[40:43], s24 offen offset:2048 nt lds\n"
"buffer_load_dwordx4 v251, s[40:43], s24 offen offset:3072 nt lds\n"
"s_waitcnt lgkmcnt(8)\n"
"s_lshl_b32 s55, %[wid], 2\n"
"s_add_u32 s55, s55, 161792\n"
"v_mov_b32 v253, s55\n"
"v_mbcnt_lo_u32_b32 v254, -1, 0\n"
"v_and_b32 v254, 3, v254\n"
"v_lshlrev_b32 v254, 2, v254\n"
"v_add_u32 v254, 161792, v254\n"
"s_xor_b32 s25, s53, 0x10000\n"
"s_mov_b32 s26, 3\n"
"v_mfma_f32_16x16x32_bf16 v[192:195], a[96:99], a[0:3], v[220:223]\n"
"v_exp_f32 v204, v204\n"
"v_mfma_f32_16x16x32_bf16 v[192:195], a[100:103], a[4:7], v[192:195]\n"
"v_cvt_pk_bf16_f32 v212, v200, v201\n"
"v_cvt_pk_bf16_f32 v213, v202, v203\n"
"v_mfma_f32_16x16x32_bf16 v[192:195], a[104:107], a[8:11], v[192:195]\n"
"v_exp_f32 v205, v205\n"
"v_mfma_f32_16x16x32_bf16 v[192:195], a[108:111], a[12:15], v[192:195]\n"
"v_cvt_pk_bf16_f32 v214, v204, v205\n"
"v_mfma_f32_16x16x32_bf16 v[196:199], a[112:115], a[12:15], v[220:223]\n"
"v_exp_f32 v206, v206\n"
"v_mfma_f32_16x16x32_bf16 v[196:199], a[116:119], a[8:11], v[196:199]\n"
"v_mfma_f32_16x16x32_bf16 v[196:199], a[120:123], a[4:7], v[196:199]\n"
"v_exp_f32 v207, v207\n"
"v_mfma_f32_16x16x32_bf16 v[196:199], a[124:127], a[0:3], v[196:199]\n"
"v_cvt_pk_bf16_f32 v215, v206, v207\n"
"s_waitcnt lgkmcnt(0)\n"
"v_mfma_f32_16x16x32_bf16 v[200:203], a[128:131], a[0:3], v[220:223]\n"
"v_exp_f32 v192, v192\n"
"v_mfma_f32_16x16x32_bf16 v[200:203], a[132:135], a[4:7], v[200:203]\n"
"v_mfma_f32_16x16x32_bf16 v[200:203], a[136:139], a[8:11], v[200:203]\n"
"v_exp_f32 v193, v193\n"
"v_mfma_f32_16x16x32_bf16 v[200:203], a[140:143], a[12:15], v[200:203]\n"
"v_cvt_pk_bf16_f32 v216, v192, v193\n"
"v_mfma_f32_16x16x32_bf16 v[204:207], a[144:147], a[12:15], v[220:223]\n"
"v_exp_f32 v194, v194\n"
"v_mfma_f32_16x16x32_bf16 v[204:207], a[148:151], a[8:11], v[204:207]\n"
"v_mfma_f32_16x16x32_bf16 v[204:207], a[152:155], a[4:7], v[204:207]\n"
"v_exp_f32 v195, v195\n"
"v_mfma_f32_16x16x32_bf16 v[204:207], a[156:159], a[0:3], v[204:207]\n"
"v_cvt_pk_bf16_f32 v217, v194, v195\n"
"s_waitcnt vmcnt(4)\n"
"s_barrier\n"
"v_smfmac_f32_16x16x64_bf16 a[240:243], a[248:251], v[208:215], v252\n"
"v_exp_f32 v196, v196\n"
"v_mfma_f32_16x16x32_bf16 v[128:131], v[208:211], a[160:163], 0\n"
"ds_read_b64_tr_b16 a[160:161], v246 offset:34816\n"
"ds_read_b64_tr_b16 a[162:163], v246 offset:43008\n"
"v_mfma_f32_16x16x32_bf16 v[128:131], v[212:215], a[164:167], v[128:131]\n"
"v_exp_f32 v197, v197\n"
"v_mfma_f32_16x16x32_bf16 v[132:135], v[212:215], a[168:171], 0\n"
"ds_read_b64_tr_b16 a[164:165], v246 offset:51200\n"
"v_cvt_pk_bf16_f32 v218, v196, v197\n"
"v_mfma_f32_16x16x32_bf16 v[132:135], v[208:211], a[172:175], v[132:135]\n"
"v_exp_f32 v198, v198\n"
"v_mfma_f32_16x16x32_bf16 v[136:139], v[208:211], a[176:179], 0\n"
"ds_read_b64_tr_b16 a[166:167], v246 offset:59392\n"
"ds_read_b64_tr_b16 a[168:169], v247 offset:51200\n"
"v_mfma_f32_16x16x32_bf16 v[136:139], v[212:215], a[180:183], v[136:139]\n"
"v_exp_f32 v199, v199\n"
"v_mfma_f32_16x16x32_bf16 v[140:143], v[212:215], a[184:187], 0\n"
"ds_read_b64_tr_b16 a[170:171], v247 offset:59392\n"
"v_cvt_pk_bf16_f32 v219, v198, v199\n"
"v_mfma_f32_16x16x32_bf16 v[140:143], v[208:211], a[188:191], v[140:143]\n"
"v_exp_f32 v200, v200\n"
"v_mfma_f32_16x16x32_bf16 v[144:147], v[208:211], a[192:195], 0\n"
"ds_read_b64_tr_b16 a[172:173], v247 offset:34816\n"
"ds_read_b64_tr_b16 a[174:175], v247 offset:43008\n"
"v_mfma_f32_16x16x32_bf16 v[144:147], v[212:215], a[196:199], v[144:147]\n"
"v_exp_f32 v201, v201\n"
"v_mfma_f32_16x16x32_bf16 v[148:151], v[212:215], a[200:203], 0\n"
"ds_read_b64_tr_b16 a[176:177], v246 offset:35328\n"
"ds_read_b64_tr_b16 a[178:179], v246 offset:43520\n"
"v_mfma_f32_16x16x32_bf16 v[148:151], v[208:211], a[204:207], v[148:151]\n"
"v_exp_f32 v202, v202\n"
"v_mfma_f32_16x16x32_bf16 v[152:155], v[208:211], a[208:211], 0\n"
"ds_read_b64_tr_b16 a[180:181], v246 offset:51712\n"
"ds_read_b64_tr_b16 a[182:183], v246 offset:59904\n"
"v_mfma_f32_16x16x32_bf16 v[152:155], v[212:215], a[212:215], v[152:155]\n"
"v_exp_f32 v203, v203\n"
"v_mfma_f32_16x16x32_bf16 v[156:159], v[212:215], a[216:219], 0\n"
"ds_read_b64_tr_b16 a[184:185], v247 offset:51712\n"
"ds_read_b64_tr_b16 a[186:187], v247 offset:59904\n"
"v_mfma_f32_16x16x32_bf16 v[156:159], v[208:211], a[220:223], v[156:159]\n"
"ds_read_b64_tr_b16 a[188:189], v247 offset:35328\n"
"ds_read_b64_tr_b16 a[190:191], v247 offset:43520\n"
"v_mfma_f32_16x16x32_bf16 v[192:195], a[96:99], a[16:19], v[224:227]\n"
"v_exp_f32 v204, v204\n"
"v_mfma_f32_16x16x32_bf16 v[192:195], a[100:103], a[20:23], v[192:195]\n"
"v_cvt_pk_bf16_f32 v212, v200, v201\n"
"v_cvt_pk_bf16_f32 v213, v202, v203\n"
"v_mfma_f32_16x16x32_bf16 v[192:195], a[104:107], a[24:27], v[192:195]\n"
"v_exp_f32 v205, v205\n"
"v_mfma_f32_16x16x32_bf16 v[192:195], a[108:111], a[28:31], v[192:195]\n"
"ds_read_b64_tr_b16 a[192:193], v246 offset:35840\n"
"v_cvt_pk_bf16_f32 v214, v204, v205\n"
"v_mfma_f32_16x16x32_bf16 v[196:199], a[112:115], a[28:31], v[224:227]\n"
"v_exp_f32 v206, v206\n"
"v_mfma_f32_16x16x32_bf16 v[196:199], a[116:119], a[24:27], v[196:199]\n"
"ds_read_b64_tr_b16 a[194:195], v246 offset:44032\n"
"ds_read_b64_tr_b16 a[196:197], v246 offset:52224\n"
"v_mfma_f32_16x16x32_bf16 v[196:199], a[120:123], a[20:23], v[196:199]\n"
"v_exp_f32 v207, v207\n"
"v_mfma_f32_16x16x32_bf16 v[196:199], a[124:127], a[16:19], v[196:199]\n"
"ds_read_b64_tr_b16 a[198:199], v246 offset:60416\n"
"v_cvt_pk_bf16_f32 v215, v206, v207\n"
"v_mfma_f32_16x16x32_bf16 v[200:203], a[128:131], a[16:19], v[224:227]\n"
"v_exp_f32 v192, v192\n"
"v_mfma_f32_16x16x32_bf16 v[200:203], a[132:135], a[20:23], v[200:203]\n"
"ds_read_b64_tr_b16 a[200:201], v247 offset:52224\n"
"ds_read_b64_tr_b16 a[202:203], v247 offset:60416\n"
"v_mfma_f32_16x16x32_bf16 v[200:203], a[136:139], a[24:27], v[200:203]\n"
"v_exp_f32 v193, v193\n"
"v_mfma_f32_16x16x32_bf16 v[200:203], a[140:143], a[28:31], v[200:203]\n"
"ds_read_b64_tr_b16 a[204:205], v247 offset:35840\n"
"v_cvt_pk_bf16_f32 v208, v192, v193\n"
"v_mfma_f32_16x16x32_bf16 v[204:207], a[144:147], a[28:31], v[224:227]\n"
"v_exp_f32 v194, v194\n"
"v_mfma_f32_16x16x32_bf16 v[204:207], a[148:151], a[24:27], v[204:207]\n"
"ds_read_b64_tr_b16 a[206:207], v247 offset:44032\n"
"ds_read_b64_tr_b16 a[208:209], v246 offset:36352\n"
"v_mfma_f32_16x16x32_bf16 v[204:207], a[152:155], a[20:23], v[204:207]\n"
"v_exp_f32 v195, v195\n"
"v_mfma_f32_16x16x32_bf16 v[204:207], a[156:159], a[16:19], v[204:207]\n"
"v_cvt_pk_bf16_f32 v209, v194, v195\n"
"s_waitcnt lgkmcnt(15)\n"
"v_smfmac_f32_16x16x64_bf16 a[224:227], a[248:251], v[212:219], v252\n"
"v_exp_f32 v196, v196\n"
"v_mfma_f32_16x16x32_bf16 v[0:3], v[216:219], a[160:163], v[0:3]\n"
"ds_read_b64_tr_b16 a[210:211], v246 offset:44544\n"
"ds_read_b64_tr_b16 a[212:213], v246 offset:52736\n"
"ds_read_b64_tr_b16 a[214:215], v246 offset:60928\n"
"v_mfma_f32_16x16x32_bf16 v[0:3], v[212:215], a[164:167], v[0:3]\n"
"ds_read_b64_tr_b16 a[216:217], v247 offset:52736\n"
"v_exp_f32 v197, v197\n"
"v_mfma_f32_16x16x32_bf16 v[4:7], v[212:215], a[168:171], v[4:7]\n"
"v_cvt_pk_bf16_f32 v210, v196, v197\n"
"s_waitcnt lgkmcnt(13)\n"
"ds_read_b64_tr_b16 a[218:219], v247 offset:60928\n"
"v_mfma_f32_16x16x32_bf16 v[4:7], v[216:219], a[172:175], v[4:7]\n"
"ds_read_b64_tr_b16 a[220:221], v247 offset:36352\n"
"v_exp_f32 v198, v198\n"
"v_mfma_f32_16x16x32_bf16 v[8:11], v[216:219], a[176:179], v[8:11]\n"
"ds_read_b64_tr_b16 a[222:223], v247 offset:44544\n"
"s_add_u32 m0, s25, 16384\n"
"s_add_u32 s24, s24, 8192\n"
"v_mfma_f32_16x16x32_bf16 v[8:11], v[212:215], a[180:183], v[8:11]\n"
"v_exp_f32 v199, v199\n"
"v_mfma_f32_16x16x32_bf16 v[12:15], v[212:215], a[184:187], v[12:15]\n"
"v_cvt_pk_bf16_f32 v211, v198, v199\n"
"s_waitcnt lgkmcnt(8)\n"
"v_mfma_f32_16x16x32_bf16 v[12:15], v[216:219], a[188:191], v[12:15]\n"
"v_exp_f32 v200, v200\n"
"v_mfma_f32_16x16x32_bf16 v[16:19], v[216:219], a[192:195], v[16:19]\n"
"buffer_load_dwordx4 v248, s[36:39], s24 offen nt lds\n"
"v_mfma_f32_16x16x32_bf16 v[16:19], v[212:215], a[196:199], v[16:19]\n"
"v_exp_f32 v201, v201\n"
"v_mfma_f32_16x16x32_bf16 v[20:23], v[212:215], a[200:203], v[20:23]\n"
"s_waitcnt lgkmcnt(0)\n"
"v_mfma_f32_16x16x32_bf16 v[20:23], v[216:219], a[204:207], v[20:23]\n"
"v_exp_f32 v202, v202\n"
"v_mfma_f32_16x16x32_bf16 v[24:27], v[216:219], a[208:211], v[24:27]\n"
"buffer_load_dwordx4 v249, s[36:39], s24 offen offset:1024 nt lds\n"
"v_mfma_f32_16x16x32_bf16 v[24:27], v[212:215], a[212:215], v[24:27]\n"
"v_exp_f32 v203, v203\n"
"v_mfma_f32_16x16x32_bf16 v[28:31], v[212:215], a[216:219], v[28:31]\n"
"v_mfma_f32_16x16x32_bf16 v[28:31], v[216:219], a[220:223], v[28:31]\n"
"v_mfma_f32_16x16x32_bf16 v[192:195], a[96:99], a[32:35], v[228:231]\n"
"v_exp_f32 v204, v204\n"
"v_mfma_f32_16x16x32_bf16 v[192:195], a[100:103], a[36:39], v[192:195]\n"
"v_cvt_pk_bf16_f32 v212, v200, v201\n"
"v_cvt_pk_bf16_f32 v213, v202, v203\n"
"v_mfma_f32_16x16x32_bf16 v[192:195], a[104:107], a[40:43], v[192:195]\n"
"v_exp_f32 v205, v205\n"
"v_mfma_f32_16x16x32_bf16 v[192:195], a[108:111], a[44:47], v[192:195]\n"
"v_cvt_pk_bf16_f32 v214, v204, v205\n"
"v_mfma_f32_16x16x32_bf16 v[196:199], a[112:115], a[44:47], v[228:231]\n"
"v_exp_f32 v206, v206\n"
"v_mfma_f32_16x16x32_bf16 v[196:199], a[116:119], a[40:43], v[196:199]\n"
"buffer_load_dwordx4 v250, s[40:43], s24 offen offset:2048 nt lds\n"
"v_mfma_f32_16x16x32_bf16 v[196:199], a[120:123], a[36:39], v[196:199]\n"
"v_exp_f32 v207, v207\n"
"v_mfma_f32_16x16x32_bf16 v[196:199], a[124:127], a[32:35], v[196:199]\n"
"v_cvt_pk_bf16_f32 v215, v206, v207\n"
"v_mfma_f32_16x16x32_bf16 v[200:203], a[128:131], a[32:35], v[228:231]\n"
"v_exp_f32 v192, v192\n"
"v_mfma_f32_16x16x32_bf16 v[200:203], a[132:135], a[36:39], v[200:203]\n"
"buffer_load_dwordx4 v251, s[40:43], s24 offen offset:3072 nt lds\n"
"v_mfma_f32_16x16x32_bf16 v[200:203], a[136:139], a[40:43], v[200:203]\n"
"v_exp_f32 v193, v193\n"
"v_mfma_f32_16x16x32_bf16 v[200:203], a[140:143], a[44:47], v[200:203]\n"
"v_cvt_pk_bf16_f32 v216, v192, v193\n"
"s_add_u32 m0, s25, 32768\n"
"s_add_u32 s24, s24, 8192\n"
"v_mfma_f32_16x16x32_bf16 v[204:207], a[144:147], a[44:47], v[228:231]\n"
"v_exp_f32 v194, v194\n"
"v_mfma_f32_16x16x32_bf16 v[204:207], a[148:151], a[40:43], v[204:207]\n"
"buffer_load_dwordx4 v248, s[36:39], s24 offen nt lds\n"
"v_mfma_f32_16x16x32_bf16 v[204:207], a[152:155], a[36:39], v[204:207]\n"
"v_exp_f32 v195, v195\n"
"v_mfma_f32_16x16x32_bf16 v[204:207], a[156:159], a[32:35], v[204:207]\n"
"v_cvt_pk_bf16_f32 v217, v194, v195\n"
"v_smfmac_f32_16x16x64_bf16 a[228:231], a[248:251], v[208:215], v252\n"
"v_exp_f32 v196, v196\n"
"v_mfma_f32_16x16x32_bf16 v[32:35], v[208:211], a[160:163], v[32:35]\n"
"buffer_load_dwordx4 v249, s[36:39], s24 offen offset:1024 nt lds\n"
"v_mfma_f32_16x16x32_bf16 v[32:35], v[212:215], a[164:167], v[32:35]\n"
"v_exp_f32 v197, v197\n"
"v_mfma_f32_16x16x32_bf16 v[36:39], v[212:215], a[168:171], v[36:39]\n"
"v_cvt_pk_bf16_f32 v218, v196, v197\n"
"v_mfma_f32_16x16x32_bf16 v[36:39], v[208:211], a[172:175], v[36:39]\n"
"v_exp_f32 v198, v198\n"
"v_mfma_f32_16x16x32_bf16 v[40:43], v[208:211], a[176:179], v[40:43]\n"
"buffer_load_dwordx4 v250, s[40:43], s24 offen offset:2048 nt lds\n"
"v_mfma_f32_16x16x32_bf16 v[40:43], v[212:215], a[180:183], v[40:43]\n"
"v_exp_f32 v199, v199\n"
"v_mfma_f32_16x16x32_bf16 v[44:47], v[212:215], a[184:187], v[44:47]\n"
"v_cvt_pk_bf16_f32 v219, v198, v199\n"
"v_mfma_f32_16x16x32_bf16 v[44:47], v[208:211], a[188:191], v[44:47]\n"
"v_exp_f32 v200, v200\n"
"v_mfma_f32_16x16x32_bf16 v[48:51], v[208:211], a[192:195], v[48:51]\n"
"s_add_u32 s27, s27, 1\n"
"v_mov_b32 v255, s27\n"
"v_mfma_f32_16x16x32_bf16 v[48:51], v[212:215], a[196:199], v[48:51]\n"
"v_exp_f32 v201, v201\n"
"v_mfma_f32_16x16x32_bf16 v[52:55], v[212:215], a[200:203], v[52:55]\n"
"v_mfma_f32_16x16x32_bf16 v[52:55], v[208:211], a[204:207], v[52:55]\n"
"v_exp_f32 v202, v202\n"
"v_mfma_f32_16x16x32_bf16 v[56:59], v[208:211], a[208:211], v[56:59]\n"
"buffer_load_dwordx4 v251, s[40:43], s24 offen offset:3072 nt lds\n"
"v_mfma_f32_16x16x32_bf16 v[56:59], v[212:215], a[212:215], v[56:59]\n"
"v_exp_f32 v203, v203\n"
"v_mfma_f32_16x16x32_bf16 v[60:63], v[212:215], a[216:219], v[60:63]\n"
"s_waitcnt vmcnt(4)\n"
"ds_write_b32 v253, v255\n"
"v_mfma_f32_16x16x32_bf16 v[60:63], v[208:211], a[220:223], v[60:63]\n"
"v_mfma_f32_16x16x32_bf16 v[192:195], a[96:99], a[48:51], v[232:235]\n"
"v_exp_f32 v204, v204\n"
"v_mfma_f32_16x16x32_bf16 v[192:195], a[100:103], a[52:55], v[192:195]\n"
"v_cvt_pk_bf16_f32 v212, v200, v201\n"
"v_cvt_pk_bf16_f32 v213, v202, v203\n"
"v_mfma_f32_16x16x32_bf16 v[192:195], a[104:107], a[56:59], v[192:195]\n"
"v_exp_f32 v205, v205\n"
"v_mfma_f32_16x16x32_bf16 v[192:195], a[108:111], a[60:63], v[192:195]\n"
"v_cvt_pk_bf16_f32 v214, v204, v205\n"
"v_mfma_f32_16x16x32_bf16 v[196:199], a[112:115], a[60:63], v[232:235]\n"
"v_exp_f32 v206, v206\n"
"v_mfma_f32_16x16x32_bf16 v[196:199], a[116:119], a[56:59], v[196:199]\n"
"v_mfma_f32_16x16x32_bf16 v[196:199], a[120:123], a[52:55], v[196:199]\n"
"v_exp_f32 v207, v207\n"
"v_mfma_f32_16x16x32_bf16 v[196:199], a[124:127], a[48:51], v[196:199]\n"
"v_cvt_pk_bf16_f32 v215, v206, v207\n"
"v_mfma_f32_16x16x32_bf16 v[200:203], a[128:131], a[48:51], v[232:235]\n"
"v_exp_f32 v192, v192\n"
"v_mfma_f32_16x16x32_bf16 v[200:203], a[132:135], a[52:55], v[200:203]\n"
"s_add_u32 m0, s25, 49152\n"
"s_add_u32 s24, s24, 8192\n"
"v_mfma_f32_16x16x32_bf16 v[200:203], a[136:139], a[56:59], v[200:203]\n"
"v_exp_f32 v193, v193\n"
"v_mfma_f32_16x16x32_bf16 v[200:203], a[140:143], a[60:63], v[200:203]\n"
"v_cvt_pk_bf16_f32 v208, v192, v193\n"
"v_xor_b32 v244, 0x10000, v244\n"
"v_mfma_f32_16x16x32_bf16 v[204:207], a[144:147], a[60:63], v[232:235]\n"
"v_exp_f32 v194, v194\n"
"v_mfma_f32_16x16x32_bf16 v[204:207], a[148:151], a[56:59], v[204:207]\n"
"buffer_load_dwordx4 v248, s[36:39], s24 offen nt lds\n"
"v_mfma_f32_16x16x32_bf16 v[204:207], a[152:155], a[52:55], v[204:207]\n"
"v_exp_f32 v195, v195\n"
"v_mfma_f32_16x16x32_bf16 v[204:207], a[156:159], a[48:51], v[204:207]\n"
"v_cvt_pk_bf16_f32 v209, v194, v195\n"
"v_xor_b32 v245, 0x10000, v245\n"
"v_smfmac_f32_16x16x64_bf16 a[232:235], a[248:251], v[212:219], v252\n"
"v_exp_f32 v196, v196\n"
"v_mfma_f32_16x16x32_bf16 v[64:67], v[216:219], a[160:163], v[64:67]\n"
"v_mfma_f32_16x16x32_bf16 v[64:67], v[212:215], a[164:167], v[64:67]\n"
"v_exp_f32 v197, v197\n"
"v_mfma_f32_16x16x32_bf16 v[68:71], v[212:215], a[168:171], v[68:71]\n"
"v_cvt_pk_bf16_f32 v210, v196, v197\n"
"v_mfma_f32_16x16x32_bf16 v[68:71], v[216:219], a[172:175], v[68:71]\n"
"v_exp_f32 v198, v198\n"
"v_mfma_f32_16x16x32_bf16 v[72:75], v[216:219], a[176:179], v[72:75]\n"
"ds_read_b32 v255, v254\n"
"v_mfma_f32_16x16x32_bf16 v[72:75], v[212:215], a[180:183], v[72:75]\n"
"v_exp_f32 v199, v199\n"
"v_mfma_f32_16x16x32_bf16 v[76:79], v[212:215], a[184:187], v[76:79]\n"
"v_cvt_pk_bf16_f32 v211, v198, v199\n"
"v_mfma_f32_16x16x32_bf16 v[76:79], v[216:219], a[188:191], v[76:79]\n"
"v_exp_f32 v200, v200\n"
"v_mfma_f32_16x16x32_bf16 v[80:83], v[216:219], a[192:195], v[80:83]\n"
"v_mfma_f32_16x16x32_bf16 v[80:83], v[212:215], a[196:199], v[80:83]\n"
"v_exp_f32 v201, v201\n"
"v_mfma_f32_16x16x32_bf16 v[84:87], v[212:215], a[200:203], v[84:87]\n"
"v_mfma_f32_16x16x32_bf16 v[84:87], v[216:219], a[204:207], v[84:87]\n"
"v_exp_f32 v202, v202\n"
"v_mfma_f32_16x16x32_bf16 v[88:91], v[216:219], a[208:211], v[88:91]\n"
"v_mfma_f32_16x16x32_bf16 v[88:91], v[212:215], a[212:215], v[88:91]\n"
"v_exp_f32 v203, v203\n"
"v_mfma_f32_16x16x32_bf16 v[92:95], v[212:215], a[216:219], v[92:95]\n"
"s_waitcnt lgkmcnt(0)\n"
"v_cmp_gt_u32 vcc, s27, v255\n"
"v_mfma_f32_16x16x32_bf16 v[92:95], v[216:219], a[220:223], v[92:95]\n"
"s_cbranch_vccnz L_late5_2_%=\n"
"L_back5_2_%=:\n"
"v_mfma_f32_16x16x32_bf16 v[192:195], a[96:99], a[64:67], v[236:239]\n"
"v_exp_f32 v204, v204\n"
"v_mfma_f32_16x16x32_bf16 v[192:195], a[100:103], a[68:71], v[192:195]\n"
"v_cvt_pk_bf16_f32 v212, v200, v201\n"
"v_cvt_pk_bf16_f32 v213, v202, v203\n"
"v_mfma_f32_16x16x32_bf16 v[192:195], a[104:107], a[72:75], v[192:195]\n"
"v_exp_f32 v205, v205\n"
"v_mfma_f32_16x16x32_bf16 v[192:195], a[108:111], a[76:79], v[192:195]\n"
"ds_read_b128 a[96:99], v244 offset:0\n"
"v_cvt_pk_bf16_f32 v214, v204, v205\n"
"v_mfma_f32_16x16x32_bf16 v[196:199], a[112:115], a[76:79], v[236:239]\n"
"v_exp_f32 v206, v206\n"
"v_mfma_f32_16x16x32_bf16 v[196:199], a[116:119], a[72:75], v[196:199]\n"
"ds_read_b128 a[100:103], v245 offset:0\n"
"ds_read_b128 a[104:107], v244 offset:1024\n"
"v_mfma_f32_16x16x32_bf16 v[196:199], a[120:123], a[68:71], v[196:199]\n"
"v_exp_f32 v207, v207\n"
"v_mfma_f32_16x16x32_bf16 v[196:199], a[124:127], a[64:67], v[196:199]\n"
"ds_read_b128 a[108:111], v245 offset:1024\n"
"v_cvt_pk_bf16_f32 v215, v206, v207\n"
"v_mfma_f32_16x16x32_bf16 v[200:203], a[128:131], a[64:67], v[236:239]\n"
"v_exp_f32 v192, v192\n"
"v_mfma_f32_16x16x32_bf16 v[200:203], a[132:135], a[68:71], v[200:203]\n"
"ds_read_b128 a[112:115], v245 offset:9216\n"
"ds_read_b128 a[116:119], v244 offset:9216\n"
"v_mfma_f32_16x16x32_bf16 v[200:203], a[136:139], a[72:75], v[200:203]\n"
"v_exp_f32 v193, v193\n"
"v_mfma_f32_16x16x32_bf16 v[200:203], a[140:143], a[76:79], v[200:203]\n"
"ds_read_b128 a[120:123], v245 offset:8192\n"
"v_cvt_pk_bf16_f32 v216, v192, v193\n"
"v_mfma_f32_16x16x32_bf16 v[204:207], a[144:147], a[76:79], v[236:239]\n"
"v_exp_f32 v194, v194\n"
"v_mfma_f32_16x16x32_bf16 v[204:207], a[148:151], a[72:75], v[204:207]\n"
"ds_read_b128 a[124:127], v244 offset:8192\n"
"v_mfma_f32_16x16x32_bf16 v[204:207], a[152:155], a[68:71], v[204:207]\n"
"v_exp_f32 v195, v195\n"
"v_mfma_f32_16x16x32_bf16 v[204:207], a[156:159], a[64:67], v[204:207]\n"
"ds_read_b128 a[128:131], v244 offset:16384\n"
"v_cvt_pk_bf16_f32 v217, v194, v195\n"
"v_smfmac_f32_16x16x64_bf16 a[236:239], a[248:251], v[208:215], v252\n"
"v_exp_f32 v196, v196\n"
"v_mfma_f32_16x16x32_bf16 v[96:99], v[208:211], a[160:163], v[96:99]\n"
"ds_read_b128 a[132:135], v245 offset:16384\n"
"v_mfma_f32_16x16x32_bf16 v[96:99], v[212:215], a[164:167], v[96:99]\n"
"v_exp_f32 v197, v197\n"
"v_mfma_f32_16x16x32_bf16 v[100:103], v[212:215], a[168:171], v[100:103]\n"
"ds_read_b128 a[136:139], v244 offset:17408\n"
"v_cvt_pk_bf16_f32 v218, v196, v197\n"
"v_mfma_f32_16x16x32_bf16 v[100:103], v[208:211], a[172:175], v[100:103]\n"
"v_exp_f32 v198, v198\n"
"v_mfma_f32_16x16x32_bf16 v[104:107], v[208:211], a[176:179], v[104:107]\n"
"ds_read_b128 a[140:143], v245 offset:17408\n"
"ds_read_b128 a[144:147], v245 offset:25600\n"
"v_mfma_f32_16x16x32_bf16 v[104:107], v[212:215], a[180:183], v[104:107]\n"
"v_exp_f32 v199, v199\n"
"v_mfma_f32_16x16x32_bf16 v[108:111], v[212:215], a[184:187], v[108:111]\n"
"ds_read_b128 a[148:151], v244 offset:25600\n"
"v_cvt_pk_bf16_f32 v219, v198, v199\n"
"v_mfma_f32_16x16x32_bf16 v[108:111], v[208:211], a[188:191], v[108:111]\n"
"v_exp_f32 v200, v200\n"
"v_mfma_f32_16x16x32_bf16 v[112:115], v[208:211], a[192:195], v[112:115]\n"
"ds_read_b128 a[152:155], v245 offset:24576\n"
"v_mfma_f32_16x16x32_bf16 v[112:115], v[212:215], a[196:199], v[112:115]\n"
"v_exp_f32 v201, v201\n"
"v_mfma_f32_16x16x32_bf16 v[116:119], v[212:215], a[200:203], v[116:119]\n"
"buffer_load_dwordx4 v249, s[36:39], s24 offen offset:1024 nt lds\n"
"v_mfma_f32_16x16x32_bf16 v[116:119], v[208:211], a[204:207], v[116:119]\n"
"v_exp_f32 v202, v202\n"
"v_mfma_f32_16x16x32_bf16 v[120:123], v[208:211], a[208:211], v[120:123]\n"
"ds_read_b128 a[156:159], v244 offset:24576\n"
"v_mfma_f32_16x16x32_bf16 v[120:123], v[212:215], a[212:215], v[120:123]\n"
"v_exp_f32 v203, v203\n"
"v_mfma_f32_16x16x32_bf16 v[124:127], v[212:215], a[216:219], v[124:127]\n"
"buffer_load_dwordx4 v250, s[40:43], s24 offen offset:2048 nt lds\n"
"v_mfma_f32_16x16x32_bf16 v[124:127], v[208:211], a[220:223], v[124:127]\n"
"s_waitcnt lgkmcnt(8)\n"
"v_mfma_f32_16x16x32_bf16 v[192:195], a[96:99], a[0:3], v[220:223]\n"
"v_exp_f32 v204, v204\n"
"v_mfma_f32_16x16x32_bf16 v[192:195], a[100:103], a[4:7], v[192:195]\n"
"v_cvt_pk_bf16_f32 v212, v200, v201\n"
"v_cvt_pk_bf16_f32 v213, v202, v203\n"
"v_mfma_f32_16x16x32_bf16 v[192:195], a[104:107], a[8:11], v[192:195]\n"
"v_exp_f32 v205, v205\n"
"v_mfma_f32_16x16x32_bf16 v[192:195], a[108:111], a[12:15], v[192:195]\n"
"v_cvt_pk_bf16_f32 v214, v204, v205\n"
"v_mfma_f32_16x16x32_bf16 v[196:199], a[112:115], a[12:15], v[220:223]\n"
"v_exp_f32 v206, v206\n"
"v_mfma_f32_16x16x32_bf16 v[196:199], a[116:119], a[8:11], v[196:199]\n"
"buffer_load_dwordx4 v251, s[40:43], s24 offen offset:3072 nt lds\n"
"v_mfma_f32_16x16x32_bf16 v[196:199], a[120:123], a[4:7], v[196:199]\n"
"v_exp_f32 v207, v207\n"
"v_mfma_f32_16x16x32_bf16 v[196:199], a[124:127], a[0:3], v[196:199]\n"
"v_cvt_pk_bf16_f32 v215, v206, v207\n"
"s_waitcnt lgkmcnt(0)\n"
"v_mfma_f32_16x16x32_bf16 v[200:203], a[128:131], a[0:3], v[220:223]\n"
"v_exp_f32 v192, v192\n"
"v_mfma_f32_16x16x32_bf16 v[200:203], a[132:135], a[4:7], v[200:203]\n"
"v_xor_b32 v246, 0x10000, v246\n"
"v_xor_b32 v247, 0x10000, v247\n"
"v_mfma_f32_16x16x32_bf16 v[200:203], a[136:139], a[8:11], v[200:203]\n"
"v_exp_f32 v193, v193\n"
"v_mfma_f32_16x16x32_bf16 v[200:203], a[140:143], a[12:15], v[200:203]\n"
"v_cvt_pk_bf16_f32 v208, v192, v193\n"
"v_mfma_f32_16x16x32_bf16 v[204:207], a[144:147], a[12:15], v[220:223]\n"
"v_exp_f32 v194, v194\n"
"v_mfma_f32_16x16x32_bf16 v[204:207], a[148:151], a[8:11], v[204:207]\n"
"v_mfma_f32_16x16x32_bf16 v[204:207], a[152:155], a[4:7], v[204:207]\n"
"v_exp_f32 v195, v195\n"
"v_mfma_f32_16x16x32_bf16 v[204:207], a[156:159], a[0:3], v[204:207]\n"
"v_cvt_pk_bf16_f32 v209, v194, v195\n"
"v_smfmac_f32_16x16x64_bf16 a[240:243], a[248:251], v[212:219], v252\n"
"v_exp_f32 v196, v196\n"
"v_mfma_f32_16x16x32_bf16 v[128:131], v[216:219], a[160:163], v[128:131]\n"
"ds_read_b64_tr_b16 a[160:161], v246 offset:2048\n"
"ds_read_b64_tr_b16 a[162:163], v246 offset:10240\n"
"v_mfma_f32_16x16x32_bf16 v[128:131], v[212:215], a[164:167], v[128:131]\n"
"v_exp_f32 v197, v197\n"
"v_mfma_f32_16x16x32_bf16 v[132:135], v[212:215], a[168:171], v[132:135]\n"
"ds_read_b64_tr_b16 a[164:165], v246 offset:18432\n"
"v_cvt_pk_bf16_f32 v210, v196, v197\n"
"v_mfma_f32_16x16x32_bf16 v[132:135], v[216:219], a[172:175], v[132:135]\n"
"v_exp_f32 v198, v198\n"
"v_mfma_f32_16x16x32_bf16 v[136:139], v[216:219], a[176:179], v[136:139]\n"
"ds_read_b64_tr_b16 a[166:167], v246 offset:26624\n"
"ds_read_b64_tr_b16 a[168:169], v247 offset:18432\n"
"v_mfma_f32_16x16x32_bf16 v[136:139], v[212:215], a[180:183], v[136:139]\n"
"v_exp_f32 v199, v199\n"
"v_mfma_f32_16x16x32_bf16 v[140:143], v[212:215], a[184:187], v[140:143]\n"
"ds_read_b64_tr_b16 a[170:171], v247 offset:26624\n"
"v_cvt_pk_bf16_f32 v211, v198, v199\n"
"v_mfma_f32_16x16x32_bf16 v[140:143], v[216:219], a[188:191], v[140:143]\n"
"v_exp_f32 v200, v200\n"
"v_mfma_f32_16x16x32_bf16 v[144:147], v[216:219], a[192:195], v[144:147]\n"
"ds_read_b64_tr_b16 a[172:173], v247 offset:2048\n"
"ds_read_b64_tr_b16 a[174:175], v247 offset:10240\n"
"v_mfma_f32_16x16x32_bf16 v[144:147], v[212:215], a[196:199], v[144:147]\n"
"v_exp_f32 v201, v201\n"
"v_mfma_f32_16x16x32_bf16 v[148:151], v[212:215], a[200:203], v[148:151]\n"
"ds_read_b64_tr_b16 a[176:177], v246 offset:2560\n"
"ds_read_b64_tr_b16 a[178:179], v246 offset:10752\n"
"v_mfma_f32_16x16x32_bf16 v[148:151], v[216:219], a[204:207], v[148:151]\n"
"v_exp_f32 v202, v202\n"
"v_mfma_f32_16x16x32_bf16 v[152:155], v[216:219], a[208:211], v[152:155]\n"
"ds_read_b64_tr_b16 a[180:181], v246 offset:18944\n"
"ds_read_b64_tr_b16 a[182:183], v246 offset:27136\n"
"v_mfma_f32_16x16x32_bf16 v[152:155], v[212:215], a[212:215], v[152:155]\n"
"v_exp_f32 v203, v203\n"
"v_mfma_f32_16x16x32_bf16 v[156:159], v[212:215], a[216:219], v[156:159]\n"
"ds_read_b64_tr_b16 a[184:185], v247 offset:18944\n"
"ds_read_b64_tr_b16 a[186:187], v247 offset:27136\n"
"v_mfma_f32_16x16x32_bf16 v[156:159], v[216:219], a[220:223], v[156:159]\n"
"ds_read_b64_tr_b16 a[188:189], v247 offset:2560\n"
"ds_read_b64_tr_b16 a[190:191], v247 offset:10752\n"
"v_mfma_f32_16x16x32_bf16 v[192:195], a[96:99], a[16:19], v[224:227]\n"
"v_exp_f32 v204, v204\n"
"v_mfma_f32_16x16x32_bf16 v[192:195], a[100:103], a[20:23], v[192:195]\n"
"v_cvt_pk_bf16_f32 v212, v200, v201\n"
"v_cvt_pk_bf16_f32 v213, v202, v203\n"
"v_mfma_f32_16x16x32_bf16 v[192:195], a[104:107], a[24:27], v[192:195]\n"
"v_exp_f32 v205, v205\n"
"v_mfma_f32_16x16x32_bf16 v[192:195], a[108:111], a[28:31], v[192:195]\n"
"ds_read_b64_tr_b16 a[192:193], v246 offset:3072\n"
"v_cvt_pk_bf16_f32 v214, v204, v205\n"
"v_mfma_f32_16x16x32_bf16 v[196:199], a[112:115], a[28:31], v[224:227]\n"
"v_exp_f32 v206, v206\n"
"v_mfma_f32_16x16x32_bf16 v[196:199], a[116:119], a[24:27], v[196:199]\n"
"ds_read_b64_tr_b16 a[194:195], v246 offset:11264\n"
"ds_read_b64_tr_b16 a[196:197], v246 offset:19456\n"
"v_mfma_f32_16x16x32_bf16 v[196:199], a[120:123], a[20:23], v[196:199]\n"
"v_exp_f32 v207, v207\n"
"v_mfma_f32_16x16x32_bf16 v[196:199], a[124:127], a[16:19], v[196:199]\n"
"ds_read_b64_tr_b16 a[198:199], v246 offset:27648\n"
"v_cvt_pk_bf16_f32 v215, v206, v207\n"
"v_mfma_f32_16x16x32_bf16 v[200:203], a[128:131], a[16:19], v[224:227]\n"
"v_exp_f32 v192, v192\n"
"v_mfma_f32_16x16x32_bf16 v[200:203], a[132:135], a[20:23], v[200:203]\n"
"ds_read_b64_tr_b16 a[200:201], v247 offset:19456\n"
"ds_read_b64_tr_b16 a[202:203], v247 offset:27648\n"
"v_mfma_f32_16x16x32_bf16 v[200:203], a[136:139], a[24:27], v[200:203]\n"
"v_exp_f32 v193, v193\n"
"v_mfma_f32_16x16x32_bf16 v[200:203], a[140:143], a[28:31], v[200:203]\n"
"ds_read_b64_tr_b16 a[204:205], v247 offset:3072\n"
"v_cvt_pk_bf16_f32 v216, v192, v193\n"
"v_mfma_f32_16x16x32_bf16 v[204:207], a[144:147], a[28:31], v[224:227]\n"
"v_exp_f32 v194, v194\n"
"v_mfma_f32_16x16x32_bf16 v[204:207], a[148:151], a[24:27], v[204:207]\n"
"ds_read_b64_tr_b16 a[206:207], v247 offset:11264\n"
"ds_read_b64_tr_b16 a[208:209], v246 offset:3584\n"
"v_mfma_f32_16x16x32_bf16 v[204:207], a[152:155], a[20:23], v[204:207]\n"
"v_exp_f32 v195, v195\n"
"v_mfma_f32_16x16x32_bf16 v[204:207], a[156:159], a[16:19], v[204:207]\n"
"v_cvt_pk_bf16_f32 v217, v194, v195\n"
"s_waitcnt lgkmcnt(15)\n"
"v_smfmac_f32_16x16x64_bf16 a[224:227], a[248:251], v[208:215], v252\n"
"v_exp_f32 v196, v196\n"
"v_mfma_f32_16x16x32_bf16 v[0:3], v[208:211], a[160:163], v[0:3]\n"
"ds_read_b64_tr_b16 a[210:211], v246 offset:11776\n"
"ds_read_b64_tr_b16 a[212:213], v246 offset:19968\n"
"ds_read_b64_tr_b16 a[214:215], v246 offset:28160\n"
"v_mfma_f32_16x16x32_bf16 v[0:3], v[212:215], a[164:167], v[0:3]\n"
"ds_read_b64_tr_b16 a[216:217], v247 offset:19968\n"
"v_exp_f32 v197, v197\n"
"v_mfma_f32_16x16x32_bf16 v[4:7], v[212:215], a[168:171], v[4:7]\n"
"v_cvt_pk_bf16_f32 v218, v196, v197\n"
"s_waitcnt lgkmcnt(13)\n"
"ds_read_b64_tr_b16 a[218:219], v247 offset:28160\n"
"v_mfma_f32_16x16x32_bf16 v[4:7], v[208:211], a[172:175], v[4:7]\n"
"ds_read_b64_tr_b16 a[220:221], v247 offset:3584\n"
"v_exp_f32 v198, v198\n"
"v_mfma_f32_16x16x32_bf16 v[8:11], v[208:211], a[176:179], v[8:11]\n"
"ds_read_b64_tr_b16 a[222:223], v247 offset:11776\n"
"s_xor_b32 s25, s25, 0x10000\n"
"s_mov_b32 m0, s25\n"
"v_mfma_f32_16x16x32_bf16 v[8:11], v[212:215], a[180:183], v[8:11]\n"
"v_exp_f32 v199, v199\n"
"v_mfma_f32_16x16x32_bf16 v[12:15], v[212:215], a[184:187], v[12:15]\n"
"v_cvt_pk_bf16_f32 v219, v198, v199\n"
"s_add_u32 s24, s24, 8192\n"
"s_waitcnt lgkmcnt(8)\n"
"v_mfma_f32_16x16x32_bf16 v[12:15], v[208:211], a[188:191], v[12:15]\n"
"v_exp_f32 v200, v200\n"
"v_mfma_f32_16x16x32_bf16 v[16:19], v[208:211], a[192:195], v[16:19]\n"
"buffer_load_dwordx4 v248, s[36:39], s24 offen nt lds\n"
"v_mfma_f32_16x16x32_bf16 v[16:19], v[212:215], a[196:199], v[16:19]\n"
"v_exp_f32 v201, v201\n"
"v_mfma_f32_16x16x32_bf16 v[20:23], v[212:215], a[200:203], v[20:23]\n"
"s_waitcnt lgkmcnt(0)\n"
"v_mfma_f32_16x16x32_bf16 v[20:23], v[208:211], a[204:207], v[20:23]\n"
"v_exp_f32 v202, v202\n"
"v_mfma_f32_16x16x32_bf16 v[24:27], v[208:211], a[208:211], v[24:27]\n"
"buffer_load_dwordx4 v249, s[36:39], s24 offen offset:1024 nt lds\n"
"v_mfma_f32_16x16x32_bf16 v[24:27], v[212:215], a[212:215], v[24:27]\n"
"v_exp_f32 v203, v203\n"
"v_mfma_f32_16x16x32_bf16 v[28:31], v[212:215], a[216:219], v[28:31]\n"
"v_mfma_f32_16x16x32_bf16 v[28:31], v[208:211], a[220:223], v[28:31]\n"
"v_mfma_f32_16x16x32_bf16 v[192:195], a[96:99], a[32:35], v[228:231]\n"
"v_exp_f32 v204, v204\n"
"v_mfma_f32_16x16x32_bf16 v[192:195], a[100:103], a[36:39], v[192:195]\n"
"v_cvt_pk_bf16_f32 v212, v200, v201\n"
"v_cvt_pk_bf16_f32 v213, v202, v203\n"
"v_mfma_f32_16x16x32_bf16 v[192:195], a[104:107], a[40:43], v[192:195]\n"
"v_exp_f32 v205, v205\n"
"v_mfma_f32_16x16x32_bf16 v[192:195], a[108:111], a[44:47], v[192:195]\n"
"v_cvt_pk_bf16_f32 v214, v204, v205\n"
"v_mfma_f32_16x16x32_bf16 v[196:199], a[112:115], a[44:47], v[228:231]\n"
"v_exp_f32 v206, v206\n"
"v_mfma_f32_16x16x32_bf16 v[196:199], a[116:119], a[40:43], v[196:199]\n"
"buffer_load_dwordx4 v250, s[40:43], s24 offen offset:2048 nt lds\n"
"v_mfma_f32_16x16x32_bf16 v[196:199], a[120:123], a[36:39], v[196:199]\n"
"v_exp_f32 v207, v207\n"
"v_mfma_f32_16x16x32_bf16 v[196:199], a[124:127], a[32:35], v[196:199]\n"
"v_cvt_pk_bf16_f32 v215, v206, v207\n"
"v_mfma_f32_16x16x32_bf16 v[200:203], a[128:131], a[32:35], v[228:231]\n"
"v_exp_f32 v192, v192\n"
"v_mfma_f32_16x16x32_bf16 v[200:203], a[132:135], a[36:39], v[200:203]\n"
"v_mfma_f32_16x16x32_bf16 v[200:203], a[136:139], a[40:43], v[200:203]\n"
"v_exp_f32 v193, v193\n"
"v_mfma_f32_16x16x32_bf16 v[200:203], a[140:143], a[44:47], v[200:203]\n"
"v_cvt_pk_bf16_f32 v208, v192, v193\n"
"v_mfma_f32_16x16x32_bf16 v[204:207], a[144:147], a[44:47], v[228:231]\n"
"v_exp_f32 v194, v194\n"
"v_mfma_f32_16x16x32_bf16 v[204:207], a[148:151], a[40:43], v[204:207]\n"
"buffer_load_dwordx4 v251, s[40:43], s24 offen offset:3072 nt lds\n"
"v_mfma_f32_16x16x32_bf16 v[204:207], a[152:155], a[36:39], v[204:207]\n"
"v_exp_f32 v195, v195\n"
"v_mfma_f32_16x16x32_bf16 v[204:207], a[156:159], a[32:35], v[204:207]\n"
"v_cvt_pk_bf16_f32 v209, v194, v195\n"
"v_smfmac_f32_16x16x64_bf16 a[228:231], a[248:251], v[212:219], v252\n"
"v_exp_f32 v196, v196\n"
"v_mfma_f32_16x16x32_bf16 v[32:35], v[216:219], a[160:163], v[32:35]\n"
"v_mfma_f32_16x16x32_bf16 v[32:35], v[212:215], a[164:167], v[32:35]\n"
"v_exp_f32 v197, v197\n"
"v_mfma_f32_16x16x32_bf16 v[36:39], v[212:215], a[168:171], v[36:39]\n"
"v_cvt_pk_bf16_f32 v210, v196, v197\n"
"v_mfma_f32_16x16x32_bf16 v[36:39], v[216:219], a[172:175], v[36:39]\n"
"v_exp_f32 v198, v198\n"
"v_mfma_f32_16x16x32_bf16 v[40:43], v[216:219], a[176:179], v[40:43]\n"
"v_mfma_f32_16x16x32_bf16 v[40:43], v[212:215], a[180:183], v[40:43]\n"
"v_exp_f32 v199, v199\n"
"v_mfma_f32_16x16x32_bf16 v[44:47], v[212:215], a[184:187], v[44:47]\n"
"v_cvt_pk_bf16_f32 v211, v198, v199\n"
"v_mfma_f32_16x16x32_bf16 v[44:47], v[216:219], a[188:191], v[44:47]\n"
"v_exp_f32 v200, v200\n"
"v_mfma_f32_16x16x32_bf16 v[48:51], v[216:219], a[192:195], v[48:51]\n"
"s_add_u32 s27, s27, 1\n"
"v_mov_b32 v255, s27\n"
"v_mfma_f32_16x16x32_bf16 v[48:51], v[212:215], a[196:199], v[48:51]\n"
"v_exp_f32 v201, v201\n"
"v_mfma_f32_16x16x32_bf16 v[52:55], v[212:215], a[200:203], v[52:55]\n"
"s_add_u32 m0, s25, 16384\n"
"s_add_u32 s24, s24, 8192\n"
"v_mfma_f32_16x16x32_bf16 v[52:55], v[216:219], a[204:207], v[52:55]\n"
"v_exp_f32 v202, v202\n"
"v_mfma_f32_16x16x32_bf16 v[56:59], v[216:219], a[208:211], v[56:59]\n"
"buffer_load_dwordx4 v248, s[36:39], s24 offen nt lds\n"
"v_mfma_f32_16x16x32_bf16 v[56:59], v[212:215], a[212:215], v[56:59]\n"
"v_exp_f32 v203, v203\n"
"v_mfma_f32_16x16x32_bf16 v[60:63], v[212:215], a[216:219], v[60:63]\n"
"s_waitcnt vmcnt(5)\n"
"ds_write_b32 v253, v255\n"
"v_mfma_f32_16x16x32_bf16 v[60:63], v[216:219], a[220:223], v[60:63]\n"
"v_mfma_f32_16x16x32_bf16 v[192:195], a[96:99], a[48:51], v[232:235]\n"
"v_exp_f32 v204, v204\n"
"v_mfma_f32_16x16x32_bf16 v[192:195], a[100:103], a[52:55], v[192:195]\n"
"v_cvt_pk_bf16_f32 v212, v200, v201\n"
"v_cvt_pk_bf16_f32 v213, v202, v203\n"
"v_mfma_f32_16x16x32_bf16 v[192:195], a[104:107], a[56:59], v[192:195]\n"
"v_exp_f32 v205, v205\n"
"v_mfma_f32_16x16x32_bf16 v[192:195], a[108:111], a[60:63], v[192:195]\n"
"v_cvt_pk_bf16_f32 v214, v204, v205\n"
"v_mfma_f32_16x16x32_bf16 v[196:199], a[112:115], a[60:63], v[232:235]\n"
"v_exp_f32 v206, v206\n"
"v_mfma_f32_16x16x32_bf16 v[196:199], a[116:119], a[56:59], v[196:199]\n"
"v_mfma_f32_16x16x32_bf16 v[196:199], a[120:123], a[52:55], v[196:199]\n"
"v_exp_f32 v207, v207\n"
"v_mfma_f32_16x16x32_bf16 v[196:199], a[124:127], a[48:51], v[196:199]\n"
"v_cvt_pk_bf16_f32 v215, v206, v207\n"
"v_mfma_f32_16x16x32_bf16 v[200:203], a[128:131], a[48:51], v[232:235]\n"
"v_exp_f32 v192, v192\n"
"v_mfma_f32_16x16x32_bf16 v[200:203], a[132:135], a[52:55], v[200:203]\n"
"v_mfma_f32_16x16x32_bf16 v[200:203], a[136:139], a[56:59], v[200:203]\n"
"v_exp_f32 v193, v193\n"
"v_mfma_f32_16x16x32_bf16 v[200:203], a[140:143], a[60:63], v[200:203]\n"
"v_cvt_pk_bf16_f32 v216, v192, v193\n"
"v_mfma_f32_16x16x32_bf16 v[204:207], a[144:147], a[60:63], v[232:235]\n"
"v_exp_f32 v194, v194\n"
"v_mfma_f32_16x16x32_bf16 v[204:207], a[148:151], a[56:59], v[204:207]\n"
"v_mfma_f32_16x16x32_bf16 v[204:207], a[152:155], a[52:55], v[204:207]\n"
"v_exp_f32 v195, v195\n"
"v_mfma_f32_16x16x32_bf16 v[204:207], a[156:159], a[48:51], v[204:207]\n"
"v_cvt_pk_bf16_f32 v217, v194, v195\n"
"v_smfmac_f32_16x16x64_bf16 a[232:235], a[248:251], v[208:215], v252\n"
"v_exp_f32 v196, v196\n"
"v_mfma_f32_16x16x32_bf16 v[64:67], v[208:211], a[160:163], v[64:67]\n"
"buffer_load_dwordx4 v249, s[36:39], s24 offen offset:1024 nt lds\n"
"v_mfma_f32_16x16x32_bf16 v[64:67], v[212:215], a[164:167], v[64:67]\n"
"v_exp_f32 v197, v197\n"
"v_mfma_f32_16x16x32_bf16 v[68:71], v[212:215], a[168:171], v[68:71]\n"
"v_cvt_pk_bf16_f32 v218, v196, v197\n"
"v_mfma_f32_16x16x32_bf16 v[68:71], v[208:211], a[172:175], v[68:71]\n"
"v_exp_f32 v198, v198\n"
"v_mfma_f32_16x16x32_bf16 v[72:75], v[208:211], a[176:179], v[72:75]\n"
"ds_read_b32 v255, v254\n"
"v_mfma_f32_16x16x32_bf16 v[72:75], v[212:215], a[180:183], v[72:75]\n"
"v_exp_f32 v199, v199\n"
"v_mfma_f32_16x16x32_bf16 v[76:79], v[212:215], a[184:187], v[76:79]\n"
"v_cvt_pk_bf16_f32 v219, v198, v199\n"
"v_mfma_f32_16x16x32_bf16 v[76:79], v[208:211], a[188:191], v[76:79]\n"
"v_exp_f32 v200, v200\n"
"v_mfma_f32_16x16x32_bf16 v[80:83], v[208:211], a[192:195], v[80:83]\n"
"v_mfma_f32_16x16x32_bf16 v[80:83], v[212:215], a[196:199], v[80:83]\n"
"v_exp_f32 v201, v201\n"
"v_mfma_f32_16x16x32_bf16 v[84:87], v[212:215], a[200:203], v[84:87]\n"
"v_mfma_f32_16x16x32_bf16 v[84:87], v[208:211], a[204:207], v[84:87]\n"
"v_exp_f32 v202, v202\n"
"v_mfma_f32_16x16x32_bf16 v[88:91], v[208:211], a[208:211], v[88:91]\n"
"v_mfma_f32_16x16x32_bf16 v[88:91], v[212:215], a[212:215], v[88:91]\n"
"v_exp_f32 v203, v203\n"
"v_mfma_f32_16x16x32_bf16 v[92:95], v[212:215], a[216:219], v[92:95]\n"
"s_waitcnt lgkmcnt(0)\n"
"v_cmp_gt_u32 vcc, s27, v255\n"
"v_mfma_f32_16x16x32_bf16 v[92:95], v[208:211], a[220:223], v[92:95]\n"
"s_cbranch_vccnz L_late5_3_%=\n"
"L_back5_3_%=:\n"
"v_mfma_f32_16x16x32_bf16 v[192:195], a[96:99], a[64:67], v[236:239]\n"
"v_exp_f32 v204, v204\n"
"v_mfma_f32_16x16x32_bf16 v[192:195], a[100:103], a[68:71], v[192:195]\n"
"v_cvt_pk_bf16_f32 v212, v200, v201\n"
"v_cvt_pk_bf16_f32 v213, v202, v203\n"
"v_mfma_f32_16x16x32_bf16 v[192:195], a[104:107], a[72:75], v[192:195]\n"
"v_exp_f32 v205, v205\n"
"v_mfma_f32_16x16x32_bf16 v[192:195], a[108:111], a[76:79], v[192:195]\n"
"ds_read_b128 a[96:99], v244 offset:32768\n"
"v_cvt_pk_bf16_f32 v214, v204, v205\n"
"v_mfma_f32_16x16x32_bf16 v[196:199], a[112:115], a[76:79], v[236:239]\n"
"v_exp_f32 v206, v206\n"
"v_mfma_f32_16x16x32_bf16 v[196:199], a[116:119], a[72:75], v[196:199]\n"
"ds_read_b128 a[100:103], v245 offset:32768\n"
"ds_read_b128 a[104:107], v244 offset:33792\n"
"v_mfma_f32_16x16x32_bf16 v[196:199], a[120:123], a[68:71], v[196:199]\n"
"v_exp_f32 v207, v207\n"
"v_mfma_f32_16x16x32_bf16 v[196:199], a[124:127], a[64:67], v[196:199]\n"
"ds_read_b128 a[108:111], v245 offset:33792\n"
"v_cvt_pk_bf16_f32 v215, v206, v207\n"
"v_mfma_f32_16x16x32_bf16 v[200:203], a[128:131], a[64:67], v[236:239]\n"
"v_exp_f32 v192, v192\n"
"v_mfma_f32_16x16x32_bf16 v[200:203], a[132:135], a[68:71], v[200:203]\n"
"ds_read_b128 a[112:115], v245 offset:41984\n"
"ds_read_b128 a[116:119], v244 offset:41984\n"
"v_mfma_f32_16x16x32_bf16 v[200:203], a[136:139], a[72:75], v[200:203]\n"
"v_exp_f32 v193, v193\n"
"v_mfma_f32_16x16x32_bf16 v[200:203], a[140:143], a[76:79], v[200:203]\n"
"ds_read_b128 a[120:123], v245 offset:40960\n"
"v_cvt_pk_bf16_f32 v208, v192, v193\n"
"v_mfma_f32_16x16x32_bf16 v[204:207], a[144:147], a[76:79], v[236:239]\n"
"v_exp_f32 v194, v194\n"
"v_mfma_f32_16x16x32_bf16 v[204:207], a[148:151], a[72:75], v[204:207]\n"
"ds_read_b128 a[124:127], v244 offset:40960\n"
"v_mfma_f32_16x16x32_bf16 v[204:207], a[152:155], a[68:71], v[204:207]\n"
"v_exp_f32 v195, v195\n"
"v_mfma_f32_16x16x32_bf16 v[204:207], a[156:159], a[64:67], v[204:207]\n"
"ds_read_b128 a[128:131], v244 offset:49152\n"
"v_cvt_pk_bf16_f32 v209, v194, v195\n"
"v_smfmac_f32_16x16x64_bf16 a[236:239], a[248:251], v[212:219], v252\n"
"v_exp_f32 v196, v196\n"
"v_mfma_f32_16x16x32_bf16 v[96:99], v[216:219], a[160:163], v[96:99]\n"
"ds_read_b128 a[132:135], v245 offset:49152\n"
"v_mfma_f32_16x16x32_bf16 v[96:99], v[212:215], a[164:167], v[96:99]\n"
"v_exp_f32 v197, v197\n"
"v_mfma_f32_16x16x32_bf16 v[100:103], v[212:215], a[168:171], v[100:103]\n"
"ds_read_b128 a[136:139], v244 offset:50176\n"
"v_cvt_pk_bf16_f32 v210, v196, v197\n"
"v_mfma_f32_16x16x32_bf16 v[100:103], v[216:219], a[172:175], v[100:103]\n"
"v_exp_f32 v198, v198\n"
"v_mfma_f32_16x16x32_bf16 v[104:107], v[216:219], a[176:179], v[104:107]\n"
"ds_read_b128 a[140:143], v245 offset:50176\n"
"ds_read_b128 a[144:147], v245 offset:58368\n"
"v_mfma_f32_16x16x32_bf16 v[104:107], v[212:215], a[180:183], v[104:107]\n"
"v_exp_f32 v199, v199\n"
"v_mfma_f32_16x16x32_bf16 v[108:111], v[212:215], a[184:187], v[108:111]\n"
"ds_read_b128 a[148:151], v244 offset:58368\n"
"v_cvt_pk_bf16_f32 v211, v198, v199\n"
"v_mfma_f32_16x16x32_bf16 v[108:111], v[216:219], a[188:191], v[108:111]\n"
"v_exp_f32 v200, v200\n"
"v_mfma_f32_16x16x32_bf16 v[112:115], v[216:219], a[192:195], v[112:115]\n"
"ds_read_b128 a[152:155], v245 offset:57344\n"
"v_mfma_f32_16x16x32_bf16 v[112:115], v[212:215], a[196:199], v[112:115]\n"
"v_exp_f32 v201, v201\n"
"v_mfma_f32_16x16x32_bf16 v[116:119], v[212:215], a[200:203], v[116:119]\n"
"buffer_load_dwordx4 v250, s[40:43], s24 offen offset:2048 nt lds\n"
"v_mfma_f32_16x16x32_bf16 v[116:119], v[216:219], a[204:207], v[116:119]\n"
"v_exp_f32 v202, v202\n"
"v_mfma_f32_16x16x32_bf16 v[120:123], v[216:219], a[208:211], v[120:123]\n"
"ds_read_b128 a[156:159], v244 offset:57344\n"
"v_mfma_f32_16x16x32_bf16 v[120:123], v[212:215], a[212:215], v[120:123]\n"
"v_exp_f32 v203, v203\n"
"v_mfma_f32_16x16x32_bf16 v[124:127], v[212:215], a[216:219], v[124:127]\n"
"buffer_load_dwordx4 v251, s[40:43], s24 offen offset:3072 nt lds\n"
"v_mfma_f32_16x16x32_bf16 v[124:127], v[216:219], a[220:223], v[124:127]\n"
"s_waitcnt lgkmcnt(8)\n"
"v_mfma_f32_16x16x32_bf16 v[192:195], a[96:99], a[0:3], v[220:223]\n"
"v_exp_f32 v204, v204\n"
"v_mfma_f32_16x16x32_bf16 v[192:195], a[100:103], a[4:7], v[192:195]\n"
"v_cvt_pk_bf16_f32 v212, v200, v201\n"
"v_cvt_pk_bf16_f32 v213, v202, v203\n"
"v_mfma_f32_16x16x32_bf16 v[192:195], a[104:107], a[8:11], v[192:195]\n"
"v_exp_f32 v205, v205\n"
"v_mfma_f32_16x16x32_bf16 v[192:195], a[108:111], a[12:15], v[192:195]\n"
"v_cvt_pk_bf16_f32 v214, v204, v205\n"
"s_add_u32 m0, s25, 32768\n"
"s_add_u32 s24, s24, 8192\n"
"v_mfma_f32_16x16x32_bf16 v[196:199], a[112:115], a[12:15], v[220:223]\n"
"v_exp_f32 v206, v206\n"
"v_mfma_f32_16x16x32_bf16 v[196:199], a[116:119], a[8:11], v[196:199]\n"
"buffer_load_dwordx4 v248, s[36:39], s24 offen nt lds\n"
"v_mfma_f32_16x16x32_bf16 v[196:199], a[120:123], a[4:7], v[196:199]\n"
"v_exp_f32 v207, v207\n"
"v_mfma_f32_16x16x32_bf16 v[196:199], a[124:127], a[0:3], v[196:199]\n"
"v_cvt_pk_bf16_f32 v215, v206, v207\n"
"s_waitcnt lgkmcnt(0)\n"
"v_mfma_f32_16x16x32_bf16 v[200:203], a[128:131], a[0:3], v[220:223]\n"
"v_exp_f32 v192, v192\n"
"v_mfma_f32_16x16x32_bf16 v[200:203], a[132:135], a[4:7], v[200:203]\n"
"v_mfma_f32_16x16x32_bf16 v[200:203], a[136:139], a[8:11], v[200:203]\n"
"v_exp_f32 v193, v193\n"
"v_mfma_f32_16x16x32_bf16 v[200:203], a[140:143], a[12:15], v[200:203]\n"
"v_cvt_pk_bf16_f32 v216, v192, v193\n"
"v_mfma_f32_16x16x32_bf16 v[204:207], a[144:147], a[12:15], v[220:223]\n"
"v_exp_f32 v194, v194\n"
"v_mfma_f32_16x16x32_bf16 v[204:207], a[148:151], a[8:11], v[204:207]\n"
"v_mfma_f32_16x16x32_bf16 v[204:207], a[152:155], a[4:7], v[204:207]\n"
"v_exp_f32 v195, v195\n"
"v_mfma_f32_16x16x32_bf16 v[204:207], a[156:159], a[0:3], v[204:207]\n"
"v_cvt_pk_bf16_f32 v217, v194, v195\n"
"v_smfmac_f32_16x16x64_bf16 a[240:243], a[248:251], v[208:215], v252\n"
"v_exp_f32 v196, v196\n"
"v_mfma_f32_16x16x32_bf16 v[128:131], v[208:211], a[160:163], v[128:131]\n"
"ds_read_b64_tr_b16 a[160:161], v246 offset:34816\n"
"ds_read_b64_tr_b16 a[162:163], v246 offset:43008\n"
"v_mfma_f32_16x16x32_bf16 v[128:131], v[212:215], a[164:167], v[128:131]\n"
"v_exp_f32 v197, v197\n"
"v_mfma_f32_16x16x32_bf16 v[132:135], v[212:215], a[168:171], v[132:135]\n"
"ds_read_b64_tr_b16 a[164:165], v246 offset:51200\n"
"v_cvt_pk_bf16_f32 v218, v196, v197\n"
"v_mfma_f32_16x16x32_bf16 v[132:135], v[208:211], a[172:175], v[132:135]\n"
"v_exp_f32 v198, v198\n"
"v_mfma_f32_16x16x32_bf16 v[136:139], v[208:211], a[176:179], v[136:139]\n"
"ds_read_b64_tr_b16 a[166:167], v246 offset:59392\n"
"ds_read_b64_tr_b16 a[168:169], v247 offset:51200\n"
"v_mfma_f32_16x16x32_bf16 v[136:139], v[212:215], a[180:183], v[136:139]\n"
"v_exp_f32 v199, v199\n"
"v_mfma_f32_16x16x32_bf16 v[140:143], v[212:215], a[184:187], v[140:143]\n"
"ds_read_b64_tr_b16 a[170:171], v247 offset:59392\n"
"v_cvt_pk_bf16_f32 v219, v198, v199\n"
"v_mfma_f32_16x16x32_bf16 v[140:143], v[208:211], a[188:191], v[140:143]\n"
"v_exp_f32 v200, v200\n"
"v_mfma_f32_16x16x32_bf16 v[144:147], v[208:211], a[192:195], v[144:147]\n"
"ds_read_b64_tr_b16 a[172:173], v247 offset:34816\n"
"ds_read_b64_tr_b16 a[174:175], v247 offset:43008\n"
"v_mfma_f32_16x16x32_bf16 v[144:147], v[212:215], a[196:199], v[144:147]\n"
"v_exp_f32 v201, v201\n"
"v_mfma_f32_16x16x32_bf16 v[148:151], v[212:215], a[200:203], v[148:151]\n"
"ds_read_b64_tr_b16 a[176:177], v246 offset:35328\n"
"ds_read_b64_tr_b16 a[178:179], v246 offset:43520\n"
"v_mfma_f32_16x16x32_bf16 v[148:151], v[208:211], a[204:207], v[148:151]\n"
"v_exp_f32 v202, v202\n"
"v_mfma_f32_16x16x32_bf16 v[152:155], v[208:211], a[208:211], v[152:155]\n"
"ds_read_b64_tr_b16 a[180:181], v246 offset:51712\n"
"ds_read_b64_tr_b16 a[182:183], v246 offset:59904\n"
"v_mfma_f32_16x16x32_bf16 v[152:155], v[212:215], a[212:215], v[152:155]\n"
"v_exp_f32 v203, v203\n"
"v_mfma_f32_16x16x32_bf16 v[156:159], v[212:215], a[216:219], v[156:159]\n"
"ds_read_b64_tr_b16 a[184:185], v247 offset:51712\n"
"ds_read_b64_tr_b16 a[186:187], v247 offset:59904\n"
"v_mfma_f32_16x16x32_bf16 v[156:159], v[208:211], a[220:223], v[156:159]\n"
"ds_read_b64_tr_b16 a[188:189], v247 offset:35328\n"
"ds_read_b64_tr_b16 a[190:191], v247 offset:43520\n"
"v_mfma_f32_16x16x32_bf16 v[192:195], a[96:99], a[16:19], v[224:227]\n"
"v_exp_f32 v204, v204\n"
"v_mfma_f32_16x16x32_bf16 v[192:195], a[100:103], a[20:23], v[192:195]\n"
"v_cvt_pk_bf16_f32 v212, v200, v201\n"
"v_cvt_pk_bf16_f32 v213, v202, v203\n"
"v_mfma_f32_16x16x32_bf16 v[192:195], a[104:107], a[24:27], v[192:195]\n"
"v_exp_f32 v205, v205\n"
"v_mfma_f32_16x16x32_bf16 v[192:195], a[108:111], a[28:31], v[192:195]\n"
"ds_read_b64_tr_b16 a[192:193], v246 offset:35840\n"
"v_cvt_pk_bf16_f32 v214, v204, v205\n"
"v_mfma_f32_16x16x32_bf16 v[196:199], a[112:115], a[28:31], v[224:227]\n"
"v_exp_f32 v206, v206\n"
"v_mfma_f32_16x16x32_bf16 v[196:199], a[116:119], a[24:27], v[196:199]\n"
"ds_read_b64_tr_b16 a[194:195], v246 offset:44032\n"
"ds_read_b64_tr_b16 a[196:197], v246 offset:52224\n"
"v_mfma_f32_16x16x32_bf16 v[196:199], a[120:123], a[20:23], v[196:199]\n"
"v_exp_f32 v207, v207\n"
"v_mfma_f32_16x16x32_bf16 v[196:199], a[124:127], a[16:19], v[196:199]\n"
"ds_read_b64_tr_b16 a[198:199], v246 offset:60416\n"
"v_cvt_pk_bf16_f32 v215, v206, v207\n"
"v_mfma_f32_16x16x32_bf16 v[200:203], a[128:131], a[16:19], v[224:227]\n"
"v_exp_f32 v192, v192\n"
"v_mfma_f32_16x16x32_bf16 v[200:203], a[132:135], a[20:23], v[200:203]\n"
"ds_read_b64_tr_b16 a[200:201], v247 offset:52224\n"
"ds_read_b64_tr_b16 a[202:203], v247 offset:60416\n"
"v_mfma_f32_16x16x32_bf16 v[200:203], a[136:139], a[24:27], v[200:203]\n"
"v_exp_f32 v193, v193\n"
"v_mfma_f32_16x16x32_bf16 v[200:203], a[140:143], a[28:31], v[200:203]\n"
"ds_read_b64_tr_b16 a[204:205], v247 offset:35840\n"
"v_cvt_pk_bf16_f32 v208, v192, v193\n"
"v_mfma_f32_16x16x32_bf16 v[204:207], a[144:147], a[28:31], v[224:227]\n"
"v_exp_f32 v194, v194\n"
"v_mfma_f32_16x16x32_bf16 v[204:207], a[148:151], a[24:27], v[204:207]\n"
"ds_read_b64_tr_b16 a[206:207], v247 offset:44032\n"
"ds_read_b64_tr_b16 a[208:209], v246 offset:36352\n"
"v_mfma_f32_16x16x32_bf16 v[204:207], a[152:155], a[20:23], v[204:207]\n"
"v_exp_f32 v195, v195\n"
"v_mfma_f32_16x16x32_bf16 v[204:207], a[156:159], a[16:19], v[204:207]\n"
"v_cvt_pk_bf16_f32 v209, v194, v195\n"
"s_waitcnt lgkmcnt(15)\n"
"v_smfmac_f32_16x16x64_bf16 a[224:227], a[248:251], v[212:219], v252\n"
"v_exp_f32 v196, v196\n"
"v_mfma_f32_16x16x32_bf16 v[0:3], v[216:219], a[160:163], v[0:3]\n"
"ds_read_b64_tr_b16 a[210:211], v246 offset:44544\n"
"ds_read_b64_tr_b16 a[212:213], v246 offset:52736\n"
"v_mfma_f32_16x16x32_bf16 v[0:3], v[212:215], a[164:167], v[0:3]\n"
"v_exp_f32 v197, v197\n"
"v_mfma_f32_16x16x32_bf16 v[4:7], v[212:215], a[168:171], v[4:7]\n"
"v_cvt_pk_bf16_f32 v210, v196, v197\n"
"s_waitcnt lgkmcnt(11)\n"
"v_mfma_f32_16x16x32_bf16 v[4:7], v[216:219], a[172:175], v[4:7]\n"
"v_exp_f32 v198, v198\n"
"v_mfma_f32_16x16x32_bf16 v[8:11], v[216:219], a[176:179], v[8:11]\n"
"ds_read_b64_tr_b16 a[214:215], v246 offset:60928\n"
"ds_read_b64_tr_b16 a[216:217], v247 offset:52736\n"
"ds_read_b64_tr_b16 a[218:219], v247 offset:60928\n"
"ds_read_b64_tr_b16 a[220:221], v247 offset:36352\n"
"ds_read_b64_tr_b16 a[222:223], v247 offset:44544\n"
"v_mfma_f32_16x16x32_bf16 v[8:11], v[212:215], a[180:183], v[8:11]\n"
"v_exp_f32 v199, v199\n"
"v_mfma_f32_16x16x32_bf16 v[12:15], v[212:215], a[184:187], v[12:15]\n"
"v_cvt_pk_bf16_f32 v211, v198, v199\n"
"s_waitcnt lgkmcnt(8)\n"
"v_mfma_f32_16x16x32_bf16 v[12:15], v[216:219], a[188:191], v[12:15]\n"
"v_exp_f32 v200, v200\n"
"v_mfma_f32_16x16x32_bf16 v[16:19], v[216:219], a[192:195], v[16:19]\n"
"buffer_load_dwordx4 v249, s[36:39], s24 offen offset:1024 nt lds\n"
"v_mfma_f32_16x16x32_bf16 v[16:19], v[212:215], a[196:199], v[16:19]\n"
"v_exp_f32 v201, v201\n"
"v_mfma_f32_16x16x32_bf16 v[20:23], v[212:215], a[200:203], v[20:23]\n"
"s_waitcnt lgkmcnt(0)\n"
"v_mfma_f32_16x16x32_bf16 v[20:23], v[216:219], a[204:207], v[20:23]\n"
"v_exp_f32 v202, v202\n"
"v_mfma_f32_16x16x32_bf16 v[24:27], v[216:219], a[208:211], v[24:27]\n"
"buffer_load_dwordx4 v250, s[40:43], s24 offen offset:2048 nt lds\n"
"v_mfma_f32_16x16x32_bf16 v[24:27], v[212:215], a[212:215], v[24:27]\n"
"v_exp_f32 v203, v203\n"
"v_mfma_f32_16x16x32_bf16 v[28:31], v[212:215], a[216:219], v[28:31]\n"
"v_mfma_f32_16x16x32_bf16 v[28:31], v[216:219], a[220:223], v[28:31]\n"
"v_mfma_f32_16x16x32_bf16 v[192:195], a[96:99], a[32:35], v[228:231]\n"
"v_exp_f32 v204, v204\n"
"v_mfma_f32_16x16x32_bf16 v[192:195], a[100:103], a[36:39], v[192:195]\n"
"v_cvt_pk_bf16_f32 v212, v200, v201\n"
"v_cvt_pk_bf16_f32 v213, v202, v203\n"
"v_mfma_f32_16x16x32_bf16 v[192:195], a[104:107], a[40:43], v[192:195]\n"
"v_exp_f32 v205, v205\n"
"v_mfma_f32_16x16x32_bf16 v[192:195], a[108:111], a[44:47], v[192:195]\n"
"v_cvt_pk_bf16_f32 v214, v204, v205\n"
"v_mfma_f32_16x16x32_bf16 v[196:199], a[112:115], a[44:47], v[228:231]\n"
"v_exp_f32 v206, v206\n"
"v_mfma_f32_16x16x32_bf16 v[196:199], a[116:119], a[40:43], v[196:199]\n"
"buffer_load_dwordx4 v251, s[40:43], s24 offen offset:3072 nt lds\n"
"v_mfma_f32_16x16x32_bf16 v[196:199], a[120:123], a[36:39], v[196:199]\n"
"v_exp_f32 v207, v207\n"
"v_mfma_f32_16x16x32_bf16 v[196:199], a[124:127], a[32:35], v[196:199]\n"
"v_cvt_pk_bf16_f32 v215, v206, v207\n"
"v_mfma_f32_16x16x32_bf16 v[200:203], a[128:131], a[32:35], v[228:231]\n"
"v_exp_f32 v192, v192\n"
"v_mfma_f32_16x16x32_bf16 v[200:203], a[132:135], a[36:39], v[200:203]\n"
"s_add_u32 m0, s25, 49152\n"
"s_add_u32 s24, s24, 8192\n"
"v_mfma_f32_16x16x32_bf16 v[200:203], a[136:139], a[40:43], v[200:203]\n"
"v_exp_f32 v193, v193\n"
"v_mfma_f32_16x16x32_bf16 v[200:203], a[140:143], a[44:47], v[200:203]\n"
"v_cvt_pk_bf16_f32 v216, v192, v193\n"
"v_mfma_f32_16x16x32_bf16 v[204:207], a[144:147], a[44:47], v[228:231]\n"
"v_exp_f32 v194, v194\n"
"v_mfma_f32_16x16x32_bf16 v[204:207], a[148:151], a[40:43], v[204:207]\n"
"buffer_load_dwordx4 v248, s[36:39], s24 offen nt lds\n"
"v_mfma_f32_16x16x32_bf16 v[204:207], a[152:155], a[36:39], v[204:207]\n"
"v_exp_f32 v195, v195\n"
"v_mfma_f32_16x16x32_bf16 v[204:207], a[156:159], a[32:35], v[204:207]\n"
"v_cvt_pk_bf16_f32 v217, v194, v195\n"
"v_smfmac_f32_16x16x64_bf16 a[228:231], a[248:251], v[208:215], v252\n"
"v_exp_f32 v196, v196\n"
"v_mfma_f32_16x16x32_bf16 v[32:35], v[208:211], a[160:163], v[32:35]\n"
"v_mfma_f32_16x16x32_bf16 v[32:35], v[212:215], a[164:167], v[32:35]\n"
"v_exp_f32 v197, v197\n"
"v_mfma_f32_16x16x32_bf16 v[36:39], v[212:215], a[168:171], v[36:39]\n"
"v_cvt_pk_bf16_f32 v218, v196, v197\n"
"v_mfma_f32_16x16x32_bf16 v[36:39], v[208:211], a[172:175], v[36:39]\n"
"v_exp_f32 v198, v198\n"
"v_mfma_f32_16x16x32_bf16 v[40:43], v[208:211], a[176:179], v[40:43]\n"
"v_mfma_f32_16x16x32_bf16 v[40:43], v[212:215], a[180:183], v[40:43]\n"
"v_exp_f32 v199, v199\n"
"v_mfma_f32_16x16x32_bf16 v[44:47], v[212:215], a[184:187], v[44:47]\n"
"v_cvt_pk_bf16_f32 v219, v198, v199\n"
"v_mfma_f32_16x16x32_bf16 v[44:47], v[208:211], a[188:191], v[44:47]\n"
"v_exp_f32 v200, v200\n"
"v_mfma_f32_16x16x32_bf16 v[48:51], v[208:211], a[192:195], v[48:51]\n"
"s_add_u32 s27, s27, 1\n"
"v_mov_b32 v255, s27\n"
"v_mfma_f32_16x16x32_bf16 v[48:51], v[212:215], a[196:199], v[48:51]\n"
"v_exp_f32 v201, v201\n"
"v_mfma_f32_16x16x32_bf16 v[52:55], v[212:215], a[200:203], v[52:55]\n"
"v_mfma_f32_16x16x32_bf16 v[52:55], v[208:211], a[204:207], v[52:55]\n"
"v_exp_f32 v202, v202\n"
"v_mfma_f32_16x16x32_bf16 v[56:59], v[208:211], a[208:211], v[56:59]\n"
"buffer_load_dwordx4 v249, s[36:39], s24 offen offset:1024 nt lds\n"
"v_mfma_f32_16x16x32_bf16 v[56:59], v[212:215], a[212:215], v[56:59]\n"
"v_exp_f32 v203, v203\n"
"v_mfma_f32_16x16x32_bf16 v[60:63], v[212:215], a[216:219], v[60:63]\n"
"s_waitcnt vmcnt(6)\n"
"ds_write_b32 v253, v255\n"
"v_mfma_f32_16x16x32_bf16 v[60:63], v[208:211], a[220:223], v[60:63]\n"
"v_mfma_f32_16x16x32_bf16 v[192:195], a[96:99], a[48:51], v[232:235]\n"
"v_exp_f32 v204, v204\n"
"v_mfma_f32_16x16x32_bf16 v[192:195], a[100:103], a[52:55], v[192:195]\n"
"v_cvt_pk_bf16_f32 v212, v200, v201\n"
"v_cvt_pk_bf16_f32 v213, v202, v203\n"
"v_mfma_f32_16x16x32_bf16 v[192:195], a[104:107], a[56:59], v[192:195]\n"
"v_exp_f32 v205, v205\n"
"v_mfma_f32_16x16x32_bf16 v[192:195], a[108:111], a[60:63], v[192:195]\n"
"v_cvt_pk_bf16_f32 v214, v204, v205\n"
"v_mfma_f32_16x16x32_bf16 v[196:199], a[112:115], a[60:63], v[232:235]\n"
"v_exp_f32 v206, v206\n"
"v_mfma_f32_16x16x32_bf16 v[196:199], a[116:119], a[56:59], v[196:199]\n"
"v_mfma_f32_16x16x32_bf16 v[196:199], a[120:123], a[52:55], v[196:199]\n"
"v_exp_f32 v207, v207\n"
"v_mfma_f32_16x16x32_bf16 v[196:199], a[124:127], a[48:51], v[196:199]\n"
"v_cvt_pk_bf16_f32 v215, v206, v207\n"
"v_mfma_f32_16x16x32_bf16 v[200:203], a[128:131], a[48:51], v[232:235]\n"
"v_exp_f32 v192, v192\n"
"v_mfma_f32_16x16x32_bf16 v[200:203], a[132:135], a[52:55], v[200:203]\n"
"v_xor_b32 v244, 0x10000, v244\n"
"v_xor_b32 v245, 0x10000, v245\n"
"v_mfma_f32_16x16x32_bf16 v[200:203], a[136:139], a[56:59], v[200:203]\n"
"v_exp_f32 v193, v193\n"
"v_mfma_f32_16x16x32_bf16 v[200:203], a[140:143], a[60:63], v[200:203]\n"
"v_cvt_pk_bf16_f32 v208, v192, v193\n"
"v_mfma_f32_16x16x32_bf16 v[204:207], a[144:147], a[60:63], v[232:235]\n"
"v_exp_f32 v194, v194\n"
"v_mfma_f32_16x16x32_bf16 v[204:207], a[148:151], a[56:59], v[204:207]\n"
"v_mfma_f32_16x16x32_bf16 v[204:207], a[152:155], a[52:55], v[204:207]\n"
"v_exp_f32 v195, v195\n"
"v_mfma_f32_16x16x32_bf16 v[204:207], a[156:159], a[48:51], v[204:207]\n"
"v_cvt_pk_bf16_f32 v209, v194, v195\n"
"v_smfmac_f32_16x16x64_bf16 a[232:235], a[248:251], v[212:219], v252\n"
"v_exp_f32 v196, v196\n"
"v_mfma_f32_16x16x32_bf16 v[64:67], v[216:219], a[160:163], v[64:67]\n"
"buffer_load_dwordx4 v250, s[40:43], s24 offen offset:2048 nt lds\n"
"v_mfma_f32_16x16x32_bf16 v[64:67], v[212:215], a[164:167], v[64:67]\n"
"v_exp_f32 v197, v197\n"
"v_mfma_f32_16x16x32_bf16 v[68:71], v[212:215], a[168:171], v[68:71]\n"
"v_cvt_pk_bf16_f32 v210, v196, v197\n"
"v_mfma_f32_16x16x32_bf16 v[68:71], v[216:219], a[172:175], v[68:71]\n"
"v_exp_f32 v198, v198\n"
"v_mfma_f32_16x16x32_bf16 v[72:75], v[216:219], a[176:179], v[72:75]\n"
"ds_read_b32 v255, v254\n"
"v_mfma_f32_16x16x32_bf16 v[72:75], v[212:215], a[180:183], v[72:75]\n"
"v_exp_f32 v199, v199\n"
"v_mfma_f32_16x16x32_bf16 v[76:79], v[212:215], a[184:187], v[76:79]\n"
"v_cvt_pk_bf16_f32 v211, v198, v199\n"
"v_mfma_f32_16x16x32_bf16 v[76:79], v[216:219], a[188:191], v[76:79]\n"
"v_exp_f32 v200, v200\n"
"v_mfma_f32_16x16x32_bf16 v[80:83], v[216:219], a[192:195], v[80:83]\n"
"v_mfma_f32_16x16x32_bf16 v[80:83], v[212:215], a[196:199], v[80:83]\n"
"v_exp_f32 v201, v201\n"
"v_mfma_f32_16x16x32_bf16 v[84:87], v[212:215], a[200:203], v[84:87]\n"
"v_mfma_f32_16x16x32_bf16 v[84:87], v[216:219], a[204:207], v[84:87]\n"
"v_exp_f32 v202, v202\n"
"v_mfma_f32_16x16x32_bf16 v[88:91], v[216:219], a[208:211], v[88:91]\n"
"v_mfma_f32_16x16x32_bf16 v[88:91], v[212:215], a[212:215], v[88:91]\n"
"v_exp_f32 v203, v203\n"
"v_mfma_f32_16x16x32_bf16 v[92:95], v[212:215], a[216:219], v[92:95]\n"
"s_waitcnt lgkmcnt(0)\n"
"v_cmp_gt_u32 vcc, s27, v255\n"
"v_mfma_f32_16x16x32_bf16 v[92:95], v[216:219], a[220:223], v[92:95]\n"
"s_cbranch_vccnz L_late5_4_%=\n"
"L_back5_4_%=:\n"
"v_mfma_f32_16x16x32_bf16 v[192:195], a[96:99], a[64:67], v[236:239]\n"
"v_exp_f32 v204, v204\n"
"v_mfma_f32_16x16x32_bf16 v[192:195], a[100:103], a[68:71], v[192:195]\n"
"v_cvt_pk_bf16_f32 v212, v200, v201\n"
"v_cvt_pk_bf16_f32 v213, v202, v203\n"
"v_mfma_f32_16x16x32_bf16 v[192:195], a[104:107], a[72:75], v[192:195]\n"
"v_exp_f32 v205, v205\n"
"v_mfma_f32_16x16x32_bf16 v[192:195], a[108:111], a[76:79], v[192:195]\n"
"ds_read_b128 a[96:99], v244 offset:0\n"
"v_cvt_pk_bf16_f32 v214, v204, v205\n"
"v_mfma_f32_16x16x32_bf16 v[196:199], a[112:115], a[76:79], v[236:239]\n"
"v_exp_f32 v206, v206\n"
"v_mfma_f32_16x16x32_bf16 v[196:199], a[116:119], a[72:75], v[196:199]\n"
"ds_read_b128 a[100:103], v245 offset:0\n"
"ds_read_b128 a[104:107], v244 offset:1024\n"
"v_mfma_f32_16x16x32_bf16 v[196:199], a[120:123], a[68:71], v[196:199]\n"
"v_exp_f32 v207, v207\n"
"v_mfma_f32_16x16x32_bf16 v[196:199], a[124:127], a[64:67], v[196:199]\n"
"ds_read_b128 a[108:111], v245 offset:1024\n"
"v_cvt_pk_bf16_f32 v215, v206, v207\n"
"v_mfma_f32_16x16x32_bf16 v[200:203], a[128:131], a[64:67], v[236:239]\n"
"v_exp_f32 v192, v192\n"
"v_mfma_f32_16x16x32_bf16 v[200:203], a[132:135], a[68:71], v[200:203]\n"
"ds_read_b128 a[112:115], v245 offset:9216\n"
"ds_read_b128 a[116:119], v244 offset:9216\n"
"v_mfma_f32_16x16x32_bf16 v[200:203], a[136:139], a[72:75], v[200:203]\n"
"v_exp_f32 v193, v193\n"
"v_mfma_f32_16x16x32_bf16 v[200:203], a[140:143], a[76:79], v[200:203]\n"
"ds_read_b128 a[120:123], v245 offset:8192\n"
"v_cvt_pk_bf16_f32 v216, v192, v193\n"
"v_mfma_f32_16x16x32_bf16 v[204:207], a[144:147], a[76:79], v[236:239]\n"
"v_exp_f32 v194, v194\n"
"v_mfma_f32_16x16x32_bf16 v[204:207], a[148:151], a[72:75], v[204:207]\n"
"ds_read_b128 a[124:127], v244 offset:8192\n"
"v_mfma_f32_16x16x32_bf16 v[204:207], a[152:155], a[68:71], v[204:207]\n"
"v_exp_f32 v195, v195\n"
"v_mfma_f32_16x16x32_bf16 v[204:207], a[156:159], a[64:67], v[204:207]\n"
"ds_read_b128 a[128:131], v244 offset:16384\n"
"v_cvt_pk_bf16_f32 v217, v194, v195\n"
"v_smfmac_f32_16x16x64_bf16 a[236:239], a[248:251], v[208:215], v252\n"
"v_exp_f32 v196, v196\n"
"v_mfma_f32_16x16x32_bf16 v[96:99], v[208:211], a[160:163], v[96:99]\n"
"ds_read_b128 a[132:135], v245 offset:16384\n"
"v_mfma_f32_16x16x32_bf16 v[96:99], v[212:215], a[164:167], v[96:99]\n"
"v_exp_f32 v197, v197\n"
"v_mfma_f32_16x16x32_bf16 v[100:103], v[212:215], a[168:171], v[100:103]\n"
"ds_read_b128 a[136:139], v244 offset:17408\n"
"v_cvt_pk_bf16_f32 v218, v196, v197\n"
"v_mfma_f32_16x16x32_bf16 v[100:103], v[208:211], a[172:175], v[100:103]\n"
"v_exp_f32 v198, v198\n"
"v_mfma_f32_16x16x32_bf16 v[104:107], v[208:211], a[176:179], v[104:107]\n"
"ds_read_b128 a[140:143], v245 offset:17408\n"
"ds_read_b128 a[144:147], v245 offset:25600\n"
"v_mfma_f32_16x16x32_bf16 v[104:107], v[212:215], a[180:183], v[104:107]\n"
"v_exp_f32 v199, v199\n"
"v_mfma_f32_16x16x32_bf16 v[108:111], v[212:215], a[184:187], v[108:111]\n"
"ds_read_b128 a[148:151], v244 offset:25600\n"
"v_cvt_pk_bf16_f32 v219, v198, v199\n"
"v_mfma_f32_16x16x32_bf16 v[108:111], v[208:211], a[188:191], v[108:111]\n"
"v_exp_f32 v200, v200\n"
"v_mfma_f32_16x16x32_bf16 v[112:115], v[208:211], a[192:195], v[112:115]\n"
"ds_read_b128 a[152:155], v245 offset:24576\n"
"v_mfma_f32_16x16x32_bf16 v[112:115], v[212:215], a[196:199], v[112:115]\n"
"ds_read_b128 a[156:159], v244 offset:24576\n"
"v_exp_f32 v201, v201\n"
"v_mfma_f32_16x16x32_bf16 v[116:119], v[212:215], a[200:203], v[116:119]\n"
"buffer_load_dwordx4 v251, s[40:43], s24 offen offset:3072 nt lds\n"
"v_mfma_f32_16x16x32_bf16 v[116:119], v[208:211], a[204:207], v[116:119]\n"
"v_exp_f32 v202, v202\n"
"v_mfma_f32_16x16x32_bf16 v[120:123], v[208:211], a[208:211], v[120:123]\n"
"s_xor_b32 s25, s25, 0x10000\n"
"s_mov_b32 m0, s25\n"
"s_add_u32 s24, s24, 8192\n"
"v_mfma_f32_16x16x32_bf16 v[120:123], v[212:215], a[212:215], v[120:123]\n"
"v_exp_f32 v203, v203\n"
"v_mfma_f32_16x16x32_bf16 v[124:127], v[212:215], a[216:219], v[124:127]\n"
"buffer_load_dwordx4 v248, s[36:39], s24 offen nt lds\n"
"v_mfma_f32_16x16x32_bf16 v[124:127], v[208:211], a[220:223], v[124:127]\n"
"s_waitcnt lgkmcnt(8)\n"
"v_mfma_f32_16x16x32_bf16 v[192:195], a[96:99], a[0:3], v[220:223]\n"
"v_exp_f32 v204, v204\n"
"v_mfma_f32_16x16x32_bf16 v[192:195], a[100:103], a[4:7], v[192:195]\n"
"v_cvt_pk_bf16_f32 v212, v200, v201\n"
"v_cvt_pk_bf16_f32 v213, v202, v203\n"
"v_mfma_f32_16x16x32_bf16 v[192:195], a[104:107], a[8:11], v[192:195]\n"
"v_exp_f32 v205, v205\n"
"v_mfma_f32_16x16x32_bf16 v[192:195], a[108:111], a[12:15], v[192:195]\n"
"v_cvt_pk_bf16_f32 v214, v204, v205\n"
"v_mfma_f32_16x16x32_bf16 v[196:199], a[112:115], a[12:15], v[220:223]\n"
"v_exp_f32 v206, v206\n"
"v_mfma_f32_16x16x32_bf16 v[196:199], a[116:119], a[8:11], v[196:199]\n"
"buffer_load_dwordx4 v249, s[36:39], s24 offen offset:1024 nt lds\n"
"v_mfma_f32_16x16x32_bf16 v[196:199], a[120:123], a[4:7], v[196:199]\n"
"v_exp_f32 v207, v207\n"
"v_mfma_f32_16x16x32_bf16 v[196:199], a[124:127], a[0:3], v[196:199]\n"
"v_cvt_pk_bf16_f32 v215, v206, v207\n"
"s_waitcnt lgkmcnt(0)\n"
"v_mfma_f32_16x16x32_bf16 v[200:203], a[128:131], a[0:3], v[220:223]\n"
"v_exp_f32 v192, v192\n"
"v_mfma_f32_16x16x32_bf16 v[200:203], a[132:135], a[4:7], v[200:203]\n"
"v_xor_b32 v246, 0x10000, v246\n"
"v_xor_b32 v247, 0x10000, v247\n"
"v_mfma_f32_16x16x32_bf16 v[200:203], a[136:139], a[8:11], v[200:203]\n"
"v_exp_f32 v193, v193\n"
"v_mfma_f32_16x16x32_bf16 v[200:203], a[140:143], a[12:15], v[200:203]\n"
"v_cvt_pk_bf16_f32 v208, v192, v193\n"
"v_mfma_f32_16x16x32_bf16 v[204:207], a[144:147], a[12:15], v[220:223]\n"
"v_exp_f32 v194, v194\n"
"v_mfma_f32_16x16x32_bf16 v[204:207], a[148:151], a[8:11], v[204:207]\n"
"v_mfma_f32_16x16x32_bf16 v[204:207], a[152:155], a[4:7], v[204:207]\n"
"v_exp_f32 v195, v195\n"
"v_mfma_f32_16x16x32_bf16 v[204:207], a[156:159], a[0:3], v[204:207]\n"
"v_cvt_pk_bf16_f32 v209, v194, v195\n"
"v_smfmac_f32_16x16x64_bf16 a[240:243], a[248:251], v[212:219], v252\n"
"v_exp_f32 v196, v196\n"
"v_mfma_f32_16x16x32_bf16 v[128:131], v[216:219], a[160:163], v[128:131]\n"
"ds_read_b64_tr_b16 a[160:161], v246 offset:2048\n"
"ds_read_b64_tr_b16 a[162:163], v246 offset:10240\n"
"v_mfma_f32_16x16x32_bf16 v[128:131], v[212:215], a[164:167], v[128:131]\n"
"v_exp_f32 v197, v197\n"
"v_mfma_f32_16x16x32_bf16 v[132:135], v[212:215], a[168:171], v[132:135]\n"
"ds_read_b64_tr_b16 a[164:165], v246 offset:18432\n"
"v_cvt_pk_bf16_f32 v210, v196, v197\n"
"v_mfma_f32_16x16x32_bf16 v[132:135], v[216:219], a[172:175], v[132:135]\n"
"v_exp_f32 v198, v198\n"
"v_mfma_f32_16x16x32_bf16 v[136:139], v[216:219], a[176:179], v[136:139]\n"
"ds_read_b64_tr_b16 a[166:167], v246 offset:26624\n"
"ds_read_b64_tr_b16 a[168:169], v247 offset:18432\n"
"v_mfma_f32_16x16x32_bf16 v[136:139], v[212:215], a[180:183], v[136:139]\n"
"v_exp_f32 v199, v199\n"
"v_mfma_f32_16x16x32_bf16 v[140:143], v[212:215], a[184:187], v[140:143]\n"
"ds_read_b64_tr_b16 a[170:171], v247 offset:26624\n"
"v_cvt_pk_bf16_f32 v211, v198, v199\n"
"v_mfma_f32_16x16x32_bf16 v[140:143], v[216:219], a[188:191], v[140:143]\n"
"v_exp_f32 v200, v200\n"
"v_mfma_f32_16x16x32_bf16 v[144:147], v[216:219], a[192:195], v[144:147]\n"
"ds_read_b64_tr_b16 a[172:173], v247 offset:2048\n"
"ds_read_b64_tr_b16 a[174:175], v247 offset:10240\n"
"v_mfma_f32_16x16x32_bf16 v[144:147], v[212:215], a[196:199], v[144:147]\n"
"v_exp_f32 v201, v201\n"
"v_mfma_f32_16x16x32_bf16 v[148:151], v[212:215], a[200:203], v[148:151]\n"
"ds_read_b64_tr_b16 a[176:177], v246 offset:2560\n"
"ds_read_b64_tr_b16 a[178:179], v246 offset:10752\n"
"v_mfma_f32_16x16x32_bf16 v[148:151], v[216:219], a[204:207], v[148:151]\n"
"v_exp_f32 v202, v202\n"
"v_mfma_f32_16x16x32_bf16 v[152:155], v[216:219], a[208:211], v[152:155]\n"
"ds_read_b64_tr_b16 a[180:181], v246 offset:18944\n"
"ds_read_b64_tr_b16 a[182:183], v246 offset:27136\n"
"v_mfma_f32_16x16x32_bf16 v[152:155], v[212:215], a[212:215], v[152:155]\n"
"v_exp_f32 v203, v203\n"
"v_mfma_f32_16x16x32_bf16 v[156:159], v[212:215], a[216:219], v[156:159]\n"
"ds_read_b64_tr_b16 a[184:185], v247 offset:18944\n"
"ds_read_b64_tr_b16 a[186:187], v247 offset:27136\n"
"v_mfma_f32_16x16x32_bf16 v[156:159], v[216:219], a[220:223], v[156:159]\n"
"ds_read_b64_tr_b16 a[188:189], v247 offset:2560\n"
"ds_read_b64_tr_b16 a[190:191], v247 offset:10752\n"
"v_mfma_f32_16x16x32_bf16 v[192:195], a[96:99], a[16:19], v[224:227]\n"
"v_exp_f32 v204, v204\n"
"v_mfma_f32_16x16x32_bf16 v[192:195], a[100:103], a[20:23], v[192:195]\n"
"v_cvt_pk_bf16_f32 v212, v200, v201\n"
"v_cvt_pk_bf16_f32 v213, v202, v203\n"
"v_mfma_f32_16x16x32_bf16 v[192:195], a[104:107], a[24:27], v[192:195]\n"
"v_exp_f32 v205, v205\n"
"v_mfma_f32_16x16x32_bf16 v[192:195], a[108:111], a[28:31], v[192:195]\n"
"ds_read_b64_tr_b16 a[192:193], v246 offset:3072\n"
"v_cvt_pk_bf16_f32 v214, v204, v205\n"
"v_mfma_f32_16x16x32_bf16 v[196:199], a[112:115], a[28:31], v[224:227]\n"
"v_exp_f32 v206, v206\n"
"v_mfma_f32_16x16x32_bf16 v[196:199], a[116:119], a[24:27], v[196:199]\n"
"ds_read_b64_tr_b16 a[194:195], v246 offset:11264\n"
"ds_read_b64_tr_b16 a[196:197], v246 offset:19456\n"
"v_mfma_f32_16x16x32_bf16 v[196:199], a[120:123], a[20:23], v[196:199]\n"
"v_exp_f32 v207, v207\n"
"v_mfma_f32_16x16x32_bf16 v[196:199], a[124:127], a[16:19], v[196:199]\n"
"ds_read_b64_tr_b16 a[198:199], v246 offset:27648\n"
"v_cvt_pk_bf16_f32 v215, v206, v207\n"
"v_mfma_f32_16x16x32_bf16 v[200:203], a[128:131], a[16:19], v[224:227]\n"
"v_exp_f32 v192, v192\n"
"v_mfma_f32_16x16x32_bf16 v[200:203], a[132:135], a[20:23], v[200:203]\n"
"ds_read_b64_tr_b16 a[200:201], v247 offset:19456\n"
"ds_read_b64_tr_b16 a[202:203], v247 offset:27648\n"
"v_mfma_f32_16x16x32_bf16 v[200:203], a[136:139], a[24:27], v[200:203]\n"
"v_exp_f32 v193, v193\n"
"v_mfma_f32_16x16x32_bf16 v[200:203], a[140:143], a[28:31], v[200:203]\n"
"ds_read_b64_tr_b16 a[204:205], v247 offset:3072\n"
"v_cvt_pk_bf16_f32 v216, v192, v193\n"
"v_mfma_f32_16x16x32_bf16 v[204:207], a[144:147], a[28:31], v[224:227]\n"
"v_exp_f32 v194, v194\n"
"v_mfma_f32_16x16x32_bf16 v[204:207], a[148:151], a[24:27], v[204:207]\n"
"ds_read_b64_tr_b16 a[206:207], v247 offset:11264\n"
"ds_read_b64_tr_b16 a[208:209], v246 offset:3584\n"
"v_mfma_f32_16x16x32_bf16 v[204:207], a[152:155], a[20:23], v[204:207]\n"
"v_exp_f32 v195, v195\n"
"v_mfma_f32_16x16x32_bf16 v[204:207], a[156:159], a[16:19], v[204:207]\n"
"v_cvt_pk_bf16_f32 v217, v194, v195\n"
"s_waitcnt lgkmcnt(15)\n"
"v_smfmac_f32_16x16x64_bf16 a[224:227], a[248:251], v[208:215], v252\n"
"v_exp_f32 v196, v196\n"
"v_mfma_f32_16x16x32_bf16 v[0:3], v[208:211], a[160:163], v[0:3]\n"
"ds_read_b64_tr_b16 a[210:211], v246 offset:11776\n"
"ds_read_b64_tr_b16 a[212:213], v246 offset:19968\n"
"v_mfma_f32_16x16x32_bf16 v[0:3], v[212:215], a[164:167], v[0:3]\n"
"v_exp_f32 v197, v197\n"
"v_mfma_f32_16x16x32_bf16 v[4:7], v[212:215], a[168:171], v[4:7]\n"
"v_cvt_pk_bf16_f32 v218, v196, v197\n"
"s_waitcnt lgkmcnt(11)\n"
"v_mfma_f32_16x16x32_bf16 v[4:7], v[208:211], a[172:175], v[4:7]\n"
"v_exp_f32 v198, v198\n"
"v_mfma_f32_16x16x32_bf16 v[8:11], v[208:211], a[176:179], v[8:11]\n"
"ds_read_b64_tr_b16 a[214:215], v246 offset:28160\n"
"ds_read_b64_tr_b16 a[216:217], v247 offset:19968\n"
"ds_read_b64_tr_b16 a[218:219], v247 offset:28160\n"
"ds_read_b64_tr_b16 a[220:221], v247 offset:3584\n"
"ds_read_b64_tr_b16 a[222:223], v247 offset:11776\n"
"v_mfma_f32_16x16x32_bf16 v[8:11], v[212:215], a[180:183], v[8:11]\n"
"v_exp_f32 v199, v199\n"
"v_mfma_f32_16x16x32_bf16 v[12:15], v[212:215], a[184:187], v[12:15]\n"
"v_cvt_pk_bf16_f32 v219, v198, v199\n"
"s_waitcnt lgkmcnt(8)\n"
"v_mfma_f32_16x16x32_bf16 v[12:15], v[208:211], a[188:191], v[12:15]\n"
"v_exp_f32 v200, v200\n"
"v_mfma_f32_16x16x32_bf16 v[16:19], v[208:211], a[192:195], v[16:19]\n"
"buffer_load_dwordx4 v250, s[40:43], s24 offen offset:2048 nt lds\n"
"v_mfma_f32_16x16x32_bf16 v[16:19], v[212:215], a[196:199], v[16:19]\n"
"v_exp_f32 v201, v201\n"
"v_mfma_f32_16x16x32_bf16 v[20:23], v[212:215], a[200:203], v[20:23]\n"
"s_waitcnt lgkmcnt(0)\n"
"v_mfma_f32_16x16x32_bf16 v[20:23], v[208:211], a[204:207], v[20:23]\n"
"v_exp_f32 v202, v202\n"
"v_mfma_f32_16x16x32_bf16 v[24:27], v[208:211], a[208:211], v[24:27]\n"
"buffer_load_dwordx4 v251, s[40:43], s24 offen offset:3072 nt lds\n"
"v_mfma_f32_16x16x32_bf16 v[24:27], v[212:215], a[212:215], v[24:27]\n"
"v_exp_f32 v203, v203\n"
"v_mfma_f32_16x16x32_bf16 v[28:31], v[212:215], a[216:219], v[28:31]\n"
"v_mfma_f32_16x16x32_bf16 v[28:31], v[208:211], a[220:223], v[28:31]\n"
"v_mfma_f32_16x16x32_bf16 v[192:195], a[96:99], a[32:35], v[228:231]\n"
"v_exp_f32 v204, v204\n"
"v_mfma_f32_16x16x32_bf16 v[192:195], a[100:103], a[36:39], v[192:195]\n"
"v_cvt_pk_bf16_f32 v212, v200, v201\n"
"v_cvt_pk_bf16_f32 v213, v202, v203\n"
"v_mfma_f32_16x16x32_bf16 v[192:195], a[104:107], a[40:43], v[192:195]\n"
"v_exp_f32 v205, v205\n"
"v_mfma_f32_16x16x32_bf16 v[192:195], a[108:111], a[44:47], v[192:195]\n"
"v_cvt_pk_bf16_f32 v214, v204, v205\n"
"s_add_u32 m0, s25, 16384\n"
"s_add_u32 s24, s24, 8192\n"
"v_mfma_f32_16x16x32_bf16 v[196:199], a[112:115], a[44:47], v[228:231]\n"
"v_exp_f32 v206, v206\n"
"v_mfma_f32_16x16x32_bf16 v[196:199], a[116:119], a[40:43], v[196:199]\n"
"buffer_load_dwordx4 v248, s[36:39], s24 offen nt lds\n"
"v_mfma_f32_16x16x32_bf16 v[196:199], a[120:123], a[36:39], v[196:199]\n"
"v_exp_f32 v207, v207\n"
"v_mfma_f32_16x16x32_bf16 v[196:199], a[124:127], a[32:35], v[196:199]\n"
"v_cvt_pk_bf16_f32 v215, v206, v207\n"
"v_mfma_f32_16x16x32_bf16 v[200:203], a[128:131], a[32:35], v[228:231]\n"
"v_exp_f32 v192, v192\n"
"v_mfma_f32_16x16x32_bf16 v[200:203], a[132:135], a[36:39], v[200:203]\n"
"v_mfma_f32_16x16x32_bf16 v[200:203], a[136:139], a[40:43], v[200:203]\n"
"v_exp_f32 v193, v193\n"
"v_mfma_f32_16x16x32_bf16 v[200:203], a[140:143], a[44:47], v[200:203]\n"
"v_cvt_pk_bf16_f32 v208, v192, v193\n"
"v_mfma_f32_16x16x32_bf16 v[204:207], a[144:147], a[44:47], v[228:231]\n"
"v_exp_f32 v194, v194\n"
"v_mfma_f32_16x16x32_bf16 v[204:207], a[148:151], a[40:43], v[204:207]\n"
"v_mfma_f32_16x16x32_bf16 v[204:207], a[152:155], a[36:39], v[204:207]\n"
"v_exp_f32 v195, v195\n"
"v_mfma_f32_16x16x32_bf16 v[204:207], a[156:159], a[32:35], v[204:207]\n"
"v_cvt_pk_bf16_f32 v209, v194, v195\n"
"v_smfmac_f32_16x16x64_bf16 a[228:231], a[248:251], v[212:219], v252\n"
"v_exp_f32 v196, v196\n"
"v_mfma_f32_16x16x32_bf16 v[32:35], v[216:219], a[160:163], v[32:35]\n"
"v_mfma_f32_16x16x32_bf16 v[32:35], v[212:215], a[164:167], v[32:35]\n"
"v_exp_f32 v197, v197\n"
"v_mfma_f32_16x16x32_bf16 v[36:39], v[212:215], a[168:171], v[36:39]\n"
"v_cvt_pk_bf16_f32 v210, v196, v197\n"
"v_mfma_f32_16x16x32_bf16 v[36:39], v[216:219], a[172:175], v[36:39]\n"
"v_exp_f32 v198, v198\n"
"v_mfma_f32_16x16x32_bf16 v[40:43], v[216:219], a[176:179], v[40:43]\n"
"buffer_load_dwordx4 v249, s[36:39], s24 offen offset:1024 nt lds\n"
"v_mfma_f32_16x16x32_bf16 v[40:43], v[212:215], a[180:183], v[40:43]\n"
"v_exp_f32 v199, v199\n"
"v_mfma_f32_16x16x32_bf16 v[44:47], v[212:215], a[184:187], v[44:47]\n"
"v_cvt_pk_bf16_f32 v211, v198, v199\n"
"v_mfma_f32_16x16x32_bf16 v[44:47], v[216:219], a[188:191], v[44:47]\n"
"v_exp_f32 v200, v200\n"
"v_mfma_f32_16x16x32_bf16 v[48:51], v[216:219], a[192:195], v[48:51]\n"
"s_add_u32 s27, s27, 1\n"
"v_mov_b32 v255, s27\n"
"v_mfma_f32_16x16x32_bf16 v[48:51], v[212:215], a[196:199], v[48:51]\n"
"v_exp_f32 v201, v201\n"
"v_mfma_f32_16x16x32_bf16 v[52:55], v[212:215], a[200:203], v[52:55]\n"
"v_mfma_f32_16x16x32_bf16 v[52:55], v[216:219], a[204:207], v[52:55]\n"
"v_exp_f32 v202, v202\n"
"v_mfma_f32_16x16x32_bf16 v[56:59], v[216:219], a[208:211], v[56:59]\n"
"v_mfma_f32_16x16x32_bf16 v[56:59], v[212:215], a[212:215], v[56:59]\n"
"v_exp_f32 v203, v203\n"
"v_mfma_f32_16x16x32_bf16 v[60:63], v[212:215], a[216:219], v[60:63]\n"
"s_waitcnt vmcnt(6)\n"
"ds_write_b32 v253, v255\n"
"v_mfma_f32_16x16x32_bf16 v[60:63], v[216:219], a[220:223], v[60:63]\n"
".p2align 6\n"
"L_loop5_%=:\n"
"v_mfma_f32_16x16x32_bf16 v[192:195], a[96:99], a[48:51], v[232:235]\n"
"v_exp_f32 v204, v204\n"
"v_mfma_f32_16x16x32_bf16 v[192:195], a[100:103], a[52:55], v[192:195]\n"
"v_cvt_pk_bf16_f32 v212, v200, v201\n"
"v_cvt_pk_bf16_f32 v213, v202, v203\n"
"v_mfma_f32_16x16x32_bf16 v[192:195], a[104:107], a[56:59], v[192:195]\n"
"v_exp_f32 v205, v205\n"
"v_mfma_f32_16x16x32_bf16 v[192:195], a[108:111], a[60:63], v[192:195]\n"
"v_cvt_pk_bf16_f32 v214, v204, v205\n"
"v_mfma_f32_16x16x32_bf16 v[196:199], a[112:115], a[60:63], v[232:235]\n"
"v_exp_f32 v206, v206\n"
"v_mfma_f32_16x16x32_bf16 v[196:199], a[116:119], a[56:59], v[196:199]\n"
"v_mfma_f32_16x16x32_bf16 v[196:199], a[120:123], a[52:55], v[196:199]\n"
"v_exp_f32 v207, v207\n"
"v_mfma_f32_16x16x32_bf16 v[196:199], a[124:127], a[48:51], v[196:199]\n"
"v_cvt_pk_bf16_f32 v215, v206, v207\n"
"v_mfma_f32_16x16x32_bf16 v[200:203], a[128:131], a[48:51], v[232:235]\n"
"v_exp_f32 v192, v192\n"
"v_mfma_f32_16x16x32_bf16 v[200:203], a[132:135], a[52:55], v[200:203]\n"
"buffer_load_dwordx4 v250, s[40:43], s24 offen offset:2048 nt lds\n"
"v_mfma_f32_16x16x32_bf16 v[200:203], a[136:139], a[56:59], v[200:203]\n"
"v_exp_f32 v193, v193\n"
"v_mfma_f32_16x16x32_bf16 v[200:203], a[140:143], a[60:63], v[200:203]\n"
"v_cvt_pk_bf16_f32 v216, v192, v193\n"
"v_mfma_f32_16x16x32_bf16 v[204:207], a[144:147], a[60:63], v[232:235]\n"
"v_exp_f32 v194, v194\n"
"v_mfma_f32_16x16x32_bf16 v[204:207], a[148:151], a[56:59], v[204:207]\n"
"v_mfma_f32_16x16x32_bf16 v[204:207], a[152:155], a[52:55], v[204:207]\n"
"v_exp_f32 v195, v195\n"
"v_mfma_f32_16x16x32_bf16 v[204:207], a[156:159], a[48:51], v[204:207]\n"
"v_cvt_pk_bf16_f32 v217, v194, v195\n"
"v_smfmac_f32_16x16x64_bf16 a[232:235], a[248:251], v[208:215], v252\n"
"v_exp_f32 v196, v196\n"
"v_mfma_f32_16x16x32_bf16 v[64:67], v[208:211], a[160:163], v[64:67]\n"
"v_mfma_f32_16x16x32_bf16 v[64:67], v[212:215], a[164:167], v[64:67]\n"
"v_exp_f32 v197, v197\n"
"v_mfma_f32_16x16x32_bf16 v[68:71], v[212:215], a[168:171], v[68:71]\n"
"v_cvt_pk_bf16_f32 v218, v196, v197\n"
"v_mfma_f32_16x16x32_bf16 v[68:71], v[208:211], a[172:175], v[68:71]\n"
"v_exp_f32 v198, v198\n"
"v_mfma_f32_16x16x32_bf16 v[72:75], v[208:211], a[176:179], v[72:75]\n"
"ds_read_b32 v255, v254\n"
"v_mfma_f32_16x16x32_bf16 v[72:75], v[212:215], a[180:183], v[72:75]\n"
"v_exp_f32 v199, v199\n"
"v_mfma_f32_16x16x32_bf16 v[76:79], v[212:215], a[184:187], v[76:79]\n"
"v_cvt_pk_bf16_f32 v219, v198, v199\n"
"v_mfma_f32_16x16x32_bf16 v[76:79], v[208:211], a[188:191], v[76:79]\n"
"v_exp_f32 v200, v200\n"
"v_mfma_f32_16x16x32_bf16 v[80:83], v[208:211], a[192:195], v[80:83]\n"
"v_mfma_f32_16x16x32_bf16 v[80:83], v[212:215], a[196:199], v[80:83]\n"
"v_exp_f32 v201, v201\n"
"v_mfma_f32_16x16x32_bf16 v[84:87], v[212:215], a[200:203], v[84:87]\n"
"v_mfma_f32_16x16x32_bf16 v[84:87], v[208:211], a[204:207], v[84:87]\n"
"v_exp_f32 v202, v202\n"
"v_mfma_f32_16x16x32_bf16 v[88:91], v[208:211], a[208:211], v[88:91]\n"
"buffer_load_dwordx4 v251, s[40:43], s24 offen offset:3072 nt lds\n"
"v_mfma_f32_16x16x32_bf16 v[88:91], v[212:215], a[212:215], v[88:91]\n"
"v_exp_f32 v203, v203\n"
"v_mfma_f32_16x16x32_bf16 v[92:95], v[212:215], a[216:219], v[92:95]\n"
"s_waitcnt lgkmcnt(0)\n"
"v_cmp_gt_u32 vcc, s27, v255\n"
"v_mfma_f32_16x16x32_bf16 v[92:95], v[208:211], a[220:223], v[92:95]\n"
"s_cbranch_vccnz L_late5_5_%=\n"
"L_back5_5_%=:\n"
"v_mfma_f32_16x16x32_bf16 v[192:195], a[96:99], a[64:67], v[236:239]\n"
"v_exp_f32 v204, v204\n"
"v_mfma_f32_16x16x32_bf16 v[192:195], a[100:103], a[68:71], v[192:195]\n"
"v_cvt_pk_bf16_f32 v212, v200, v201\n"
"v_cvt_pk_bf16_f32 v213, v202, v203\n"
"v_mfma_f32_16x16x32_bf16 v[192:195], a[104:107], a[72:75], v[192:195]\n"
"v_exp_f32 v205, v205\n"
"v_mfma_f32_16x16x32_bf16 v[192:195], a[108:111], a[76:79], v[192:195]\n"
"ds_read_b128 a[96:99], v244 offset:32768\n"
"v_cvt_pk_bf16_f32 v214, v204, v205\n"
"v_mfma_f32_16x16x32_bf16 v[196:199], a[112:115], a[76:79], v[236:239]\n"
"v_exp_f32 v206, v206\n"
"v_mfma_f32_16x16x32_bf16 v[196:199], a[116:119], a[72:75], v[196:199]\n"
"ds_read_b128 a[100:103], v245 offset:32768\n"
"ds_read_b128 a[104:107], v244 offset:33792\n"
"v_mfma_f32_16x16x32_bf16 v[196:199], a[120:123], a[68:71], v[196:199]\n"
"v_exp_f32 v207, v207\n"
"v_mfma_f32_16x16x32_bf16 v[196:199], a[124:127], a[64:67], v[196:199]\n"
"ds_read_b128 a[108:111], v245 offset:33792\n"
"v_cvt_pk_bf16_f32 v215, v206, v207\n"
"v_mfma_f32_16x16x32_bf16 v[200:203], a[128:131], a[64:67], v[236:239]\n"
"v_exp_f32 v192, v192\n"
"v_mfma_f32_16x16x32_bf16 v[200:203], a[132:135], a[68:71], v[200:203]\n"
"ds_read_b128 a[112:115], v245 offset:41984\n"
"ds_read_b128 a[116:119], v244 offset:41984\n"
"v_mfma_f32_16x16x32_bf16 v[200:203], a[136:139], a[72:75], v[200:203]\n"
"v_exp_f32 v193, v193\n"
"v_mfma_f32_16x16x32_bf16 v[200:203], a[140:143], a[76:79], v[200:203]\n"
"ds_read_b128 a[120:123], v245 offset:40960\n"
"v_cvt_pk_bf16_f32 v208, v192, v193\n"
"v_mfma_f32_16x16x32_bf16 v[204:207], a[144:147], a[76:79], v[236:239]\n"
"v_exp_f32 v194, v194\n"
"v_mfma_f32_16x16x32_bf16 v[204:207], a[148:151], a[72:75], v[204:207]\n"
"ds_read_b128 a[124:127], v244 offset:40960\n"
"v_mfma_f32_16x16x32_bf16 v[204:207], a[152:155], a[68:71], v[204:207]\n"
"v_exp_f32 v195, v195\n"
"v_mfma_f32_16x16x32_bf16 v[204:207], a[156:159], a[64:67], v[204:207]\n"
"ds_read_b128 a[128:131], v244 offset:49152\n"
"v_cvt_pk_bf16_f32 v209, v194, v195\n"
"v_smfmac_f32_16x16x64_bf16 a[236:239], a[248:251], v[212:219], v252\n"
"v_exp_f32 v196, v196\n"
"v_mfma_f32_16x16x32_bf16 v[96:99], v[216:219], a[160:163], v[96:99]\n"
"ds_read_b128 a[132:135], v245 offset:49152\n"
"v_mfma_f32_16x16x32_bf16 v[96:99], v[212:215], a[164:167], v[96:99]\n"
"v_exp_f32 v197, v197\n"
"v_mfma_f32_16x16x32_bf16 v[100:103], v[212:215], a[168:171], v[100:103]\n"
"ds_read_b128 a[136:139], v244 offset:50176\n"
"v_cvt_pk_bf16_f32 v210, v196, v197\n"
"v_mfma_f32_16x16x32_bf16 v[100:103], v[216:219], a[172:175], v[100:103]\n"
"v_exp_f32 v198, v198\n"
"v_mfma_f32_16x16x32_bf16 v[104:107], v[216:219], a[176:179], v[104:107]\n"
"ds_read_b128 a[140:143], v245 offset:50176\n"
"ds_read_b128 a[144:147], v245 offset:58368\n"
"v_mfma_f32_16x16x32_bf16 v[104:107], v[212:215], a[180:183], v[104:107]\n"
"v_exp_f32 v199, v199\n"
"v_mfma_f32_16x16x32_bf16 v[108:111], v[212:215], a[184:187], v[108:111]\n"
"v_cvt_pk_bf16_f32 v211, v198, v199\n"
"s_add_u32 m0, s25, 32768\n"
"v_mfma_f32_16x16x32_bf16 v[108:111], v[216:219], a[188:191], v[108:111]\n"
"v_exp_f32 v200, v200\n"
"v_mfma_f32_16x16x32_bf16 v[112:115], v[216:219], a[192:195], v[112:115]\n"
"ds_read_b128 a[148:151], v244 offset:58368\n"
"s_add_u32 s24, s24, 8192\n"
"v_mfma_f32_16x16x32_bf16 v[112:115], v[212:215], a[196:199], v[112:115]\n"
"v_exp_f32 v201, v201\n"
"v_mfma_f32_16x16x32_bf16 v[116:119], v[212:215], a[200:203], v[116:119]\n"
"buffer_load_dwordx4 v248, s[36:39], s24 offen nt lds\n"
"v_mfma_f32_16x16x32_bf16 v[116:119], v[216:219], a[204:207], v[116:119]\n"
"v_exp_f32 v202, v202\n"
"v_mfma_f32_16x16x32_bf16 v[120:123], v[216:219], a[208:211], v[120:123]\n"
"ds_read_b128 a[152:155], v245 offset:57344\n"
"ds_read_b128 a[156:159], v244 offset:57344\n"
"v_mfma_f32_16x16x32_bf16 v[120:123], v[212:215], a[212:215], v[120:123]\n"
"v_exp_f32 v203, v203\n"
"v_mfma_f32_16x16x32_bf16 v[124:127], v[212:215], a[216:219], v[124:127]\n"
"v_mfma_f32_16x16x32_bf16 v[124:127], v[216:219], a[220:223], v[124:127]\n"
"s_waitcnt lgkmcnt(8)\n"
"v_mfma_f32_16x16x32_bf16 v[192:195], a[96:99], a[0:3], v[220:223]\n"
"v_exp_f32 v204, v204\n"
"v_mfma_f32_16x16x32_bf16 v[192:195], a[100:103], a[4:7], v[192:195]\n"
"v_cvt_pk_bf16_f32 v212, v200, v201\n"
"v_cvt_pk_bf16_f32 v213, v202, v203\n"
"v_mfma_f32_16x16x32_bf16 v[192:195], a[104:107], a[8:11], v[192:195]\n"
"v_exp_f32 v205, v205\n"
"v_mfma_f32_16x16x32_bf16 v[192:195], a[108:111], a[12:15], v[192:195]\n"
"v_cvt_pk_bf16_f32 v214, v204, v205\n"
"v_mfma_f32_16x16x32_bf16 v[196:199], a[112:115], a[12:15], v[220:223]\n"
"v_exp_f32 v206, v206\n"
"v_mfma_f32_16x16x32_bf16 v[196:199], a[116:119], a[8:11], v[196:199]\n"
"buffer_load_dwordx4 v249, s[36:39], s24 offen offset:1024 nt lds\n"
"v_mfma_f32_16x16x32_bf16 v[196:199], a[120:123], a[4:7], v[196:199]\n"
"v_exp_f32 v207, v207\n"
"v_mfma_f32_16x16x32_bf16 v[196:199], a[124:127], a[0:3], v[196:199]\n"
"v_cvt_pk_bf16_f32 v215, v206, v207\n"
"s_waitcnt lgkmcnt(0)\n"
"v_mfma_f32_16x16x32_bf16 v[200:203], a[128:131], a[0:3], v[220:223]\n"
"v_exp_f32 v192, v192\n"
"v_mfma_f32_16x16x32_bf16 v[200:203], a[132:135], a[4:7], v[200:203]\n"
"v_mfma_f32_16x16x32_bf16 v[200:203], a[136:139], a[8:11], v[200:203]\n"
"v_exp_f32 v193, v193\n"
"v_mfma_f32_16x16x32_bf16 v[200:203], a[140:143], a[12:15], v[200:203]\n"
"v_cvt_pk_bf16_f32 v216, v192, v193\n"
"v_mfma_f32_16x16x32_bf16 v[204:207], a[144:147], a[12:15], v[220:223]\n"
"v_exp_f32 v194, v194\n"
"v_mfma_f32_16x16x32_bf16 v[204:207], a[148:151], a[8:11], v[204:207]\n"
"v_mfma_f32_16x16x32_bf16 v[204:207], a[152:155], a[4:7], v[204:207]\n"
"v_exp_f32 v195, v195\n"
"v_mfma_f32_16x16x32_bf16 v[204:207], a[156:159], a[0:3], v[204:207]\n"
"v_cvt_pk_bf16_f32 v217, v194, v195\n"
"v_smfmac_f32_16x16x64_bf16 a[240:243], a[248:251], v[208:215], v252\n"
"v_exp_f32 v196, v196\n"
"v_mfma_f32_16x16x32_bf16 v[128:131], v[208:211], a[160:163], v[128:131]\n"
"ds_read_b64_tr_b16 a[160:161], v246 offset:34816\n"
"ds_read_b64_tr_b16 a[162:163], v246 offset:43008\n"
"v_mfma_f32_16x16x32_bf16 v[128:131], v[212:215], a[164:167], v[128:131]\n"
"v_exp_f32 v197, v197\n"
"v_mfma_f32_16x16x32_bf16 v[132:135], v[212:215], a[168:171], v[132:135]\n"
"ds_read_b64_tr_b16 a[164:165], v246 offset:51200\n"
"v_cvt_pk_bf16_f32 v218, v196, v197\n"
"v_mfma_f32_16x16x32_bf16 v[132:135], v[208:211], a[172:175], v[132:135]\n"
"v_exp_f32 v198, v198\n"
"v_mfma_f32_16x16x32_bf16 v[136:139], v[208:211], a[176:179], v[136:139]\n"
"ds_read_b64_tr_b16 a[166:167], v246 offset:59392\n"
"ds_read_b64_tr_b16 a[168:169], v247 offset:51200\n"
"v_mfma_f32_16x16x32_bf16 v[136:139], v[212:215], a[180:183], v[136:139]\n"
"v_exp_f32 v199, v199\n"
"v_mfma_f32_16x16x32_bf16 v[140:143], v[212:215], a[184:187], v[140:143]\n"
"ds_read_b64_tr_b16 a[170:171], v247 offset:59392\n"
"v_cvt_pk_bf16_f32 v219, v198, v199\n"
"v_mfma_f32_16x16x32_bf16 v[140:143], v[208:211], a[188:191], v[140:143]\n"
"v_exp_f32 v200, v200\n"
"v_mfma_f32_16x16x32_bf16 v[144:147], v[208:211], a[192:195], v[144:147]\n"
"ds_read_b64_tr_b16 a[172:173], v247 offset:34816\n"
"ds_read_b64_tr_b16 a[174:175], v247 offset:43008\n"
"v_mfma_f32_16x16x32_bf16 v[144:147], v[212:215], a[196:199], v[144:147]\n"
"v_exp_f32 v201, v201\n"
"v_mfma_f32_16x16x32_bf16 v[148:151], v[212:215], a[200:203], v[148:151]\n"
"ds_read_b64_tr_b16 a[176:177], v246 offset:35328\n"
"ds_read_b64_tr_b16 a[178:179], v246 offset:43520\n"
"v_mfma_f32_16x16x32_bf16 v[148:151], v[208:211], a[204:207], v[148:151]\n"
"v_exp_f32 v202, v202\n"
"v_mfma_f32_16x16x32_bf16 v[152:155], v[208:211], a[208:211], v[152:155]\n"
"ds_read_b64_tr_b16 a[180:181], v246 offset:51712\n"
"ds_read_b64_tr_b16 a[182:183], v246 offset:59904\n"
"v_mfma_f32_16x16x32_bf16 v[152:155], v[212:215], a[212:215], v[152:155]\n"
"v_exp_f32 v203, v203\n"
"v_mfma_f32_16x16x32_bf16 v[156:159], v[212:215], a[216:219], v[156:159]\n"
"ds_read_b64_tr_b16 a[184:185], v247 offset:51712\n"
"ds_read_b64_tr_b16 a[186:187], v247 offset:59904\n"
"v_mfma_f32_16x16x32_bf16 v[156:159], v[208:211], a[220:223], v[156:159]\n"
"ds_read_b64_tr_b16 a[188:189], v247 offset:35328\n"
"ds_read_b64_tr_b16 a[190:191], v247 offset:43520\n"
"v_mfma_f32_16x16x32_bf16 v[192:195], a[96:99], a[16:19], v[224:227]\n"
"v_exp_f32 v204, v204\n"
"v_mfma_f32_16x16x32_bf16 v[192:195], a[100:103], a[20:23], v[192:195]\n"
"v_cvt_pk_bf16_f32 v212, v200, v201\n"
"v_cvt_pk_bf16_f32 v213, v202, v203\n"
"v_mfma_f32_16x16x32_bf16 v[192:195], a[104:107], a[24:27], v[192:195]\n"
"v_exp_f32 v205, v205\n"
"v_mfma_f32_16x16x32_bf16 v[192:195], a[108:111], a[28:31], v[192:195]\n"
"ds_read_b64_tr_b16 a[192:193], v246 offset:35840\n"
"v_cvt_pk_bf16_f32 v214, v204, v205\n"
"v_mfma_f32_16x16x32_bf16 v[196:199], a[112:115], a[28:31], v[224:227]\n"
"v_exp_f32 v206, v206\n"
"v_mfma_f32_16x16x32_bf16 v[196:199], a[116:119], a[24:27], v[196:199]\n"
"ds_read_b64_tr_b16 a[194:195], v246 offset:44032\n"
"ds_read_b64_tr_b16 a[196:197], v246 offset:52224\n"
"v_mfma_f32_16x16x32_bf16 v[196:199], a[120:123], a[20:23], v[196:199]\n"
"v_exp_f32 v207, v207\n"
"v_mfma_f32_16x16x32_bf16 v[196:199], a[124:127], a[16:19], v[196:199]\n"
"ds_read_b64_tr_b16 a[198:199], v246 offset:60416\n"
"v_cvt_pk_bf16_f32 v215, v206, v207\n"
"v_mfma_f32_16x16x32_bf16 v[200:203], a[128:131], a[16:19], v[224:227]\n"
"v_exp_f32 v192, v192\n"
"v_mfma_f32_16x16x32_bf16 v[200:203], a[132:135], a[20:23], v[200:203]\n"
"ds_read_b64_tr_b16 a[200:201], v247 offset:52224\n"
"ds_read_b64_tr_b16 a[202:203], v247 offset:60416\n"
"v_mfma_f32_16x16x32_bf16 v[200:203], a[136:139], a[24:27], v[200:203]\n"
"v_exp_f32 v193, v193\n"
"v_mfma_f32_16x16x32_bf16 v[200:203], a[140:143], a[28:31], v[200:203]\n"
"ds_read_b64_tr_b16 a[204:205], v247 offset:35840\n"
"v_cvt_pk_bf16_f32 v208, v192, v193\n"
"v_mfma_f32_16x16x32_bf16 v[204:207], a[144:147], a[28:31], v[224:227]\n"
"v_exp_f32 v194, v194\n"
"v_mfma_f32_16x16x32_bf16 v[204:207], a[148:151], a[24:27], v[204:207]\n"
"ds_read_b64_tr_b16 a[206:207], v247 offset:44032\n"
"ds_read_b64_tr_b16 a[208:209], v246 offset:36352\n"
"v_mfma_f32_16x16x32_bf16 v[204:207], a[152:155], a[20:23], v[204:207]\n"
"v_exp_f32 v195, v195\n"
"v_mfma_f32_16x16x32_bf16 v[204:207], a[156:159], a[16:19], v[204:207]\n"
"v_cvt_pk_bf16_f32 v209, v194, v195\n"
"s_waitcnt lgkmcnt(15)\n"
"v_smfmac_f32_16x16x64_bf16 a[224:227], a[248:251], v[212:219], v252\n"
"v_exp_f32 v196, v196\n"
"v_mfma_f32_16x16x32_bf16 v[0:3], v[216:219], a[160:163], v[0:3]\n"
"ds_read_b64_tr_b16 a[210:211], v246 offset:44544\n"
"ds_read_b64_tr_b16 a[212:213], v246 offset:52736\n"
"v_mfma_f32_16x16x32_bf16 v[0:3], v[212:215], a[164:167], v[0:3]\n"
"v_exp_f32 v197, v197\n"
"v_mfma_f32_16x16x32_bf16 v[4:7], v[212:215], a[168:171], v[4:7]\n"
"v_cvt_pk_bf16_f32 v210, v196, v197\n"
"s_waitcnt lgkmcnt(11)\n"
"v_mfma_f32_16x16x32_bf16 v[4:7], v[216:219], a[172:175], v[4:7]\n"
"v_exp_f32 v198, v198\n"
"v_mfma_f32_16x16x32_bf16 v[8:11], v[216:219], a[176:179], v[8:11]\n"
"ds_read_b64_tr_b16 a[214:215], v246 offset:60928\n"
"ds_read_b64_tr_b16 a[216:217], v247 offset:52736\n"
"ds_read_b64_tr_b16 a[218:219], v247 offset:60928\n"
"ds_read_b64_tr_b16 a[220:221], v247 offset:36352\n"
"ds_read_b64_tr_b16 a[222:223], v247 offset:44544\n"
"v_mfma_f32_16x16x32_bf16 v[8:11], v[212:215], a[180:183], v[8:11]\n"
"v_exp_f32 v199, v199\n"
"v_mfma_f32_16x16x32_bf16 v[12:15], v[212:215], a[184:187], v[12:15]\n"
"v_cvt_pk_bf16_f32 v211, v198, v199\n"
"s_waitcnt lgkmcnt(8)\n"
"v_mfma_f32_16x16x32_bf16 v[12:15], v[216:219], a[188:191], v[12:15]\n"
"v_exp_f32 v200, v200\n"
"v_mfma_f32_16x16x32_bf16 v[16:19], v[216:219], a[192:195], v[16:19]\n"
"buffer_load_dwordx4 v250, s[40:43], s24 offen offset:2048 nt lds\n"
"v_mfma_f32_16x16x32_bf16 v[16:19], v[212:215], a[196:199], v[16:19]\n"
"v_exp_f32 v201, v201\n"
"v_mfma_f32_16x16x32_bf16 v[20:23], v[212:215], a[200:203], v[20:23]\n"
"s_waitcnt lgkmcnt(0)\n"
"v_mfma_f32_16x16x32_bf16 v[20:23], v[216:219], a[204:207], v[20:23]\n"
"v_exp_f32 v202, v202\n"
"v_mfma_f32_16x16x32_bf16 v[24:27], v[216:219], a[208:211], v[24:27]\n"
"buffer_load_dwordx4 v251, s[40:43], s24 offen offset:3072 nt lds\n"
"v_mfma_f32_16x16x32_bf16 v[24:27], v[212:215], a[212:215], v[24:27]\n"
"v_exp_f32 v203, v203\n"
"v_mfma_f32_16x16x32_bf16 v[28:31], v[212:215], a[216:219], v[28:31]\n"
"v_mfma_f32_16x16x32_bf16 v[28:31], v[216:219], a[220:223], v[28:31]\n"
"v_mfma_f32_16x16x32_bf16 v[192:195], a[96:99], a[32:35], v[228:231]\n"
"v_exp_f32 v204, v204\n"
"v_mfma_f32_16x16x32_bf16 v[192:195], a[100:103], a[36:39], v[192:195]\n"
"v_cvt_pk_bf16_f32 v212, v200, v201\n"
"v_cvt_pk_bf16_f32 v213, v202, v203\n"
"v_mfma_f32_16x16x32_bf16 v[192:195], a[104:107], a[40:43], v[192:195]\n"
"v_exp_f32 v205, v205\n"
"v_mfma_f32_16x16x32_bf16 v[192:195], a[108:111], a[44:47], v[192:195]\n"
"v_cvt_pk_bf16_f32 v214, v204, v205\n"
"s_add_u32 m0, s25, 49152\n"
"s_add_u32 s24, s24, 8192\n"
"v_mfma_f32_16x16x32_bf16 v[196:199], a[112:115], a[44:47], v[228:231]\n"
"v_exp_f32 v206, v206\n"
"v_mfma_f32_16x16x32_bf16 v[196:199], a[116:119], a[40:43], v[196:199]\n"
"buffer_load_dwordx4 v248, s[36:39], s24 offen nt lds\n"
"v_mfma_f32_16x16x32_bf16 v[196:199], a[120:123], a[36:39], v[196:199]\n"
"v_exp_f32 v207, v207\n"
"v_mfma_f32_16x16x32_bf16 v[196:199], a[124:127], a[32:35], v[196:199]\n"
"v_cvt_pk_bf16_f32 v215, v206, v207\n"
"v_mfma_f32_16x16x32_bf16 v[200:203], a[128:131], a[32:35], v[228:231]\n"
"v_exp_f32 v192, v192\n"
"v_mfma_f32_16x16x32_bf16 v[200:203], a[132:135], a[36:39], v[200:203]\n"
"v_mfma_f32_16x16x32_bf16 v[200:203], a[136:139], a[40:43], v[200:203]\n"
"v_exp_f32 v193, v193\n"
"v_mfma_f32_16x16x32_bf16 v[200:203], a[140:143], a[44:47], v[200:203]\n"
"v_cvt_pk_bf16_f32 v216, v192, v193\n"
"v_mfma_f32_16x16x32_bf16 v[204:207], a[144:147], a[44:47], v[228:231]\n"
"v_exp_f32 v194, v194\n"
"v_mfma_f32_16x16x32_bf16 v[204:207], a[148:151], a[40:43], v[204:207]\n"
"v_mfma_f32_16x16x32_bf16 v[204:207], a[152:155], a[36:39], v[204:207]\n"
"v_exp_f32 v195, v195\n"
"v_mfma_f32_16x16x32_bf16 v[204:207], a[156:159], a[32:35], v[204:207]\n"
"v_cvt_pk_bf16_f32 v217, v194, v195\n"
"v_smfmac_f32_16x16x64_bf16 a[228:231], a[248:251], v[208:215], v252\n"
"v_exp_f32 v196, v196\n"
"v_mfma_f32_16x16x32_bf16 v[32:35], v[208:211], a[160:163], v[32:35]\n"
"v_mfma_f32_16x16x32_bf16 v[32:35], v[212:215], a[164:167], v[32:35]\n"
"v_exp_f32 v197, v197\n"
"v_mfma_f32_16x16x32_bf16 v[36:39], v[212:215], a[168:171], v[36:39]\n"
"v_cvt_pk_bf16_f32 v218, v196, v197\n"
"v_mfma_f32_16x16x32_bf16 v[36:39], v[208:211], a[172:175], v[36:39]\n"
"v_exp_f32 v198, v198\n"
"v_mfma_f32_16x16x32_bf16 v[40:43], v[208:211], a[176:179], v[40:43]\n"
"buffer_load_dwordx4 v249, s[36:39], s24 offen offset:1024 nt lds\n"
"v_mfma_f32_16x16x32_bf16 v[40:43], v[212:215], a[180:183], v[40:43]\n"
"v_exp_f32 v199, v199\n"
"v_mfma_f32_16x16x32_bf16 v[44:47], v[212:215], a[184:187], v[44:47]\n"
"v_cvt_pk_bf16_f32 v219, v198, v199\n"
"v_mfma_f32_16x16x32_bf16 v[44:47], v[208:211], a[188:191], v[44:47]\n"
"v_exp_f32 v200, v200\n"
"v_mfma_f32_16x16x32_bf16 v[48:51], v[208:211], a[192:195], v[48:51]\n"
"s_add_u32 s27, s27, 1\n"
"v_mov_b32 v255, s27\n"
"v_mfma_f32_16x16x32_bf16 v[48:51], v[212:215], a[196:199], v[48:51]\n"
"v_exp_f32 v201, v201\n"
"v_mfma_f32_16x16x32_bf16 v[52:55], v[212:215], a[200:203], v[52:55]\n"
"v_mfma_f32_16x16x32_bf16 v[52:55], v[208:211], a[204:207], v[52:55]\n"
"v_exp_f32 v202, v202\n"
"v_mfma_f32_16x16x32_bf16 v[56:59], v[208:211], a[208:211], v[56:59]\n"
"v_mfma_f32_16x16x32_bf16 v[56:59], v[212:215], a[212:215], v[56:59]\n"
"v_exp_f32 v203, v203\n"
"v_mfma_f32_16x16x32_bf16 v[60:63], v[212:215], a[216:219], v[60:63]\n"
"s_waitcnt vmcnt(6)\n"
"ds_write_b32 v253, v255\n"
"v_mfma_f32_16x16x32_bf16 v[60:63], v[208:211], a[220:223], v[60:63]\n"
"v_mfma_f32_16x16x32_bf16 v[192:195], a[96:99], a[48:51], v[232:235]\n"
"v_exp_f32 v204, v204\n"
"v_mfma_f32_16x16x32_bf16 v[192:195], a[100:103], a[52:55], v[192:195]\n"
"v_cvt_pk_bf16_f32 v212, v200, v201\n"
"v_cvt_pk_bf16_f32 v213, v202, v203\n"
"v_mfma_f32_16x16x32_bf16 v[192:195], a[104:107], a[56:59], v[192:195]\n"
"v_exp_f32 v205, v205\n"
"v_mfma_f32_16x16x32_bf16 v[192:195], a[108:111], a[60:63], v[192:195]\n"
"v_cvt_pk_bf16_f32 v214, v204, v205\n"
"v_mfma_f32_16x16x32_bf16 v[196:199], a[112:115], a[60:63], v[232:235]\n"
"v_exp_f32 v206, v206\n"
"v_mfma_f32_16x16x32_bf16 v[196:199], a[116:119], a[56:59], v[196:199]\n"
"v_mfma_f32_16x16x32_bf16 v[196:199], a[120:123], a[52:55], v[196:199]\n"
"v_exp_f32 v207, v207\n"
"v_mfma_f32_16x16x32_bf16 v[196:199], a[124:127], a[48:51], v[196:199]\n"
"v_cvt_pk_bf16_f32 v215, v206, v207\n"
"v_mfma_f32_16x16x32_bf16 v[200:203], a[128:131], a[48:51], v[232:235]\n"
"v_exp_f32 v192, v192\n"
"v_mfma_f32_16x16x32_bf16 v[200:203], a[132:135], a[52:55], v[200:203]\n"
"buffer_load_dwordx4 v250, s[40:43], s24 offen offset:2048 nt lds\n"
"v_mfma_f32_16x16x32_bf16 v[200:203], a[136:139], a[56:59], v[200:203]\n"
"v_exp_f32 v193, v193\n"
"v_mfma_f32_16x16x32_bf16 v[200:203], a[140:143], a[60:63], v[200:203]\n"
"v_cvt_pk_bf16_f32 v208, v192, v193\n"
"v_xor_b32 v244, 0x10000, v244\n"
"v_mfma_f32_16x16x32_bf16 v[204:207], a[144:147], a[60:63], v[232:235]\n"
"v_exp_f32 v194, v194\n"
"v_mfma_f32_16x16x32_bf16 v[204:207], a[148:151], a[56:59], v[204:207]\n"
"v_xor_b32 v245, 0x10000, v245\n"
"v_mfma_f32_16x16x32_bf16 v[204:207], a[152:155], a[52:55], v[204:207]\n"
"v_exp_f32 v195, v195\n"
"v_mfma_f32_16x16x32_bf16 v[204:207], a[156:159], a[48:51], v[204:207]\n"
"v_cvt_pk_bf16_f32 v209, v194, v195\n"
"v_smfmac_f32_16x16x64_bf16 a[232:235], a[248:251], v[212:219], v252\n"
"v_exp_f32 v196, v196\n"
"v_mfma_f32_16x16x32_bf16 v[64:67], v[216:219], a[160:163], v[64:67]\n"
"v_mfma_f32_16x16x32_bf16 v[64:67], v[212:215], a[164:167], v[64:67]\n"
"v_exp_f32 v197, v197\n"
"v_mfma_f32_16x16x32_bf16 v[68:71], v[212:215], a[168:171], v[68:71]\n"
"v_cvt_pk_bf16_f32 v210, v196, v197\n"
"v_mfma_f32_16x16x32_bf16 v[68:71], v[216:219], a[172:175], v[68:71]\n"
"v_exp_f32 v198, v198\n"
"v_mfma_f32_16x16x32_bf16 v[72:75], v[216:219], a[176:179], v[72:75]\n"
"ds_read_b32 v255, v254\n"
"v_mfma_f32_16x16x32_bf16 v[72:75], v[212:215], a[180:183], v[72:75]\n"
"v_exp_f32 v199, v199\n"
"v_mfma_f32_16x16x32_bf16 v[76:79], v[212:215], a[184:187], v[76:79]\n"
"v_cvt_pk_bf16_f32 v211, v198, v199\n"
"v_mfma_f32_16x16x32_bf16 v[76:79], v[216:219], a[188:191], v[76:79]\n"
"v_exp_f32 v200, v200\n"
"v_mfma_f32_16x16x32_bf16 v[80:83], v[216:219], a[192:195], v[80:83]\n"
"v_mfma_f32_16x16x32_bf16 v[80:83], v[212:215], a[196:199], v[80:83]\n"
"v_exp_f32 v201, v201\n"
"v_mfma_f32_16x16x32_bf16 v[84:87], v[212:215], a[200:203], v[84:87]\n"
"v_mfma_f32_16x16x32_bf16 v[84:87], v[216:219], a[204:207], v[84:87]\n"
"v_exp_f32 v202, v202\n"
"v_mfma_f32_16x16x32_bf16 v[88:91], v[216:219], a[208:211], v[88:91]\n"
"buffer_load_dwordx4 v251, s[40:43], s24 offen offset:3072 nt lds\n"
"v_mfma_f32_16x16x32_bf16 v[88:91], v[212:215], a[212:215], v[88:91]\n"
"v_exp_f32 v203, v203\n"
"v_mfma_f32_16x16x32_bf16 v[92:95], v[212:215], a[216:219], v[92:95]\n"
"s_waitcnt lgkmcnt(0)\n"
"v_cmp_gt_u32 vcc, s27, v255\n"
"v_mfma_f32_16x16x32_bf16 v[92:95], v[216:219], a[220:223], v[92:95]\n"
"s_cbranch_vccnz L_late5_6_%=\n"
"L_back5_6_%=:\n"
"v_mfma_f32_16x16x32_bf16 v[192:195], a[96:99], a[64:67], v[236:239]\n"
"v_exp_f32 v204, v204\n"
"v_mfma_f32_16x16x32_bf16 v[192:195], a[100:103], a[68:71], v[192:195]\n"
"v_cvt_pk_bf16_f32 v212, v200, v201\n"
"v_cvt_pk_bf16_f32 v213, v202, v203\n"
"v_mfma_f32_16x16x32_bf16 v[192:195], a[104:107], a[72:75], v[192:195]\n"
"v_exp_f32 v205, v205\n"
"v_mfma_f32_16x16x32_bf16 v[192:195], a[108:111], a[76:79], v[192:195]\n"
"ds_read_b128 a[96:99], v244 offset:0\n"
"v_cvt_pk_bf16_f32 v214, v204, v205\n"
"v_mfma_f32_16x16x32_bf16 v[196:199], a[112:115], a[76:79], v[236:239]\n"
"v_exp_f32 v206, v206\n"
"v_mfma_f32_16x16x32_bf16 v[196:199], a[116:119], a[72:75], v[196:199]\n"
"ds_read_b128 a[100:103], v245 offset:0\n"
"ds_read_b128 a[104:107], v244 offset:1024\n"
"v_mfma_f32_16x16x32_bf16 v[196:199], a[120:123], a[68:71], v[196:199]\n"
"v_exp_f32 v207, v207\n"
"v_mfma_f32_16x16x32_bf16 v[196:199], a[124:127], a[64:67], v[196:199]\n"
"ds_read_b128 a[108:111], v245 offset:1024\n"
"v_cvt_pk_bf16_f32 v215, v206, v207\n"
"v_mfma_f32_16x16x32_bf16 v[200:203], a[128:131], a[64:67], v[236:239]\n"
"v_exp_f32 v192, v192\n"
"v_mfma_f32_16x16x32_bf16 v[200:203], a[132:135], a[68:71], v[200:203]\n"
"ds_read_b128 a[112:115], v245 offset:9216\n"
"ds_read_b128 a[116:119], v244 offset:9216\n"
"v_mfma_f32_16x16x32_bf16 v[200:203], a[136:139], a[72:75], v[200:203]\n"
"v_exp_f32 v193, v193\n"
"v_mfma_f32_16x16x32_bf16 v[200:203], a[140:143], a[76:79], v[200:203]\n"
"ds_read_b128 a[120:123], v245 offset:8192\n"
"v_cvt_pk_bf16_f32 v216, v192, v193\n"
"v_mfma_f32_16x16x32_bf16 v[204:207], a[144:147], a[76:79], v[236:239]\n"
"v_exp_f32 v194, v194\n"
"v_mfma_f32_16x16x32_bf16 v[204:207], a[148:151], a[72:75], v[204:207]\n"
"ds_read_b128 a[124:127], v244 offset:8192\n"
"v_mfma_f32_16x16x32_bf16 v[204:207], a[152:155], a[68:71], v[204:207]\n"
"v_exp_f32 v195, v195\n"
"v_mfma_f32_16x16x32_bf16 v[204:207], a[156:159], a[64:67], v[204:207]\n"
"ds_read_b128 a[128:131], v244 offset:16384\n"
"v_cvt_pk_bf16_f32 v217, v194, v195\n"
"v_smfmac_f32_16x16x64_bf16 a[236:239], a[248:251], v[208:215], v252\n"
"v_exp_f32 v196, v196\n"
"v_mfma_f32_16x16x32_bf16 v[96:99], v[208:211], a[160:163], v[96:99]\n"
"ds_read_b128 a[132:135], v245 offset:16384\n"
"v_mfma_f32_16x16x32_bf16 v[96:99], v[212:215], a[164:167], v[96:99]\n"
"v_exp_f32 v197, v197\n"
"v_mfma_f32_16x16x32_bf16 v[100:103], v[212:215], a[168:171], v[100:103]\n"
"ds_read_b128 a[136:139], v244 offset:17408\n"
"v_cvt_pk_bf16_f32 v218, v196, v197\n"
"v_mfma_f32_16x16x32_bf16 v[100:103], v[208:211], a[172:175], v[100:103]\n"
"v_exp_f32 v198, v198\n"
"v_mfma_f32_16x16x32_bf16 v[104:107], v[208:211], a[176:179], v[104:107]\n"
"ds_read_b128 a[140:143], v245 offset:17408\n"
"ds_read_b128 a[144:147], v245 offset:25600\n"
"v_mfma_f32_16x16x32_bf16 v[104:107], v[212:215], a[180:183], v[104:107]\n"
"v_exp_f32 v199, v199\n"
"v_mfma_f32_16x16x32_bf16 v[108:111], v[212:215], a[184:187], v[108:111]\n"
"v_cvt_pk_bf16_f32 v219, v198, v199\n"
"s_xor_b32 s25, s25, 0x10000\n"
"v_mfma_f32_16x16x32_bf16 v[108:111], v[208:211], a[188:191], v[108:111]\n"
"v_exp_f32 v200, v200\n"
"v_mfma_f32_16x16x32_bf16 v[112:115], v[208:211], a[192:195], v[112:115]\n"
"s_mov_b32 m0, s25\n"
"s_add_u32 s24, s24, 8192\n"
"v_mfma_f32_16x16x32_bf16 v[112:115], v[212:215], a[196:199], v[112:115]\n"
"v_exp_f32 v201, v201\n"
"v_mfma_f32_16x16x32_bf16 v[116:119], v[212:215], a[200:203], v[116:119]\n"
"buffer_load_dwordx4 v248, s[36:39], s24 offen nt lds\n"
"v_mfma_f32_16x16x32_bf16 v[116:119], v[208:211], a[204:207], v[116:119]\n"
"v_exp_f32 v202, v202\n"
"v_mfma_f32_16x16x32_bf16 v[120:123], v[208:211], a[208:211], v[120:123]\n"
"ds_read_b128 a[148:151], v244 offset:25600\n"
"ds_read_b128 a[152:155], v245 offset:24576\n"
"ds_read_b128 a[156:159], v244 offset:24576\n"
"v_mfma_f32_16x16x32_bf16 v[120:123], v[212:215], a[212:215], v[120:123]\n"
"v_exp_f32 v203, v203\n"
"v_mfma_f32_16x16x32_bf16 v[124:127], v[212:215], a[216:219], v[124:127]\n"
"v_mfma_f32_16x16x32_bf16 v[124:127], v[208:211], a[220:223], v[124:127]\n"
"s_waitcnt lgkmcnt(8)\n"
"v_mfma_f32_16x16x32_bf16 v[192:195], a[96:99], a[0:3], v[220:223]\n"
"v_exp_f32 v204, v204\n"
"v_mfma_f32_16x16x32_bf16 v[192:195], a[100:103], a[4:7], v[192:195]\n"
"v_cvt_pk_bf16_f32 v212, v200, v201\n"
"v_cvt_pk_bf16_f32 v213, v202, v203\n"
"v_mfma_f32_16x16x32_bf16 v[192:195], a[104:107], a[8:11], v[192:195]\n"
"v_exp_f32 v205, v205\n"
"v_mfma_f32_16x16x32_bf16 v[192:195], a[108:111], a[12:15], v[192:195]\n"
"v_cvt_pk_bf16_f32 v214, v204, v205\n"
"v_mfma_f32_16x16x32_bf16 v[196:199], a[112:115], a[12:15], v[220:223]\n"
"v_exp_f32 v206, v206\n"
"v_mfma_f32_16x16x32_bf16 v[196:199], a[116:119], a[8:11], v[196:199]\n"
"buffer_load_dwordx4 v249, s[36:39], s24 offen offset:1024 nt lds\n"
"v_mfma_f32_16x16x32_bf16 v[196:199], a[120:123], a[4:7], v[196:199]\n"
"v_exp_f32 v207, v207\n"
"v_mfma_f32_16x16x32_bf16 v[196:199], a[124:127], a[0:3], v[196:199]\n"
"v_cvt_pk_bf16_f32 v215, v206, v207\n"
"s_waitcnt lgkmcnt(0)\n"
"v_mfma_f32_16x16x32_bf16 v[200:203], a[128:131], a[0:3], v[220:223]\n"
"v_exp_f32 v192, v192\n"
"v_mfma_f32_16x16x32_bf16 v[200:203], a[132:135], a[4:7], v[200:203]\n"
"v_xor_b32 v246, 0x10000, v246\n"
"v_xor_b32 v247, 0x10000, v247\n"
"v_mfma_f32_16x16x32_bf16 v[200:203], a[136:139], a[8:11], v[200:203]\n"
"v_exp_f32 v193, v193\n"
"v_mfma_f32_16x16x32_bf16 v[200:203], a[140:143], a[12:15], v[200:203]\n"
"v_cvt_pk_bf16_f32 v208, v192, v193\n"
"v_mfma_f32_16x16x32_bf16 v[204:207], a[144:147], a[12:15], v[220:223]\n"
"v_exp_f32 v194, v194\n"
"v_mfma_f32_16x16x32_bf16 v[204:207], a[148:151], a[8:11], v[204:207]\n"
"v_mfma_f32_16x16x32_bf16 v[204:207], a[152:155], a[4:7], v[204:207]\n"
"v_exp_f32 v195, v195\n"
"v_mfma_f32_16x16x32_bf16 v[204:207], a[156:159], a[0:3], v[204:207]\n"
"v_cvt_pk_bf16_f32 v209, v194, v195\n"
"v_smfmac_f32_16x16x64_bf16 a[240:243], a[248:251], v[212:219], v252\n"
"v_exp_f32 v196, v196\n"
"v_mfma_f32_16x16x32_bf16 v[128:131], v[216:219], a[160:163], v[128:131]\n"
"ds_read_b64_tr_b16 a[160:161], v246 offset:2048\n"
"ds_read_b64_tr_b16 a[162:163], v246 offset:10240\n"
"v_mfma_f32_16x16x32_bf16 v[128:131], v[212:215], a[164:167], v[128:131]\n"
"v_exp_f32 v197, v197\n"
"v_mfma_f32_16x16x32_bf16 v[132:135], v[212:215], a[168:171], v[132:135]\n"
"ds_read_b64_tr_b16 a[164:165], v246 offset:18432\n"
"v_cvt_pk_bf16_f32 v210, v196, v197\n"
"v_mfma_f32_16x16x32_bf16 v[132:135], v[216:219], a[172:175], v[132:135]\n"
"v_exp_f32 v198, v198\n"
"v_mfma_f32_16x16x32_bf16 v[136:139], v[216:219], a[176:179], v[136:139]\n"
"ds_read_b64_tr_b16 a[166:167], v246 offset:26624\n"
"ds_read_b64_tr_b16 a[168:169], v247 offset:18432\n"
"v_mfma_f32_16x16x32_bf16 v[136:139], v[212:215], a[180:183], v[136:139]\n"
"v_exp_f32 v199, v199\n"
"v_mfma_f32_16x16x32_bf16 v[140:143], v[212:215], a[184:187], v[140:143]\n"
"ds_read_b64_tr_b16 a[170:171], v247 offset:26624\n"
"v_cvt_pk_bf16_f32 v211, v198, v199\n"
"v_mfma_f32_16x16x32_bf16 v[140:143], v[216:219], a[188:191], v[140:143]\n"
"v_exp_f32 v200, v200\n"
"v_mfma_f32_16x16x32_bf16 v[144:147], v[216:219], a[192:195], v[144:147]\n"
"ds_read_b64_tr_b16 a[172:173], v247 offset:2048\n"
"ds_read_b64_tr_b16 a[174:175], v247 offset:10240\n"
"v_mfma_f32_16x16x32_bf16 v[144:147], v[212:215], a[196:199], v[144:147]\n"
"v_exp_f32 v201, v201\n"
"v_mfma_f32_16x16x32_bf16 v[148:151], v[212:215], a[200:203], v[148:151]\n"
"ds_read_b64_tr_b16 a[176:177], v246 offset:2560\n"
"ds_read_b64_tr_b16 a[178:179], v246 offset:10752\n"
"v_mfma_f32_16x16x32_bf16 v[148:151], v[216:219], a[204:207], v[148:151]\n"
"v_exp_f32 v202, v202\n"
"v_mfma_f32_16x16x32_bf16 v[152:155], v[216:219], a[208:211], v[152:155]\n"
"ds_read_b64_tr_b16 a[180:181], v246 offset:18944\n"
"ds_read_b64_tr_b16 a[182:183], v246 offset:27136\n"
"v_mfma_f32_16x16x32_bf16 v[152:155], v[212:215], a[212:215], v[152:155]\n"
"v_exp_f32 v203, v203\n"
"v_mfma_f32_16x16x32_bf16 v[156:159], v[212:215], a[216:219], v[156:159]\n"
"ds_read_b64_tr_b16 a[184:185], v247 offset:18944\n"
"ds_read_b64_tr_b16 a[186:187], v247 offset:27136\n"
"v_mfma_f32_16x16x32_bf16 v[156:159], v[216:219], a[220:223], v[156:159]\n"
"ds_read_b64_tr_b16 a[188:189], v247 offset:2560\n"
"ds_read_b64_tr_b16 a[190:191], v247 offset:10752\n"
"v_mfma_f32_16x16x32_bf16 v[192:195], a[96:99], a[16:19], v[224:227]\n"
"v_exp_f32 v204, v204\n"
"v_mfma_f32_16x16x32_bf16 v[192:195], a[100:103], a[20:23], v[192:195]\n"
"v_cvt_pk_bf16_f32 v212, v200, v201\n"
"v_cvt_pk_bf16_f32 v213, v202, v203\n"
"v_mfma_f32_16x16x32_bf16 v[192:195], a[104:107], a[24:27], v[192:195]\n"
"v_exp_f32 v205, v205\n"
"v_mfma_f32_16x16x32_bf16 v[192:195], a[108:111], a[28:31], v[192:195]\n"
"ds_read_b64_tr_b16 a[192:193], v246 offset:3072\n"
"v_cvt_pk_bf16_f32 v214, v204, v205\n"
"v_mfma_f32_16x16x32_bf16 v[196:199], a[112:115], a[28:31], v[224:227]\n"
"v_exp_f32 v206, v206\n"
"v_mfma_f32_16x16x32_bf16 v[196:199], a[116:119], a[24:27], v[196:199]\n"
"ds_read_b64_tr_b16 a[194:195], v246 offset:11264\n"
"ds_read_b64_tr_b16 a[196:197], v246 offset:19456\n"
"v_mfma_f32_16x16x32_bf16 v[196:199], a[120:123], a[20:23], v[196:199]\n"
"v_exp_f32 v207, v207\n"
"v_mfma_f32_16x16x32_bf16 v[196:199], a[124:127], a[16:19], v[196:199]\n"
"ds_read_b64_tr_b16 a[198:199], v246 offset:27648\n"
"v_cvt_pk_bf16_f32 v215, v206, v207\n"
"v_mfma_f32_16x16x32_bf16 v[200:203], a[128:131], a[16:19], v[224:227]\n"
"v_exp_f32 v192, v192\n"
"v_mfma_f32_16x16x32_bf16 v[200:203], a[132:135], a[20:23], v[200:203]\n"
"ds_read_b64_tr_b16 a[200:201], v247 offset:19456\n"
"ds_read_b64_tr_b16 a[202:203], v247 offset:27648\n"
"v_mfma_f32_16x16x32_bf16 v[200:203], a[136:139], a[24:27], v[200:203]\n"
"v_exp_f32 v193, v193\n"
"v_mfma_f32_16x16x32_bf16 v[200:203], a[140:143], a[28:31], v[200:203]\n"
"ds_read_b64_tr_b16 a[204:205], v247 offset:3072\n"
"v_cvt_pk_bf16_f32 v216, v192, v193\n"
"v_mfma_f32_16x16x32_bf16 v[204:207], a[144:147], a[28:31], v[224:227]\n"
"v_exp_f32 v194, v194\n"
"v_mfma_f32_16x16x32_bf16 v[204:207], a[148:151], a[24:27], v[204:207]\n"
"ds_read_b64_tr_b16 a[206:207], v247 offset:11264\n"
"ds_read_b64_tr_b16 a[208:209], v246 offset:3584\n"
"v_mfma_f32_16x16x32_bf16 v[204:207], a[152:155], a[20:23], v[204:207]\n"
"v_exp_f32 v195, v195\n"
"v_mfma_f32_16x16x32_bf16 v[204:207], a[156:159], a[16:19], v[204:207]\n"
"v_cvt_pk_bf16_f32 v217, v194, v195\n"
"s_waitcnt lgkmcnt(15)\n"
"v_smfmac_f32_16x16x64_bf16 a[224:227], a[248:251], v[208:215], v252\n"
"v_exp_f32 v196, v196\n"
"v_mfma_f32_16x16x32_bf16 v[0:3], v[208:211], a[160:163], v[0:3]\n"
"ds_read_b64_tr_b16 a[210:211], v246 offset:11776\n"
"ds_read_b64_tr_b16 a[212:213], v246 offset:19968\n"
"v_mfma_f32_16x16x32_bf16 v[0:3], v[212:215], a[164:167], v[0:3]\n"
"v_exp_f32 v197, v197\n"
"v_mfma_f32_16x16x32_bf16 v[4:7], v[212:215], a[168:171], v[4:7]\n"
"v_cvt_pk_bf16_f32 v218, v196, v197\n"
"s_waitcnt lgkmcnt(11)\n"
"v_mfma_f32_16x16x32_bf16 v[4:7], v[208:211], a[172:175], v[4:7]\n"
"v_exp_f32 v198, v198\n"
"v_mfma_f32_16x16x32_bf16 v[8:11], v[208:211], a[176:179], v[8:11]\n"
"ds_read_b64_tr_b16 a[214:215], v246 offset:28160\n"
"ds_read_b64_tr_b16 a[216:217], v247 offset:19968\n"
"ds_read_b64_tr_b16 a[218:219], v247 offset:28160\n"
"ds_read_b64_tr_b16 a[220:221], v247 offset:3584\n"
"ds_read_b64_tr_b16 a[222:223], v247 offset:11776\n"
"v_mfma_f32_16x16x32_bf16 v[8:11], v[212:215], a[180:183], v[8:11]\n"
"v_exp_f32 v199, v199\n"
"v_mfma_f32_16x16x32_bf16 v[12:15], v[212:215], a[184:187], v[12:15]\n"
"v_cvt_pk_bf16_f32 v219, v198, v199\n"
"s_waitcnt lgkmcnt(8)\n"
"v_mfma_f32_16x16x32_bf16 v[12:15], v[208:211], a[188:191], v[12:15]\n"
"v_exp_f32 v200, v200\n"
"v_mfma_f32_16x16x32_bf16 v[16:19], v[208:211], a[192:195], v[16:19]\n"
"buffer_load_dwordx4 v250, s[40:43], s24 offen offset:2048 nt lds\n"
"v_mfma_f32_16x16x32_bf16 v[16:19], v[212:215], a[196:199], v[16:19]\n"
"v_exp_f32 v201, v201\n"
"v_mfma_f32_16x16x32_bf16 v[20:23], v[212:215], a[200:203], v[20:23]\n"
"s_waitcnt lgkmcnt(0)\n"
"v_mfma_f32_16x16x32_bf16 v[20:23], v[208:211], a[204:207], v[20:23]\n"
"v_exp_f32 v202, v202\n"
"v_mfma_f32_16x16x32_bf16 v[24:27], v[208:211], a[208:211], v[24:27]\n"
"buffer_load_dwordx4 v251, s[40:43], s24 offen offset:3072 nt lds\n"
"v_mfma_f32_16x16x32_bf16 v[24:27], v[212:215], a[212:215], v[24:27]\n"
"v_exp_f32 v203, v203\n"
"v_mfma_f32_16x16x32_bf16 v[28:31], v[212:215], a[216:219], v[28:31]\n"
"v_mfma_f32_16x16x32_bf16 v[28:31], v[208:211], a[220:223], v[28:31]\n"
"v_mfma_f32_16x16x32_bf16 v[192:195], a[96:99], a[32:35], v[228:231]\n"
"v_exp_f32 v204, v204\n"
"v_mfma_f32_16x16x32_bf16 v[192:195], a[100:103], a[36:39], v[192:195]\n"
"v_cvt_pk_bf16_f32 v212, v200, v201\n"
"v_cvt_pk_bf16_f32 v213, v202, v203\n"
"v_mfma_f32_16x16x32_bf16 v[192:195], a[104:107], a[40:43], v[192:195]\n"
"v_exp_f32 v205, v205\n"
"v_mfma_f32_16x16x32_bf16 v[192:195], a[108:111], a[44:47], v[192:195]\n"
"v_cvt_pk_bf16_f32 v214, v204, v205\n"
"s_add_u32 m0, s25, 16384\n"
"s_add_u32 s24, s24, 8192\n"
"v_mfma_f32_16x16x32_bf16 v[196:199], a[112:115], a[44:47], v[228:231]\n"
"v_exp_f32 v206, v206\n"
"v_mfma_f32_16x16x32_bf16 v[196:199], a[116:119], a[40:43], v[196:199]\n"
"buffer_load_dwordx4 v248, s[36:39], s24 offen nt lds\n"
"v_mfma_f32_16x16x32_bf16 v[196:199], a[120:123], a[36:39], v[196:199]\n"
"v_exp_f32 v207, v207\n"
"v_mfma_f32_16x16x32_bf16 v[196:199], a[124:127], a[32:35], v[196:199]\n"
"v_cvt_pk_bf16_f32 v215, v206, v207\n"
"v_mfma_f32_16x16x32_bf16 v[200:203], a[128:131], a[32:35], v[228:231]\n"
"v_exp_f32 v192, v192\n"
"v_mfma_f32_16x16x32_bf16 v[200:203], a[132:135], a[36:39], v[200:203]\n"
"v_mfma_f32_16x16x32_bf16 v[200:203], a[136:139], a[40:43], v[200:203]\n"
"v_exp_f32 v193, v193\n"
"v_mfma_f32_16x16x32_bf16 v[200:203], a[140:143], a[44:47], v[200:203]\n"
"v_cvt_pk_bf16_f32 v208, v192, v193\n"
"v_mfma_f32_16x16x32_bf16 v[204:207], a[144:147], a[44:47], v[228:231]\n"
"v_exp_f32 v194, v194\n"
"v_mfma_f32_16x16x32_bf16 v[204:207], a[148:151], a[40:43], v[204:207]\n"
"v_mfma_f32_16x16x32_bf16 v[204:207], a[152:155], a[36:39], v[204:207]\n"
"v_exp_f32 v195, v195\n"
"v_mfma_f32_16x16x32_bf16 v[204:207], a[156:159], a[32:35], v[204:207]\n"
"v_cvt_pk_bf16_f32 v209, v194, v195\n"
"v_smfmac_f32_16x16x64_bf16 a[228:231], a[248:251], v[212:219], v252\n"
"v_exp_f32 v196, v196\n"
"v_mfma_f32_16x16x32_bf16 v[32:35], v[216:219], a[160:163], v[32:35]\n"
"v_mfma_f32_16x16x32_bf16 v[32:35], v[212:215], a[164:167], v[32:35]\n"
"v_exp_f32 v197, v197\n"
"v_mfma_f32_16x16x32_bf16 v[36:39], v[212:215], a[168:171], v[36:39]\n"
"v_cvt_pk_bf16_f32 v210, v196, v197\n"
"v_mfma_f32_16x16x32_bf16 v[36:39], v[216:219], a[172:175], v[36:39]\n"
"v_exp_f32 v198, v198\n"
"v_mfma_f32_16x16x32_bf16 v[40:43], v[216:219], a[176:179], v[40:43]\n"
"buffer_load_dwordx4 v249, s[36:39], s24 offen offset:1024 nt lds\n"
"v_mfma_f32_16x16x32_bf16 v[40:43], v[212:215], a[180:183], v[40:43]\n"
"v_exp_f32 v199, v199\n"
"v_mfma_f32_16x16x32_bf16 v[44:47], v[212:215], a[184:187], v[44:47]\n"
"v_cvt_pk_bf16_f32 v211, v198, v199\n"
"v_mfma_f32_16x16x32_bf16 v[44:47], v[216:219], a[188:191], v[44:47]\n"
"v_exp_f32 v200, v200\n"
"v_mfma_f32_16x16x32_bf16 v[48:51], v[216:219], a[192:195], v[48:51]\n"
"s_add_u32 s27, s27, 1\n"
"v_mov_b32 v255, s27\n"
"v_mfma_f32_16x16x32_bf16 v[48:51], v[212:215], a[196:199], v[48:51]\n"
"v_exp_f32 v201, v201\n"
"v_mfma_f32_16x16x32_bf16 v[52:55], v[212:215], a[200:203], v[52:55]\n"
"v_mfma_f32_16x16x32_bf16 v[52:55], v[216:219], a[204:207], v[52:55]\n"
"v_exp_f32 v202, v202\n"
"v_mfma_f32_16x16x32_bf16 v[56:59], v[216:219], a[208:211], v[56:59]\n"
"v_mfma_f32_16x16x32_bf16 v[56:59], v[212:215], a[212:215], v[56:59]\n"
"v_exp_f32 v203, v203\n"
"v_mfma_f32_16x16x32_bf16 v[60:63], v[212:215], a[216:219], v[60:63]\n"
"s_waitcnt vmcnt(6)\n"
"ds_write_b32 v253, v255\n"
"v_mfma_f32_16x16x32_bf16 v[60:63], v[216:219], a[220:223], v[60:63]\n"
"s_sub_u32 s26, s26, 1\n"
"s_cbranch_scc0 L_loop5_%=\n"
"v_mfma_f32_16x16x32_bf16 v[192:195], a[96:99], a[48:51], v[232:235]\n"
"v_exp_f32 v204, v204\n"
"v_mfma_f32_16x16x32_bf16 v[192:195], a[100:103], a[52:55], v[192:195]\n"
"v_cvt_pk_bf16_f32 v212, v200, v201\n"
"v_cvt_pk_bf16_f32 v213, v202, v203\n"
"v_mfma_f32_16x16x32_bf16 v[192:195], a[104:107], a[56:59], v[192:195]\n"
"v_exp_f32 v205, v205\n"
"v_mfma_f32_16x16x32_bf16 v[192:195], a[108:111], a[60:63], v[192:195]\n"
"v_cvt_pk_bf16_f32 v214, v204, v205\n"
"v_mfma_f32_16x16x32_bf16 v[196:199], a[112:115], a[60:63], v[232:235]\n"
"v_exp_f32 v206, v206\n"
"v_mfma_f32_16x16x32_bf16 v[196:199], a[116:119], a[56:59], v[196:199]\n"
"v_mfma_f32_16x16x32_bf16 v[196:199], a[120:123], a[52:55], v[196:199]\n"
"v_exp_f32 v207, v207\n"
"v_mfma_f32_16x16x32_bf16 v[196:199], a[124:127], a[48:51], v[196:199]\n"
"v_cvt_pk_bf16_f32 v215, v206, v207\n"
"v_mfma_f32_16x16x32_bf16 v[200:203], a[128:131], a[48:51], v[232:235]\n"
"v_exp_f32 v192, v192\n"
"v_mfma_f32_16x16x32_bf16 v[200:203], a[132:135], a[52:55], v[200:203]\n"
"buffer_load_dwordx4 v250, s[40:43], s24 offen offset:2048 nt lds\n"
"v_mfma_f32_16x16x32_bf16 v[200:203], a[136:139], a[56:59], v[200:203]\n"
"v_exp_f32 v193, v193\n"
"v_mfma_f32_16x16x32_bf16 v[200:203], a[140:143], a[60:63], v[200:203]\n"
"v_cvt_pk_bf16_f32 v216, v192, v193\n"
"v_mfma_f32_16x16x32_bf16 v[204:207], a[144:147], a[60:63], v[232:235]\n"
"v_exp_f32 v194, v194\n"
"v_mfma_f32_16x16x32_bf16 v[204:207], a[148:151], a[56:59], v[204:207]\n"
"v_mfma_f32_16x16x32_bf16 v[204:207], a[152:155], a[52:55], v[204:207]\n"
"v_exp_f32 v195, v195\n"
"v_mfma_f32_16x16x32_bf16 v[204:207], a[156:159], a[48:51], v[204:207]\n"
"v_cvt_pk_bf16_f32 v217, v194, v195\n"
"v_smfmac_f32_16x16x64_bf16 a[232:235], a[248:251], v[208:215], v252\n"
"v_exp_f32 v196, v196\n"
"v_mfma_f32_16x16x32_bf16 v[64:67], v[208:211], a[160:163], v[64:67]\n"
"v_mfma_f32_16x16x32_bf16 v[64:67], v[212:215], a[164:167], v[64:67]\n"
"v_exp_f32 v197, v197\n"
"v_mfma_f32_16x16x32_bf16 v[68:71], v[212:215], a[168:171], v[68:71]\n"
"v_cvt_pk_bf16_f32 v218, v196, v197\n"
"v_mfma_f32_16x16x32_bf16 v[68:71], v[208:211], a[172:175], v[68:71]\n"
"v_exp_f32 v198, v198\n"
"v_mfma_f32_16x16x32_bf16 v[72:75], v[208:211], a[176:179], v[72:75]\n"
"ds_read_b32 v255, v254\n"
"v_mfma_f32_16x16x32_bf16 v[72:75], v[212:215], a[180:183], v[72:75]\n"
"v_exp_f32 v199, v199\n"
"v_mfma_f32_16x16x32_bf16 v[76:79], v[212:215], a[184:187], v[76:79]\n"
"v_cvt_pk_bf16_f32 v219, v198, v199\n"
"v_mfma_f32_16x16x32_bf16 v[76:79], v[208:211], a[188:191], v[76:79]\n"
"v_exp_f32 v200, v200\n"
"v_mfma_f32_16x16x32_bf16 v[80:83], v[208:211], a[192:195], v[80:83]\n"
"v_mfma_f32_16x16x32_bf16 v[80:83], v[212:215], a[196:199], v[80:83]\n"
"v_exp_f32 v201, v201\n"
"v_mfma_f32_16x16x32_bf16 v[84:87], v[212:215], a[200:203], v[84:87]\n"
"v_mfma_f32_16x16x32_bf16 v[84:87], v[208:211], a[204:207], v[84:87]\n"
"v_exp_f32 v202, v202\n"
"v_mfma_f32_16x16x32_bf16 v[88:91], v[208:211], a[208:211], v[88:91]\n"
"buffer_load_dwordx4 v251, s[40:43], s24 offen offset:3072 nt lds\n"
"v_mfma_f32_16x16x32_bf16 v[88:91], v[212:215], a[212:215], v[88:91]\n"
"v_exp_f32 v203, v203\n"
"v_mfma_f32_16x16x32_bf16 v[92:95], v[212:215], a[216:219], v[92:95]\n"
"s_waitcnt lgkmcnt(0)\n"
"v_cmp_gt_u32 vcc, s27, v255\n"
"v_mfma_f32_16x16x32_bf16 v[92:95], v[208:211], a[220:223], v[92:95]\n"
"s_cbranch_vccnz L_late5_13_%=\n"
"L_back5_13_%=:\n"
"v_mfma_f32_16x16x32_bf16 v[192:195], a[96:99], a[64:67], v[236:239]\n"
"v_exp_f32 v204, v204\n"
"v_mfma_f32_16x16x32_bf16 v[192:195], a[100:103], a[68:71], v[192:195]\n"
"v_cvt_pk_bf16_f32 v212, v200, v201\n"
"v_cvt_pk_bf16_f32 v213, v202, v203\n"
"v_mfma_f32_16x16x32_bf16 v[192:195], a[104:107], a[72:75], v[192:195]\n"
"v_exp_f32 v205, v205\n"
"v_mfma_f32_16x16x32_bf16 v[192:195], a[108:111], a[76:79], v[192:195]\n"
"ds_read_b128 a[96:99], v244 offset:32768\n"
"v_cvt_pk_bf16_f32 v214, v204, v205\n"
"v_mfma_f32_16x16x32_bf16 v[196:199], a[112:115], a[76:79], v[236:239]\n"
"v_exp_f32 v206, v206\n"
"v_mfma_f32_16x16x32_bf16 v[196:199], a[116:119], a[72:75], v[196:199]\n"
"ds_read_b128 a[100:103], v245 offset:32768\n"
"ds_read_b128 a[104:107], v244 offset:33792\n"
"v_mfma_f32_16x16x32_bf16 v[196:199], a[120:123], a[68:71], v[196:199]\n"
"v_exp_f32 v207, v207\n"
"v_mfma_f32_16x16x32_bf16 v[196:199], a[124:127], a[64:67], v[196:199]\n"
"ds_read_b128 a[108:111], v245 offset:33792\n"
"v_cvt_pk_bf16_f32 v215, v206, v207\n"
"v_mfma_f32_16x16x32_bf16 v[200:203], a[128:131], a[64:67], v[236:239]\n"
"v_exp_f32 v192, v192\n"
"v_mfma_f32_16x16x32_bf16 v[200:203], a[132:135], a[68:71], v[200:203]\n"
"ds_read_b128 a[112:115], v245 offset:41984\n"
"ds_read_b128 a[116:119], v244 offset:41984\n"
"v_mfma_f32_16x16x32_bf16 v[200:203], a[136:139], a[72:75], v[200:203]\n"
"v_exp_f32 v193, v193\n"
"v_mfma_f32_16x16x32_bf16 v[200:203], a[140:143], a[76:79], v[200:203]\n"
"ds_read_b128 a[120:123], v245 offset:40960\n"
"v_cvt_pk_bf16_f32 v208, v192, v193\n"
"v_mfma_f32_16x16x32_bf16 v[204:207], a[144:147], a[76:79], v[236:239]\n"
"v_exp_f32 v194, v194\n"
"v_mfma_f32_16x16x32_bf16 v[204:207], a[148:151], a[72:75], v[204:207]\n"
"ds_read_b128 a[124:127], v244 offset:40960\n"
"v_mfma_f32_16x16x32_bf16 v[204:207], a[152:155], a[68:71], v[204:207]\n"
"v_exp_f32 v195, v195\n"
"v_mfma_f32_16x16x32_bf16 v[204:207], a[156:159], a[64:67], v[204:207]\n"
"ds_read_b128 a[128:131], v244 offset:49152\n"
"v_cvt_pk_bf16_f32 v209, v194, v195\n"
"v_smfmac_f32_16x16x64_bf16 a[236:239], a[248:251], v[212:219], v252\n"
"v_exp_f32 v196, v196\n"
"v_mfma_f32_16x16x32_bf16 v[96:99], v[216:219], a[160:163], v[96:99]\n"
"ds_read_b128 a[132:135], v245 offset:49152\n"
"v_mfma_f32_16x16x32_bf16 v[96:99], v[212:215], a[164:167], v[96:99]\n"
"v_exp_f32 v197, v197\n"
"v_mfma_f32_16x16x32_bf16 v[100:103], v[212:215], a[168:171], v[100:103]\n"
"ds_read_b128 a[136:139], v244 offset:50176\n"
"v_cvt_pk_bf16_f32 v210, v196, v197\n"
"v_mfma_f32_16x16x32_bf16 v[100:103], v[216:219], a[172:175], v[100:103]\n"
"v_exp_f32 v198, v198\n"
"v_mfma_f32_16x16x32_bf16 v[104:107], v[216:219], a[176:179], v[104:107]\n"
"ds_read_b128 a[140:143], v245 offset:50176\n"
"ds_read_b128 a[144:147], v245 offset:58368\n"
"v_mfma_f32_16x16x32_bf16 v[104:107], v[212:215], a[180:183], v[104:107]\n"
"v_exp_f32 v199, v199\n"
"v_mfma_f32_16x16x32_bf16 v[108:111], v[212:215], a[184:187], v[108:111]\n"
"v_cvt_pk_bf16_f32 v211, v198, v199\n"
"s_add_u32 m0, s25, 32768\n"
"v_mfma_f32_16x16x32_bf16 v[108:111], v[216:219], a[188:191], v[108:111]\n"
"v_exp_f32 v200, v200\n"
"v_mfma_f32_16x16x32_bf16 v[112:115], v[216:219], a[192:195], v[112:115]\n"
"ds_read_b128 a[148:151], v244 offset:58368\n"
"s_add_u32 s24, s24, 8192\n"
"v_mfma_f32_16x16x32_bf16 v[112:115], v[212:215], a[196:199], v[112:115]\n"
"v_exp_f32 v201, v201\n"
"v_mfma_f32_16x16x32_bf16 v[116:119], v[212:215], a[200:203], v[116:119]\n"
"buffer_load_dwordx4 v248, s[36:39], s24 offen nt lds\n"
"v_mfma_f32_16x16x32_bf16 v[116:119], v[216:219], a[204:207], v[116:119]\n"
"v_exp_f32 v202, v202\n"
"v_mfma_f32_16x16x32_bf16 v[120:123], v[216:219], a[208:211], v[120:123]\n"
"ds_read_b128 a[152:155], v245 offset:57344\n"
"ds_read_b128 a[156:159], v244 offset:57344\n"
"v_mfma_f32_16x16x32_bf16 v[120:123], v[212:215], a[212:215], v[120:123]\n"
"v_exp_f32 v203, v203\n"
"v_mfma_f32_16x16x32_bf16 v[124:127], v[212:215], a[216:219], v[124:127]\n"
"v_mfma_f32_16x16x32_bf16 v[124:127], v[216:219], a[220:223], v[124:127]\n"
"s_waitcnt lgkmcnt(8)\n"
"v_mfma_f32_16x16x32_bf16 v[192:195], a[96:99], a[0:3], v[220:223]\n"
"v_exp_f32 v204, v204\n"
"v_mfma_f32_16x16x32_bf16 v[192:195], a[100:103], a[4:7], v[192:195]\n"
"v_cvt_pk_bf16_f32 v212, v200, v201\n"
"v_cvt_pk_bf16_f32 v213, v202, v203\n"
"v_mfma_f32_16x16x32_bf16 v[192:195], a[104:107], a[8:11], v[192:195]\n"
"v_exp_f32 v205, v205\n"
"v_mfma_f32_16x16x32_bf16 v[192:195], a[108:111], a[12:15], v[192:195]\n"
"v_cvt_pk_bf16_f32 v214, v204, v205\n"
"v_mfma_f32_16x16x32_bf16 v[196:199], a[112:115], a[12:15], v[220:223]\n"
"v_exp_f32 v206, v206\n"
"v_mfma_f32_16x16x32_bf16 v[196:199], a[116:119], a[8:11], v[196:199]\n"
"buffer_load_dwordx4 v249, s[36:39], s24 offen offset:1024 nt lds\n"
"v_mfma_f32_16x16x32_bf16 v[196:199], a[120:123], a[4:7], v[196:199]\n"
"v_exp_f32 v207, v207\n"
"v_mfma_f32_16x16x32_bf16 v[196:199], a[124:127], a[0:3], v[196:199]\n"
"v_cvt_pk_bf16_f32 v215, v206, v207\n"
"s_waitcnt lgkmcnt(0)\n"
"v_mfma_f32_16x16x32_bf16 v[200:203], a[128:131], a[0:3], v[220:223]\n"
"v_exp_f32 v192, v192\n"
"v_mfma_f32_16x16x32_bf16 v[200:203], a[132:135], a[4:7], v[200:203]\n"
"v_mfma_f32_16x16x32_bf16 v[200:203], a[136:139], a[8:11], v[200:203]\n"
"v_exp_f32 v193, v193\n"
"v_mfma_f32_16x16x32_bf16 v[200:203], a[140:143], a[12:15], v[200:203]\n"
"v_cvt_pk_bf16_f32 v216, v192, v193\n"
"v_mfma_f32_16x16x32_bf16 v[204:207], a[144:147], a[12:15], v[220:223]\n"
"v_exp_f32 v194, v194\n"
"v_mfma_f32_16x16x32_bf16 v[204:207], a[148:151], a[8:11], v[204:207]\n"
"v_mfma_f32_16x16x32_bf16 v[204:207], a[152:155], a[4:7], v[204:207]\n"
"v_exp_f32 v195, v195\n"
"v_mfma_f32_16x16x32_bf16 v[204:207], a[156:159], a[0:3], v[204:207]\n"
"v_cvt_pk_bf16_f32 v217, v194, v195\n"
"v_smfmac_f32_16x16x64_bf16 a[240:243], a[248:251], v[208:215], v252\n"
"v_exp_f32 v196, v196\n"
"v_mfma_f32_16x16x32_bf16 v[128:131], v[208:211], a[160:163], v[128:131]\n"
"ds_read_b64_tr_b16 a[160:161], v246 offset:34816\n"
"ds_read_b64_tr_b16 a[162:163], v246 offset:43008\n"
"v_mfma_f32_16x16x32_bf16 v[128:131], v[212:215], a[164:167], v[128:131]\n"
"v_exp_f32 v197, v197\n"
"v_mfma_f32_16x16x32_bf16 v[132:135], v[212:215], a[168:171], v[132:135]\n"
"ds_read_b64_tr_b16 a[164:165], v246 offset:51200\n"
"v_cvt_pk_bf16_f32 v218, v196, v197\n"
"v_mfma_f32_16x16x32_bf16 v[132:135], v[208:211], a[172:175], v[132:135]\n"
"v_exp_f32 v198, v198\n"
"v_mfma_f32_16x16x32_bf16 v[136:139], v[208:211], a[176:179], v[136:139]\n"
"ds_read_b64_tr_b16 a[166:167], v246 offset:59392\n"
"ds_read_b64_tr_b16 a[168:169], v247 offset:51200\n"
"v_mfma_f32_16x16x32_bf16 v[136:139], v[212:215], a[180:183], v[136:139]\n"
"v_exp_f32 v199, v199\n"
"v_mfma_f32_16x16x32_bf16 v[140:143], v[212:215], a[184:187], v[140:143]\n"
"ds_read_b64_tr_b16 a[170:171], v247 offset:59392\n"
"v_cvt_pk_bf16_f32 v219, v198, v199\n"
"v_mfma_f32_16x16x32_bf16 v[140:143], v[208:211], a[188:191], v[140:143]\n"
"v_exp_f32 v200, v200\n"
"v_mfma_f32_16x16x32_bf16 v[144:147], v[208:211], a[192:195], v[144:147]\n"
"ds_read_b64_tr_b16 a[172:173], v247 offset:34816\n"
"ds_read_b64_tr_b16 a[174:175], v247 offset:43008\n"
"v_mfma_f32_16x16x32_bf16 v[144:147], v[212:215], a[196:199], v[144:147]\n"
"v_exp_f32 v201, v201\n"
"v_mfma_f32_16x16x32_bf16 v[148:151], v[212:215], a[200:203], v[148:151]\n"
"ds_read_b64_tr_b16 a[176:177], v246 offset:35328\n"
"ds_read_b64_tr_b16 a[178:179], v246 offset:43520\n"
"v_mfma_f32_16x16x32_bf16 v[148:151], v[208:211], a[204:207], v[148:151]\n"
"v_exp_f32 v202, v202\n"
"v_mfma_f32_16x16x32_bf16 v[152:155], v[208:211], a[208:211], v[152:155]\n"
"ds_read_b64_tr_b16 a[180:181], v246 offset:51712\n"
"ds_read_b64_tr_b16 a[182:183], v246 offset:59904\n"
"v_mfma_f32_16x16x32_bf16 v[152:155], v[212:215], a[212:215], v[152:155]\n"
"v_exp_f32 v203, v203\n"
"v_mfma_f32_16x16x32_bf16 v[156:159], v[212:215], a[216:219], v[156:159]\n"
"ds_read_b64_tr_b16 a[184:185], v247 offset:51712\n"
"ds_read_b64_tr_b16 a[186:187], v247 offset:59904\n"
"v_mfma_f32_16x16x32_bf16 v[156:159], v[208:211], a[220:223], v[156:159]\n"
"ds_read_b64_tr_b16 a[188:189], v247 offset:35328\n"
"ds_read_b64_tr_b16 a[190:191], v247 offset:43520\n"
"v_mfma_f32_16x16x32_bf16 v[192:195], a[96:99], a[16:19], v[224:227]\n"
"v_exp_f32 v204, v204\n"
"v_mfma_f32_16x16x32_bf16 v[192:195], a[100:103], a[20:23], v[192:195]\n"
"v_cvt_pk_bf16_f32 v212, v200, v201\n"
"v_cvt_pk_bf16_f32 v213, v202, v203\n"
"v_mfma_f32_16x16x32_bf16 v[192:195], a[104:107], a[24:27], v[192:195]\n"
"v_exp_f32 v205, v205\n"
"v_mfma_f32_16x16x32_bf16 v[192:195], a[108:111], a[28:31], v[192:195]\n"
"ds_read_b64_tr_b16 a[192:193], v246 offset:35840\n"
"v_cvt_pk_bf16_f32 v214, v204, v205\n"
"v_mfma_f32_16x16x32_bf16 v[196:199], a[112:115], a[28:31], v[224:227]\n"
"v_exp_f32 v206, v206\n"
"v_mfma_f32_16x16x32_bf16 v[196:199], a[116:119], a[24:27], v[196:199]\n"
"ds_read_b64_tr_b16 a[194:195], v246 offset:44032\n"
"ds_read_b64_tr_b16 a[196:197], v246 offset:52224\n"
"v_mfma_f32_16x16x32_bf16 v[196:199], a[120:123], a[20:23], v[196:199]\n"
"v_exp_f32 v207, v207\n"
"v_mfma_f32_16x16x32_bf16 v[196:199], a[124:127], a[16:19], v[196:199]\n"
"ds_read_b64_tr_b16 a[198:199], v246 offset:60416\n"
"v_cvt_pk_bf16_f32 v215, v206, v207\n"
"v_mfma_f32_16x16x32_bf16 v[200:203], a[128:131], a[16:19], v[224:227]\n"
"v_exp_f32 v192, v192\n"
"v_mfma_f32_16x16x32_bf16 v[200:203], a[132:135], a[20:23], v[200:203]\n"
"ds_read_b64_tr_b16 a[200:201], v247 offset:52224\n"
"ds_read_b64_tr_b16 a[202:203], v247 offset:60416\n"
"v_mfma_f32_16x16x32_bf16 v[200:203], a[136:139], a[24:27], v[200:203]\n"
"v_exp_f32 v193, v193\n"
"v_mfma_f32_16x16x32_bf16 v[200:203], a[140:143], a[28:31], v[200:203]\n"
"ds_read_b64_tr_b16 a[204:205], v247 offset:35840\n"
"v_cvt_pk_bf16_f32 v208, v192, v193\n"
"v_mfma_f32_16x16x32_bf16 v[204:207], a[144:147], a[28:31], v[224:227]\n"
"v_exp_f32 v194, v194\n"
"v_mfma_f32_16x16x32_bf16 v[204:207], a[148:151], a[24:27], v[204:207]\n"
"ds_read_b64_tr_b16 a[206:207], v247 offset:44032\n"
"ds_read_b64_tr_b16 a[208:209], v246 offset:36352\n"
"v_mfma_f32_16x16x32_bf16 v[204:207], a[152:155], a[20:23], v[204:207]\n"
"v_exp_f32 v195, v195\n"
"v_mfma_f32_16x16x32_bf16 v[204:207], a[156:159], a[16:19], v[204:207]\n"
"v_cvt_pk_bf16_f32 v209, v194, v195\n"
"s_waitcnt lgkmcnt(15)\n"
"v_smfmac_f32_16x16x64_bf16 a[224:227], a[248:251], v[212:219], v252\n"
"v_exp_f32 v196, v196\n"
"v_mfma_f32_16x16x32_bf16 v[0:3], v[216:219], a[160:163], v[0:3]\n"
"ds_read_b64_tr_b16 a[210:211], v246 offset:44544\n"
"ds_read_b64_tr_b16 a[212:213], v246 offset:52736\n"
"v_mfma_f32_16x16x32_bf16 v[0:3], v[212:215], a[164:167], v[0:3]\n"
"v_exp_f32 v197, v197\n"
"v_mfma_f32_16x16x32_bf16 v[4:7], v[212:215], a[168:171], v[4:7]\n"
"v_cvt_pk_bf16_f32 v210, v196, v197\n"
"s_waitcnt lgkmcnt(11)\n"
"v_mfma_f32_16x16x32_bf16 v[4:7], v[216:219], a[172:175], v[4:7]\n"
"v_exp_f32 v198, v198\n"
"v_mfma_f32_16x16x32_bf16 v[8:11], v[216:219], a[176:179], v[8:11]\n"
"ds_read_b64_tr_b16 a[214:215], v246 offset:60928\n"
"ds_read_b64_tr_b16 a[216:217], v247 offset:52736\n"
"ds_read_b64_tr_b16 a[218:219], v247 offset:60928\n"
"ds_read_b64_tr_b16 a[220:221], v247 offset:36352\n"
"ds_read_b64_tr_b16 a[222:223], v247 offset:44544\n"
"v_mfma_f32_16x16x32_bf16 v[8:11], v[212:215], a[180:183], v[8:11]\n"
"v_exp_f32 v199, v199\n"
"v_mfma_f32_16x16x32_bf16 v[12:15], v[212:215], a[184:187], v[12:15]\n"
"v_cvt_pk_bf16_f32 v211, v198, v199\n"
"s_waitcnt lgkmcnt(8)\n"
"v_mfma_f32_16x16x32_bf16 v[12:15], v[216:219], a[188:191], v[12:15]\n"
"v_exp_f32 v200, v200\n"
"v_mfma_f32_16x16x32_bf16 v[16:19], v[216:219], a[192:195], v[16:19]\n"
"buffer_load_dwordx4 v250, s[40:43], s24 offen offset:2048 nt lds\n"
"v_mfma_f32_16x16x32_bf16 v[16:19], v[212:215], a[196:199], v[16:19]\n"
"v_exp_f32 v201, v201\n"
"v_mfma_f32_16x16x32_bf16 v[20:23], v[212:215], a[200:203], v[20:23]\n"
"s_waitcnt lgkmcnt(0)\n"
"v_mfma_f32_16x16x32_bf16 v[20:23], v[216:219], a[204:207], v[20:23]\n"
"v_exp_f32 v202, v202\n"
"v_mfma_f32_16x16x32_bf16 v[24:27], v[216:219], a[208:211], v[24:27]\n"
"buffer_load_dwordx4 v251, s[40:43], s24 offen offset:3072 nt lds\n"
"v_mfma_f32_16x16x32_bf16 v[24:27], v[212:215], a[212:215], v[24:27]\n"
"v_exp_f32 v203, v203\n"
"v_mfma_f32_16x16x32_bf16 v[28:31], v[212:215], a[216:219], v[28:31]\n"
"v_mfma_f32_16x16x32_bf16 v[28:31], v[216:219], a[220:223], v[28:31]\n"
"v_mfma_f32_16x16x32_bf16 v[192:195], a[96:99], a[32:35], v[228:231]\n"
"v_exp_f32 v204, v204\n"
"v_mfma_f32_16x16x32_bf16 v[192:195], a[100:103], a[36:39], v[192:195]\n"
"v_cvt_pk_bf16_f32 v212, v200, v201\n"
"v_cvt_pk_bf16_f32 v213, v202, v203\n"
"v_mfma_f32_16x16x32_bf16 v[192:195], a[104:107], a[40:43], v[192:195]\n"
"v_exp_f32 v205, v205\n"
"v_mfma_f32_16x16x32_bf16 v[192:195], a[108:111], a[44:47], v[192:195]\n"
"v_cvt_pk_bf16_f32 v214, v204, v205\n"
"s_add_u32 m0, s25, 49152\n"
"s_add_u32 s24, s24, 8192\n"
"v_mfma_f32_16x16x32_bf16 v[196:199], a[112:115], a[44:47], v[228:231]\n"
"v_exp_f32 v206, v206\n"
"v_mfma_f32_16x16x32_bf16 v[196:199], a[116:119], a[40:43], v[196:199]\n"
"buffer_load_dwordx4 v248, s[36:39], s24 offen nt lds\n"
"v_mfma_f32_16x16x32_bf16 v[196:199], a[120:123], a[36:39], v[196:199]\n"
"v_exp_f32 v207, v207\n"
"v_mfma_f32_16x16x32_bf16 v[196:199], a[124:127], a[32:35], v[196:199]\n"
"v_cvt_pk_bf16_f32 v215, v206, v207\n"
"v_mfma_f32_16x16x32_bf16 v[200:203], a[128:131], a[32:35], v[228:231]\n"
"v_exp_f32 v192, v192\n"
"v_mfma_f32_16x16x32_bf16 v[200:203], a[132:135], a[36:39], v[200:203]\n"
"v_mfma_f32_16x16x32_bf16 v[200:203], a[136:139], a[40:43], v[200:203]\n"
"v_exp_f32 v193, v193\n"
"v_mfma_f32_16x16x32_bf16 v[200:203], a[140:143], a[44:47], v[200:203]\n"
"v_cvt_pk_bf16_f32 v216, v192, v193\n"
"v_mfma_f32_16x16x32_bf16 v[204:207], a[144:147], a[44:47], v[228:231]\n"
"v_exp_f32 v194, v194\n"
"v_mfma_f32_16x16x32_bf16 v[204:207], a[148:151], a[40:43], v[204:207]\n"
"v_mfma_f32_16x16x32_bf16 v[204:207], a[152:155], a[36:39], v[204:207]\n"
"v_exp_f32 v195, v195\n"
"v_mfma_f32_16x16x32_bf16 v[204:207], a[156:159], a[32:35], v[204:207]\n"
"v_cvt_pk_bf16_f32 v217, v194, v195\n"
"v_smfmac_f32_16x16x64_bf16 a[228:231], a[248:251], v[208:215], v252\n"
"v_exp_f32 v196, v196\n"
"v_mfma_f32_16x16x32_bf16 v[32:35], v[208:211], a[160:163], v[32:35]\n"
"v_mfma_f32_16x16x32_bf16 v[32:35], v[212:215], a[164:167], v[32:35]\n"
"v_exp_f32 v197, v197\n"
"v_mfma_f32_16x16x32_bf16 v[36:39], v[212:215], a[168:171], v[36:39]\n"
"v_cvt_pk_bf16_f32 v218, v196, v197\n"
"v_mfma_f32_16x16x32_bf16 v[36:39], v[208:211], a[172:175], v[36:39]\n"
"v_exp_f32 v198, v198\n"
"v_mfma_f32_16x16x32_bf16 v[40:43], v[208:211], a[176:179], v[40:43]\n"
"buffer_load_dwordx4 v249, s[36:39], s24 offen offset:1024 nt lds\n"
"v_mfma_f32_16x16x32_bf16 v[40:43], v[212:215], a[180:183], v[40:43]\n"
"v_exp_f32 v199, v199\n"
"v_mfma_f32_16x16x32_bf16 v[44:47], v[212:215], a[184:187], v[44:47]\n"
"v_cvt_pk_bf16_f32 v219, v198, v199\n"
"v_mfma_f32_16x16x32_bf16 v[44:47], v[208:211], a[188:191], v[44:47]\n"
"v_exp_f32 v200, v200\n"
"v_mfma_f32_16x16x32_bf16 v[48:51], v[208:211], a[192:195], v[48:51]\n"
"s_add_u32 s27, s27, 1\n"
"v_mov_b32 v255, s27\n"
"v_mfma_f32_16x16x32_bf16 v[48:51], v[212:215], a[196:199], v[48:51]\n"
"v_exp_f32 v201, v201\n"
"v_mfma_f32_16x16x32_bf16 v[52:55], v[212:215], a[200:203], v[52:55]\n"
"v_mfma_f32_16x16x32_bf16 v[52:55], v[208:211], a[204:207], v[52:55]\n"
"v_exp_f32 v202, v202\n"
"v_mfma_f32_16x16x32_bf16 v[56:59], v[208:211], a[208:211], v[56:59]\n"
"v_mfma_f32_16x16x32_bf16 v[56:59], v[212:215], a[212:215], v[56:59]\n"
"v_exp_f32 v203, v203\n"
"v_mfma_f32_16x16x32_bf16 v[60:63], v[212:215], a[216:219], v[60:63]\n"
"s_waitcnt vmcnt(6)\n"
"ds_write_b32 v253, v255\n"
"v_mfma_f32_16x16x32_bf16 v[60:63], v[208:211], a[220:223], v[60:63]\n"
"v_mfma_f32_16x16x32_bf16 v[192:195], a[96:99], a[48:51], v[232:235]\n"
"v_exp_f32 v204, v204\n"
"v_mfma_f32_16x16x32_bf16 v[192:195], a[100:103], a[52:55], v[192:195]\n"
"v_cvt_pk_bf16_f32 v212, v200, v201\n"
"v_cvt_pk_bf16_f32 v213, v202, v203\n"
"v_mfma_f32_16x16x32_bf16 v[192:195], a[104:107], a[56:59], v[192:195]\n"
"v_exp_f32 v205, v205\n"
"v_mfma_f32_16x16x32_bf16 v[192:195], a[108:111], a[60:63], v[192:195]\n"
"v_cvt_pk_bf16_f32 v214, v204, v205\n"
"v_mfma_f32_16x16x32_bf16 v[196:199], a[112:115], a[60:63], v[232:235]\n"
"v_exp_f32 v206, v206\n"
"v_mfma_f32_16x16x32_bf16 v[196:199], a[116:119], a[56:59], v[196:199]\n"
"v_mfma_f32_16x16x32_bf16 v[196:199], a[120:123], a[52:55], v[196:199]\n"
"v_exp_f32 v207, v207\n"
"v_mfma_f32_16x16x32_bf16 v[196:199], a[124:127], a[48:51], v[196:199]\n"
"v_cvt_pk_bf16_f32 v215, v206, v207\n"
"v_mfma_f32_16x16x32_bf16 v[200:203], a[128:131], a[48:51], v[232:235]\n"
"v_exp_f32 v192, v192\n"
"v_mfma_f32_16x16x32_bf16 v[200:203], a[132:135], a[52:55], v[200:203]\n"
"buffer_load_dwordx4 v250, s[40:43], s24 offen offset:2048 nt lds\n"
"v_mfma_f32_16x16x32_bf16 v[200:203], a[136:139], a[56:59], v[200:203]\n"
"v_exp_f32 v193, v193\n"
"v_mfma_f32_16x16x32_bf16 v[200:203], a[140:143], a[60:63], v[200:203]\n"
"v_cvt_pk_bf16_f32 v208, v192, v193\n"
"v_xor_b32 v244, 0x10000, v244\n"
"v_mfma_f32_16x16x32_bf16 v[204:207], a[144:147], a[60:63], v[232:235]\n"
"v_exp_f32 v194, v194\n"
"v_mfma_f32_16x16x32_bf16 v[204:207], a[148:151], a[56:59], v[204:207]\n"
"v_xor_b32 v245, 0x10000, v245\n"
"v_mfma_f32_16x16x32_bf16 v[204:207], a[152:155], a[52:55], v[204:207]\n"
"v_exp_f32 v195, v195\n"
"v_mfma_f32_16x16x32_bf16 v[204:207], a[156:159], a[48:51], v[204:207]\n"
"v_cvt_pk_bf16_f32 v209, v194, v195\n"
"v_smfmac_f32_16x16x64_bf16 a[232:235], a[248:251], v[212:219], v252\n"
"v_exp_f32 v196, v196\n"
"v_mfma_f32_16x16x32_bf16 v[64:67], v[216:219], a[160:163], v[64:67]\n"
"v_mfma_f32_16x16x32_bf16 v[64:67], v[212:215], a[164:167], v[64:67]\n"
"v_exp_f32 v197, v197\n"
"v_mfma_f32_16x16x32_bf16 v[68:71], v[212:215], a[168:171], v[68:71]\n"
"v_cvt_pk_bf16_f32 v210, v196, v197\n"
"v_mfma_f32_16x16x32_bf16 v[68:71], v[216:219], a[172:175], v[68:71]\n"
"v_exp_f32 v198, v198\n"
"v_mfma_f32_16x16x32_bf16 v[72:75], v[216:219], a[176:179], v[72:75]\n"
"ds_read_b32 v255, v254\n"
"v_mfma_f32_16x16x32_bf16 v[72:75], v[212:215], a[180:183], v[72:75]\n"
"v_exp_f32 v199, v199\n"
"v_mfma_f32_16x16x32_bf16 v[76:79], v[212:215], a[184:187], v[76:79]\n"
"v_cvt_pk_bf16_f32 v211, v198, v199\n"
"v_mfma_f32_16x16x32_bf16 v[76:79], v[216:219], a[188:191], v[76:79]\n"
"v_exp_f32 v200, v200\n"
"v_mfma_f32_16x16x32_bf16 v[80:83], v[216:219], a[192:195], v[80:83]\n"
"v_mfma_f32_16x16x32_bf16 v[80:83], v[212:215], a[196:199], v[80:83]\n"
"v_exp_f32 v201, v201\n"
"v_mfma_f32_16x16x32_bf16 v[84:87], v[212:215], a[200:203], v[84:87]\n"
"v_mfma_f32_16x16x32_bf16 v[84:87], v[216:219], a[204:207], v[84:87]\n"
"v_exp_f32 v202, v202\n"
"v_mfma_f32_16x16x32_bf16 v[88:91], v[216:219], a[208:211], v[88:91]\n"
"buffer_load_dwordx4 v251, s[40:43], s24 offen offset:3072 nt lds\n"
"v_mfma_f32_16x16x32_bf16 v[88:91], v[212:215], a[212:215], v[88:91]\n"
"v_exp_f32 v203, v203\n"
"v_mfma_f32_16x16x32_bf16 v[92:95], v[212:215], a[216:219], v[92:95]\n"
"s_waitcnt lgkmcnt(0)\n"
"v_cmp_gt_u32 vcc, s27, v255\n"
"v_mfma_f32_16x16x32_bf16 v[92:95], v[216:219], a[220:223], v[92:95]\n"
"s_cbranch_vccnz L_late5_14_%=\n"
"L_back5_14_%=:\n"
"v_mfma_f32_16x16x32_bf16 v[192:195], a[96:99], a[64:67], v[236:239]\n"
"v_exp_f32 v204, v204\n"
"v_mfma_f32_16x16x32_bf16 v[192:195], a[100:103], a[68:71], v[192:195]\n"
"v_cvt_pk_bf16_f32 v212, v200, v201\n"
"v_cvt_pk_bf16_f32 v213, v202, v203\n"
"v_mfma_f32_16x16x32_bf16 v[192:195], a[104:107], a[72:75], v[192:195]\n"
"v_exp_f32 v205, v205\n"
"v_mfma_f32_16x16x32_bf16 v[192:195], a[108:111], a[76:79], v[192:195]\n"
"ds_read_b128 a[96:99], v244 offset:0\n"
"v_cvt_pk_bf16_f32 v214, v204, v205\n"
"v_mfma_f32_16x16x32_bf16 v[196:199], a[112:115], a[76:79], v[236:239]\n"
"v_exp_f32 v206, v206\n"
"v_mfma_f32_16x16x32_bf16 v[196:199], a[116:119], a[72:75], v[196:199]\n"
"ds_read_b128 a[100:103], v245 offset:0\n"
"ds_read_b128 a[104:107], v244 offset:1024\n"
"v_mfma_f32_16x16x32_bf16 v[196:199], a[120:123], a[68:71], v[196:199]\n"
"v_exp_f32 v207, v207\n"
"v_mfma_f32_16x16x32_bf16 v[196:199], a[124:127], a[64:67], v[196:199]\n"
"ds_read_b128 a[108:111], v245 offset:1024\n"
"v_cvt_pk_bf16_f32 v215, v206, v207\n"
"v_mfma_f32_16x16x32_bf16 v[200:203], a[128:131], a[64:67], v[236:239]\n"
"v_exp_f32 v192, v192\n"
"v_mfma_f32_16x16x32_bf16 v[200:203], a[132:135], a[68:71], v[200:203]\n"
"ds_read_b128 a[112:115], v245 offset:9216\n"
"ds_read_b128 a[116:119], v244 offset:9216\n"
"v_mfma_f32_16x16x32_bf16 v[200:203], a[136:139], a[72:75], v[200:203]\n"
"v_exp_f32 v193, v193\n"
"v_mfma_f32_16x16x32_bf16 v[200:203], a[140:143], a[76:79], v[200:203]\n"
"ds_read_b128 a[120:123], v245 offset:8192\n"
"v_cvt_pk_bf16_f32 v216, v192, v193\n"
"v_mfma_f32_16x16x32_bf16 v[204:207], a[144:147], a[76:79], v[236:239]\n"
"v_exp_f32 v194, v194\n"
"v_mfma_f32_16x16x32_bf16 v[204:207], a[148:151], a[72:75], v[204:207]\n"
"ds_read_b128 a[124:127], v244 offset:8192\n"
"v_mfma_f32_16x16x32_bf16 v[204:207], a[152:155], a[68:71], v[204:207]\n"
"v_exp_f32 v195, v195\n"
"v_mfma_f32_16x16x32_bf16 v[204:207], a[156:159], a[64:67], v[204:207]\n"
"ds_read_b128 a[128:131], v244 offset:16384\n"
"v_cvt_pk_bf16_f32 v217, v194, v195\n"
"v_smfmac_f32_16x16x64_bf16 a[236:239], a[248:251], v[208:215], v252\n"
"v_exp_f32 v196, v196\n"
"v_mfma_f32_16x16x32_bf16 v[96:99], v[208:211], a[160:163], v[96:99]\n"
"ds_read_b128 a[132:135], v245 offset:16384\n"
"v_mfma_f32_16x16x32_bf16 v[96:99], v[212:215], a[164:167], v[96:99]\n"
"v_exp_f32 v197, v197\n"
"v_mfma_f32_16x16x32_bf16 v[100:103], v[212:215], a[168:171], v[100:103]\n"
"ds_read_b128 a[136:139], v244 offset:17408\n"
"v_cvt_pk_bf16_f32 v218, v196, v197\n"
"v_mfma_f32_16x16x32_bf16 v[100:103], v[208:211], a[172:175], v[100:103]\n"
"v_exp_f32 v198, v198\n"
"v_mfma_f32_16x16x32_bf16 v[104:107], v[208:211], a[176:179], v[104:107]\n"
"ds_read_b128 a[140:143], v245 offset:17408\n"
"ds_read_b128 a[144:147], v245 offset:25600\n"
"v_mfma_f32_16x16x32_bf16 v[104:107], v[212:215], a[180:183], v[104:107]\n"
"v_exp_f32 v199, v199\n"
"v_mfma_f32_16x16x32_bf16 v[108:111], v[212:215], a[184:187], v[108:111]\n"
"ds_read_b128 a[148:151], v244 offset:25600\n"
"v_cvt_pk_bf16_f32 v219, v198, v199\n"
"v_mfma_f32_16x16x32_bf16 v[108:111], v[208:211], a[188:191], v[108:111]\n"
"v_exp_f32 v200, v200\n"
"v_mfma_f32_16x16x32_bf16 v[112:115], v[208:211], a[192:195], v[112:115]\n"
"ds_read_b128 a[152:155], v245 offset:24576\n"
"v_mfma_f32_16x16x32_bf16 v[112:115], v[212:215], a[196:199], v[112:115]\n"
"v_exp_f32 v201, v201\n"
"v_mfma_f32_16x16x32_bf16 v[116:119], v[212:215], a[200:203], v[116:119]\n"
"ds_read_b128 a[156:159], v244 offset:24576\n"
"v_mfma_f32_16x16x32_bf16 v[116:119], v[208:211], a[204:207], v[116:119]\n"
"v_exp_f32 v202, v202\n"
"v_mfma_f32_16x16x32_bf16 v[120:123], v[208:211], a[208:211], v[120:123]\n"
"v_mfma_f32_16x16x32_bf16 v[120:123], v[212:215], a[212:215], v[120:123]\n"
"v_exp_f32 v203, v203\n"
"v_mfma_f32_16x16x32_bf16 v[124:127], v[212:215], a[216:219], v[124:127]\n"
"v_mfma_f32_16x16x32_bf16 v[124:127], v[208:211], a[220:223], v[124:127]\n"
"s_waitcnt lgkmcnt(8)\n"
"v_mfma_f32_16x16x32_bf16 v[192:195], a[96:99], a[0:3], v[220:223]\n"
"v_exp_f32 v204, v204\n"
"v_mfma_f32_16x16x32_bf16 v[192:195], a[100:103], a[4:7], v[192:195]\n"
"v_cvt_pk_bf16_f32 v212, v200, v201\n"
"v_cvt_pk_bf16_f32 v213, v202, v203\n"
"v_mfma_f32_16x16x32_bf16 v[192:195], a[104:107], a[8:11], v[192:195]\n"
"v_exp_f32 v205, v205\n"
"v_mfma_f32_16x16x32_bf16 v[192:195], a[108:111], a[12:15], v[192:195]\n"
"v_cvt_pk_bf16_f32 v214, v204, v205\n"
"v_mfma_f32_16x16x32_bf16 v[196:199], a[112:115], a[12:15], v[220:223]\n"
"v_exp_f32 v206, v206\n"
"v_mfma_f32_16x16x32_bf16 v[196:199], a[116:119], a[8:11], v[196:199]\n"
"v_mfma_f32_16x16x32_bf16 v[196:199], a[120:123], a[4:7], v[196:199]\n"
"v_exp_f32 v207, v207\n"
"v_mfma_f32_16x16x32_bf16 v[196:199], a[124:127], a[0:3], v[196:199]\n"
"v_cvt_pk_bf16_f32 v215, v206, v207\n"
"s_waitcnt lgkmcnt(0)\n"
"v_mfma_f32_16x16x32_bf16 v[200:203], a[128:131], a[0:3], v[220:223]\n"
"v_exp_f32 v192, v192\n"
"v_mfma_f32_16x16x32_bf16 v[200:203], a[132:135], a[4:7], v[200:203]\n"
"v_xor_b32 v246, 0x10000, v246\n"
"v_xor_b32 v247, 0x10000, v247\n"
"v_mfma_f32_16x16x32_bf16 v[200:203], a[136:139], a[8:11], v[200:203]\n"
"v_exp_f32 v193, v193\n"
"v_mfma_f32_16x16x32_bf16 v[200:203], a[140:143], a[12:15], v[200:203]\n"
"v_cvt_pk_bf16_f32 v208, v192, v193\n"
"v_mfma_f32_16x16x32_bf16 v[204:207], a[144:147], a[12:15], v[220:223]\n"
"v_exp_f32 v194, v194\n"
"v_mfma_f32_16x16x32_bf16 v[204:207], a[148:151], a[8:11], v[204:207]\n"
"v_mfma_f32_16x16x32_bf16 v[204:207], a[152:155], a[4:7], v[204:207]\n"
"v_exp_f32 v195, v195\n"
"v_mfma_f32_16x16x32_bf16 v[204:207], a[156:159], a[0:3], v[204:207]\n"
"v_cvt_pk_bf16_f32 v209, v194, v195\n"
"v_smfmac_f32_16x16x64_bf16 a[240:243], a[248:251], v[212:219], v252\n"
"v_exp_f32 v196, v196\n"
"v_mfma_f32_16x16x32_bf16 v[128:131], v[216:219], a[160:163], v[128:131]\n"
"ds_read_b64_tr_b16 a[160:161], v246 offset:2048\n"
"ds_read_b64_tr_b16 a[162:163], v246 offset:10240\n"
"v_mfma_f32_16x16x32_bf16 v[128:131], v[212:215], a[164:167], v[128:131]\n"
"v_exp_f32 v197, v197\n"
"v_mfma_f32_16x16x32_bf16 v[132:135], v[212:215], a[168:171], v[132:135]\n"
"ds_read_b64_tr_b16 a[164:165], v246 offset:18432\n"
"v_cvt_pk_bf16_f32 v210, v196, v197\n"
"v_mfma_f32_16x16x32_bf16 v[132:135], v[216:219], a[172:175], v[132:135]\n"
"v_exp_f32 v198, v198\n"
"v_mfma_f32_16x16x32_bf16 v[136:139], v[216:219], a[176:179], v[136:139]\n"
"ds_read_b64_tr_b16 a[166:167], v246 offset:26624\n"
"ds_read_b64_tr_b16 a[168:169], v247 offset:18432\n"
"v_mfma_f32_16x16x32_bf16 v[136:139], v[212:215], a[180:183], v[136:139]\n"
"v_exp_f32 v199, v199\n"
"v_mfma_f32_16x16x32_bf16 v[140:143], v[212:215], a[184:187], v[140:143]\n"
"ds_read_b64_tr_b16 a[170:171], v247 offset:26624\n"
"v_cvt_pk_bf16_f32 v211, v198, v199\n"
"v_mfma_f32_16x16x32_bf16 v[140:143], v[216:219], a[188:191], v[140:143]\n"
"v_exp_f32 v200, v200\n"
"v_mfma_f32_16x16x32_bf16 v[144:147], v[216:219], a[192:195], v[144:147]\n"
"ds_read_b64_tr_b16 a[172:173], v247 offset:2048\n"
"ds_read_b64_tr_b16 a[174:175], v247 offset:10240\n"
"v_mfma_f32_16x16x32_bf16 v[144:147], v[212:215], a[196:199], v[144:147]\n"
"v_exp_f32 v201, v201\n"
"v_mfma_f32_16x16x32_bf16 v[148:151], v[212:215], a[200:203], v[148:151]\n"
"ds_read_b64_tr_b16 a[176:177], v246 offset:2560\n"
"ds_read_b64_tr_b16 a[178:179], v246 offset:10752\n"
"v_mfma_f32_16x16x32_bf16 v[148:151], v[216:219], a[204:207], v[148:151]\n"
"v_exp_f32 v202, v202\n"
"v_mfma_f32_16x16x32_bf16 v[152:155], v[216:219], a[208:211], v[152:155]\n"
"ds_read_b64_tr_b16 a[180:181], v246 offset:18944\n"
"ds_read_b64_tr_b16 a[182:183], v246 offset:27136\n"
"v_mfma_f32_16x16x32_bf16 v[152:155], v[212:215], a[212:215], v[152:155]\n"
"v_exp_f32 v203, v203\n"
"v_mfma_f32_16x16x32_bf16 v[156:159], v[212:215], a[216:219], v[156:159]\n"
"ds_read_b64_tr_b16 a[184:185], v247 offset:18944\n"
"ds_read_b64_tr_b16 a[186:187], v247 offset:27136\n"
"v_mfma_f32_16x16x32_bf16 v[156:159], v[216:219], a[220:223], v[156:159]\n"
"ds_read_b64_tr_b16 a[188:189], v247 offset:2560\n"
"ds_read_b64_tr_b16 a[190:191], v247 offset:10752\n"
"v_mfma_f32_16x16x32_bf16 v[192:195], a[96:99], a[16:19], v[224:227]\n"
"v_exp_f32 v204, v204\n"
"v_mfma_f32_16x16x32_bf16 v[192:195], a[100:103], a[20:23], v[192:195]\n"
"v_cvt_pk_bf16_f32 v212, v200, v201\n"
"v_cvt_pk_bf16_f32 v213, v202, v203\n"
"v_mfma_f32_16x16x32_bf16 v[192:195], a[104:107], a[24:27], v[192:195]\n"
"v_exp_f32 v205, v205\n"
"v_mfma_f32_16x16x32_bf16 v[192:195], a[108:111], a[28:31], v[192:195]\n"
"ds_read_b64_tr_b16 a[192:193], v246 offset:3072\n"
"v_cvt_pk_bf16_f32 v214, v204, v205\n"
"v_mfma_f32_16x16x32_bf16 v[196:199], a[112:115], a[28:31], v[224:227]\n"
"v_exp_f32 v206, v206\n"
"v_mfma_f32_16x16x32_bf16 v[196:199], a[116:119], a[24:27], v[196:199]\n"
"ds_read_b64_tr_b16 a[194:195], v246 offset:11264\n"
"ds_read_b64_tr_b16 a[196:197], v246 offset:19456\n"
"v_mfma_f32_16x16x32_bf16 v[196:199], a[120:123], a[20:23], v[196:199]\n"
"v_exp_f32 v207, v207\n"
"v_mfma_f32_16x16x32_bf16 v[196:199], a[124:127], a[16:19], v[196:199]\n"
"ds_read_b64_tr_b16 a[198:199], v246 offset:27648\n"
"v_cvt_pk_bf16_f32 v215, v206, v207\n"
"v_mfma_f32_16x16x32_bf16 v[200:203], a[128:131], a[16:19], v[224:227]\n"
"v_exp_f32 v192, v192\n"
"v_mfma_f32_16x16x32_bf16 v[200:203], a[132:135], a[20:23], v[200:203]\n"
"ds_read_b64_tr_b16 a[200:201], v247 offset:19456\n"
"ds_read_b64_tr_b16 a[202:203], v247 offset:27648\n"
"v_mfma_f32_16x16x32_bf16 v[200:203], a[136:139], a[24:27], v[200:203]\n"
"v_exp_f32 v193, v193\n"
"v_mfma_f32_16x16x32_bf16 v[200:203], a[140:143], a[28:31], v[200:203]\n"
"ds_read_b64_tr_b16 a[204:205], v247 offset:3072\n"
"v_cvt_pk_bf16_f32 v216, v192, v193\n"
"v_mfma_f32_16x16x32_bf16 v[204:207], a[144:147], a[28:31], v[224:227]\n"
"v_exp_f32 v194, v194\n"
"v_mfma_f32_16x16x32_bf16 v[204:207], a[148:151], a[24:27], v[204:207]\n"
"ds_read_b64_tr_b16 a[206:207], v247 offset:11264\n"
"ds_read_b64_tr_b16 a[208:209], v246 offset:3584\n"
"v_mfma_f32_16x16x32_bf16 v[204:207], a[152:155], a[20:23], v[204:207]\n"
"v_exp_f32 v195, v195\n"
"v_mfma_f32_16x16x32_bf16 v[204:207], a[156:159], a[16:19], v[204:207]\n"
"v_cvt_pk_bf16_f32 v217, v194, v195\n"
"s_waitcnt lgkmcnt(15)\n"
"v_smfmac_f32_16x16x64_bf16 a[224:227], a[248:251], v[208:215], v252\n"
"v_exp_f32 v196, v196\n"
"v_mfma_f32_16x16x32_bf16 v[0:3], v[208:211], a[160:163], v[0:3]\n"
"ds_read_b64_tr_b16 a[210:211], v246 offset:11776\n"
"ds_read_b64_tr_b16 a[212:213], v246 offset:19968\n"
"v_mfma_f32_16x16x32_bf16 v[0:3], v[212:215], a[164:167], v[0:3]\n"
"v_exp_f32 v197, v197\n"
"v_mfma_f32_16x16x32_bf16 v[4:7], v[212:215], a[168:171], v[4:7]\n"
"v_cvt_pk_bf16_f32 v218, v196, v197\n"
"s_waitcnt lgkmcnt(11)\n"
"v_mfma_f32_16x16x32_bf16 v[4:7], v[208:211], a[172:175], v[4:7]\n"
"v_exp_f32 v198, v198\n"
"v_mfma_f32_16x16x32_bf16 v[8:11], v[208:211], a[176:179], v[8:11]\n"
"ds_read_b64_tr_b16 a[214:215], v246 offset:28160\n"
"ds_read_b64_tr_b16 a[216:217], v247 offset:19968\n"
"ds_read_b64_tr_b16 a[218:219], v247 offset:28160\n"
"ds_read_b64_tr_b16 a[220:221], v247 offset:3584\n"
"ds_read_b64_tr_b16 a[222:223], v247 offset:11776\n"
"v_mfma_f32_16x16x32_bf16 v[8:11], v[212:215], a[180:183], v[8:11]\n"
"v_exp_f32 v199, v199\n"
"v_mfma_f32_16x16x32_bf16 v[12:15], v[212:215], a[184:187], v[12:15]\n"
"v_cvt_pk_bf16_f32 v219, v198, v199\n"
"s_waitcnt lgkmcnt(8)\n"
"v_mfma_f32_16x16x32_bf16 v[12:15], v[208:211], a[188:191], v[12:15]\n"
"v_exp_f32 v200, v200\n"
"v_mfma_f32_16x16x32_bf16 v[16:19], v[208:211], a[192:195], v[16:19]\n"
"v_mfma_f32_16x16x32_bf16 v[16:19], v[212:215], a[196:199], v[16:19]\n"
"v_exp_f32 v201, v201\n"
"v_mfma_f32_16x16x32_bf16 v[20:23], v[212:215], a[200:203], v[20:23]\n"
"s_waitcnt lgkmcnt(0)\n"
"v_mfma_f32_16x16x32_bf16 v[20:23], v[208:211], a[204:207], v[20:23]\n"
"v_exp_f32 v202, v202\n"
"v_mfma_f32_16x16x32_bf16 v[24:27], v[208:211], a[208:211], v[24:27]\n"
"v_mfma_f32_16x16x32_bf16 v[24:27], v[212:215], a[212:215], v[24:27]\n"
"v_exp_f32 v203, v203\n"
"v_mfma_f32_16x16x32_bf16 v[28:31], v[212:215], a[216:219], v[28:31]\n"
"v_mfma_f32_16x16x32_bf16 v[28:31], v[208:211], a[220:223], v[28:31]\n"
"v_mfma_f32_16x16x32_bf16 v[192:195], a[96:99], a[32:35], v[228:231]\n"
"v_exp_f32 v204, v204\n"
"v_mfma_f32_16x16x32_bf16 v[192:195], a[100:103], a[36:39], v[192:195]\n"
"v_cvt_pk_bf16_f32 v212, v200, v201\n"
"v_cvt_pk_bf16_f32 v213, v202, v203\n"
"v_mfma_f32_16x16x32_bf16 v[192:195], a[104:107], a[40:43], v[192:195]\n"
"v_exp_f32 v205, v205\n"
"v_mfma_f32_16x16x32_bf16 v[192:195], a[108:111], a[44:47], v[192:195]\n"
"v_cvt_pk_bf16_f32 v214, v204, v205\n"
"v_mfma_f32_16x16x32_bf16 v[196:199], a[112:115], a[44:47], v[228:231]\n"
"v_exp_f32 v206, v206\n"
"v_mfma_f32_16x16x32_bf16 v[196:199], a[116:119], a[40:43], v[196:199]\n"
"v_mfma_f32_16x16x32_bf16 v[196:199], a[120:123], a[36:39], v[196:199]\n"
"v_exp_f32 v207, v207\n"
"v_mfma_f32_16x16x32_bf16 v[196:199], a[124:127], a[32:35], v[196:199]\n"
"v_cvt_pk_bf16_f32 v215, v206, v207\n"
"v_mfma_f32_16x16x32_bf16 v[200:203], a[128:131], a[32:35], v[228:231]\n"
"v_exp_f32 v192, v192\n"
"v_mfma_f32_16x16x32_bf16 v[200:203], a[132:135], a[36:39], v[200:203]\n"
"v_mfma_f32_16x16x32_bf16 v[200:203], a[136:139], a[40:43], v[200:203]\n"
"v_exp_f32 v193, v193\n"
"v_mfma_f32_16x16x32_bf16 v[200:203], a[140:143], a[44:47], v[200:203]\n"
"v_cvt_pk_bf16_f32 v208, v192, v193\n"
"v_mfma_f32_16x16x32_bf16 v[204:207], a[144:147], a[44:47], v[228:231]\n"
"v_exp_f32 v194, v194\n"
"v_mfma_f32_16x16x32_bf16 v[204:207], a[148:151], a[40:43], v[204:207]\n"
"v_mfma_f32_16x16x32_bf16 v[204:207], a[152:155], a[36:39], v[204:207]\n"
"v_exp_f32 v195, v195\n"
"v_mfma_f32_16x16x32_bf16 v[204:207], a[156:159], a[32:35], v[204:207]\n"
"v_cvt_pk_bf16_f32 v209, v194, v195\n"
"v_smfmac_f32_16x16x64_bf16 a[228:231], a[248:251], v[212:219], v252\n"
"v_exp_f32 v196, v196\n"
"v_mfma_f32_16x16x32_bf16 v[32:35], v[216:219], a[160:163], v[32:35]\n"
"v_mfma_f32_16x16x32_bf16 v[32:35], v[212:215], a[164:167], v[32:35]\n"
"v_exp_f32 v197, v197\n"
"v_mfma_f32_16x16x32_bf16 v[36:39], v[212:215], a[168:171], v[36:39]\n"
"v_cvt_pk_bf16_f32 v210, v196, v197\n"
"v_mfma_f32_16x16x32_bf16 v[36:39], v[216:219], a[172:175], v[36:39]\n"
"v_exp_f32 v198, v198\n"
"v_mfma_f32_16x16x32_bf16 v[40:43], v[216:219], a[176:179], v[40:43]\n"
"v_mfma_f32_16x16x32_bf16 v[40:43], v[212:215], a[180:183], v[40:43]\n"
"v_exp_f32 v199, v199\n"
"v_mfma_f32_16x16x32_bf16 v[44:47], v[212:215], a[184:187], v[44:47]\n"
"v_cvt_pk_bf16_f32 v211, v198, v199\n"
"v_mfma_f32_16x16x32_bf16 v[44:47], v[216:219], a[188:191], v[44:47]\n"
"v_exp_f32 v200, v200\n"
"v_mfma_f32_16x16x32_bf16 v[48:51], v[216:219], a[192:195], v[48:51]\n"
"s_add_u32 s27, s27, 1\n"
"v_mov_b32 v255, s27\n"
"v_mfma_f32_16x16x32_bf16 v[48:51], v[212:215], a[196:199], v[48:51]\n"
"v_exp_f32 v201, v201\n"
"v_mfma_f32_16x16x32_bf16 v[52:55], v[212:215], a[200:203], v[52:55]\n"
"v_mfma_f32_16x16x32_bf16 v[52:55], v[216:219], a[204:207], v[52:55]\n"
"v_exp_f32 v202, v202\n"
"v_mfma_f32_16x16x32_bf16 v[56:59], v[216:219], a[208:211], v[56:59]\n"
"v_mfma_f32_16x16x32_bf16 v[56:59], v[212:215], a[212:215], v[56:59]\n"
"v_exp_f32 v203, v203\n"
"v_mfma_f32_16x16x32_bf16 v[60:63], v[212:215], a[216:219], v[60:63]\n"
"s_waitcnt vmcnt(0)\n"
"ds_write_b32 v253, v255\n"
"v_mfma_f32_16x16x32_bf16 v[60:63], v[216:219], a[220:223], v[60:63]\n"
"v_mfma_f32_16x16x32_bf16 v[192:195], a[96:99], a[48:51], v[232:235]\n"
"v_exp_f32 v204, v204\n"
"v_mfma_f32_16x16x32_bf16 v[192:195], a[100:103], a[52:55], v[192:195]\n"
"v_cvt_pk_bf16_f32 v212, v200, v201\n"
"v_cvt_pk_bf16_f32 v213, v202, v203\n"
"v_mfma_f32_16x16x32_bf16 v[192:195], a[104:107], a[56:59], v[192:195]\n"
"v_exp_f32 v205, v205\n"
"v_mfma_f32_16x16x32_bf16 v[192:195], a[108:111], a[60:63], v[192:195]\n"
"v_cvt_pk_bf16_f32 v214, v204, v205\n"
"v_mfma_f32_16x16x32_bf16 v[196:199], a[112:115], a[60:63], v[232:235]\n"
"v_exp_f32 v206, v206\n"
"v_mfma_f32_16x16x32_bf16 v[196:199], a[116:119], a[56:59], v[196:199]\n"
"v_mfma_f32_16x16x32_bf16 v[196:199], a[120:123], a[52:55], v[196:199]\n"
"v_exp_f32 v207, v207\n"
"v_mfma_f32_16x16x32_bf16 v[196:199], a[124:127], a[48:51], v[196:199]\n"
"v_cvt_pk_bf16_f32 v215, v206, v207\n"
"v_mfma_f32_16x16x32_bf16 v[200:203], a[128:131], a[48:51], v[232:235]\n"
"v_exp_f32 v192, v192\n"
"v_mfma_f32_16x16x32_bf16 v[200:203], a[132:135], a[52:55], v[200:203]\n"
"v_mfma_f32_16x16x32_bf16 v[200:203], a[136:139], a[56:59], v[200:203]\n"
"v_exp_f32 v193, v193\n"
"v_mfma_f32_16x16x32_bf16 v[200:203], a[140:143], a[60:63], v[200:203]\n"
"v_cvt_pk_bf16_f32 v216, v192, v193\n"
"v_mfma_f32_16x16x32_bf16 v[204:207], a[144:147], a[60:63], v[232:235]\n"
"v_exp_f32 v194, v194\n"
"v_mfma_f32_16x16x32_bf16 v[204:207], a[148:151], a[56:59], v[204:207]\n"
"v_mfma_f32_16x16x32_bf16 v[204:207], a[152:155], a[52:55], v[204:207]\n"
"v_exp_f32 v195, v195\n"
"v_mfma_f32_16x16x32_bf16 v[204:207], a[156:159], a[48:51], v[204:207]\n"
"v_cvt_pk_bf16_f32 v217, v194, v195\n"
"v_smfmac_f32_16x16x64_bf16 a[232:235], a[248:251], v[208:215], v252\n"
"v_exp_f32 v196, v196\n"
"v_mfma_f32_16x16x32_bf16 v[64:67], v[208:211], a[160:163], v[64:67]\n"
"v_mfma_f32_16x16x32_bf16 v[64:67], v[212:215], a[164:167], v[64:67]\n"
"v_exp_f32 v197, v197\n"
"v_mfma_f32_16x16x32_bf16 v[68:71], v[212:215], a[168:171], v[68:71]\n"
"v_cvt_pk_bf16_f32 v218, v196, v197\n"
"v_mfma_f32_16x16x32_bf16 v[68:71], v[208:211], a[172:175], v[68:71]\n"
"v_exp_f32 v198, v198\n"
"v_mfma_f32_16x16x32_bf16 v[72:75], v[208:211], a[176:179], v[72:75]\n"
"ds_read_b32 v255, v254\n"
"v_mfma_f32_16x16x32_bf16 v[72:75], v[212:215], a[180:183], v[72:75]\n"
"v_exp_f32 v199, v199\n"
"v_mfma_f32_16x16x32_bf16 v[76:79], v[212:215], a[184:187], v[76:79]\n"
"v_cvt_pk_bf16_f32 v219, v198, v199\n"
"v_mfma_f32_16x16x32_bf16 v[76:79], v[208:211], a[188:191], v[76:79]\n"
"v_exp_f32 v200, v200\n"
"v_mfma_f32_16x16x32_bf16 v[80:83], v[208:211], a[192:195], v[80:83]\n"
"v_mfma_f32_16x16x32_bf16 v[80:83], v[212:215], a[196:199], v[80:83]\n"
"v_exp_f32 v201, v201\n"
"v_mfma_f32_16x16x32_bf16 v[84:87], v[212:215], a[200:203], v[84:87]\n"
"v_mfma_f32_16x16x32_bf16 v[84:87], v[208:211], a[204:207], v[84:87]\n"
"v_exp_f32 v202, v202\n"
"v_mfma_f32_16x16x32_bf16 v[88:91], v[208:211], a[208:211], v[88:91]\n"
"v_mfma_f32_16x16x32_bf16 v[88:91], v[212:215], a[212:215], v[88:91]\n"
"v_exp_f32 v203, v203\n"
"v_mfma_f32_16x16x32_bf16 v[92:95], v[212:215], a[216:219], v[92:95]\n"
"s_waitcnt lgkmcnt(0)\n"
"v_cmp_gt_u32 vcc, s27, v255\n"
"v_mfma_f32_16x16x32_bf16 v[92:95], v[208:211], a[220:223], v[92:95]\n"
"s_cbranch_vccnz L_late5_15_%=\n"
"L_back5_15_%=:\n"
"v_mfma_f32_16x16x32_bf16 v[192:195], a[96:99], a[64:67], v[236:239]\n"
"v_exp_f32 v204, v204\n"
"v_mfma_f32_16x16x32_bf16 v[192:195], a[100:103], a[68:71], v[192:195]\n"
"v_cvt_pk_bf16_f32 v212, v200, v201\n"
"v_cvt_pk_bf16_f32 v213, v202, v203\n"
"v_mfma_f32_16x16x32_bf16 v[192:195], a[104:107], a[72:75], v[192:195]\n"
"v_exp_f32 v205, v205\n"
"v_mfma_f32_16x16x32_bf16 v[192:195], a[108:111], a[76:79], v[192:195]\n"
"ds_read_b128 a[96:99], v244 offset:32768\n"
"v_cvt_pk_bf16_f32 v214, v204, v205\n"
"v_mfma_f32_16x16x32_bf16 v[196:199], a[112:115], a[76:79], v[236:239]\n"
"v_exp_f32 v206, v206\n"
"v_mfma_f32_16x16x32_bf16 v[196:199], a[116:119], a[72:75], v[196:199]\n"
"ds_read_b128 a[100:103], v245 offset:32768\n"
"ds_read_b128 a[104:107], v244 offset:33792\n"
"v_mfma_f32_16x16x32_bf16 v[196:199], a[120:123], a[68:71], v[196:199]\n"
"v_exp_f32 v207, v207\n"
"v_mfma_f32_16x16x32_bf16 v[196:199], a[124:127], a[64:67], v[196:199]\n"
"ds_read_b128 a[108:111], v245 offset:33792\n"
"v_cvt_pk_bf16_f32 v215, v206, v207\n"
"v_mfma_f32_16x16x32_bf16 v[200:203], a[128:131], a[64:67], v[236:239]\n"
"v_exp_f32 v192, v192\n"
"v_mfma_f32_16x16x32_bf16 v[200:203], a[132:135], a[68:71], v[200:203]\n"
"ds_read_b128 a[112:115], v245 offset:41984\n"
"ds_read_b128 a[116:119], v244 offset:41984\n"
"v_mfma_f32_16x16x32_bf16 v[200:203], a[136:139], a[72:75], v[200:203]\n"
"v_exp_f32 v193, v193\n"
"v_mfma_f32_16x16x32_bf16 v[200:203], a[140:143], a[76:79], v[200:203]\n"
"ds_read_b128 a[120:123], v245 offset:40960\n"
"v_cvt_pk_bf16_f32 v208, v192, v193\n"
"v_mfma_f32_16x16x32_bf16 v[204:207], a[144:147], a[76:79], v[236:239]\n"
"v_exp_f32 v194, v194\n"
"v_mfma_f32_16x16x32_bf16 v[204:207], a[148:151], a[72:75], v[204:207]\n"
"ds_read_b128 a[124:127], v244 offset:40960\n"
"v_mfma_f32_16x16x32_bf16 v[204:207], a[152:155], a[68:71], v[204:207]\n"
"v_exp_f32 v195, v195\n"
"v_mfma_f32_16x16x32_bf16 v[204:207], a[156:159], a[64:67], v[204:207]\n"
"ds_read_b128 a[128:131], v244 offset:49152\n"
"v_cvt_pk_bf16_f32 v209, v194, v195\n"
"v_smfmac_f32_16x16x64_bf16 a[236:239], a[248:251], v[212:219], v252\n"
"v_exp_f32 v196, v196\n"
"v_mfma_f32_16x16x32_bf16 v[96:99], v[216:219], a[160:163], v[96:99]\n"
"ds_read_b128 a[132:135], v245 offset:49152\n"
"v_mfma_f32_16x16x32_bf16 v[96:99], v[212:215], a[164:167], v[96:99]\n"
"v_exp_f32 v197, v197\n"
"v_mfma_f32_16x16x32_bf16 v[100:103], v[212:215], a[168:171], v[100:103]\n"
"ds_read_b128 a[136:139], v244 offset:50176\n"
"v_cvt_pk_bf16_f32 v210, v196, v197\n"
"v_mfma_f32_16x16x32_bf16 v[100:103], v[216:219], a[172:175], v[100:103]\n"
"v_exp_f32 v198, v198\n"
"v_mfma_f32_16x16x32_bf16 v[104:107], v[216:219], a[176:179], v[104:107]\n"
"ds_read_b128 a[140:143], v245 offset:50176\n"
"ds_read_b128 a[144:147], v245 offset:58368\n"
"v_mfma_f32_16x16x32_bf16 v[104:107], v[212:215], a[180:183], v[104:107]\n"
"v_exp_f32 v199, v199\n"
"v_mfma_f32_16x16x32_bf16 v[108:111], v[212:215], a[184:187], v[108:111]\n"
"ds_read_b128 a[148:151], v244 offset:58368\n"
"v_cvt_pk_bf16_f32 v211, v198, v199\n"
"v_mfma_f32_16x16x32_bf16 v[108:111], v[216:219], a[188:191], v[108:111]\n"
"v_exp_f32 v200, v200\n"
"v_mfma_f32_16x16x32_bf16 v[112:115], v[216:219], a[192:195], v[112:115]\n"
"ds_read_b128 a[152:155], v245 offset:57344\n"
"v_mfma_f32_16x16x32_bf16 v[112:115], v[212:215], a[196:199], v[112:115]\n"
"v_exp_f32 v201, v201\n"
"v_mfma_f32_16x16x32_bf16 v[116:119], v[212:215], a[200:203], v[116:119]\n"
"ds_read_b128 a[156:159], v244 offset:57344\n"
"v_mfma_f32_16x16x32_bf16 v[116:119], v[216:219], a[204:207], v[116:119]\n"
"v_exp_f32 v202, v202\n"
"v_mfma_f32_16x16x32_bf16 v[120:123], v[216:219], a[208:211], v[120:123]\n"
"v_mfma_f32_16x16x32_bf16 v[120:123], v[212:215], a[212:215], v[120:123]\n"
"v_exp_f32 v203, v203\n"
"v_mfma_f32_16x16x32_bf16 v[124:127], v[212:215], a[216:219], v[124:127]\n"
"v_mfma_f32_16x16x32_bf16 v[124:127], v[216:219], a[220:223], v[124:127]\n"
"s_waitcnt lgkmcnt(8)\n"
"v_mfma_f32_16x16x32_bf16 v[192:195], a[96:99], a[0:3], v[220:223]\n"
"v_exp_f32 v204, v204\n"
"v_mfma_f32_16x16x32_bf16 v[192:195], a[100:103], a[4:7], v[192:195]\n"
"v_cvt_pk_bf16_f32 v212, v200, v201\n"
"v_cvt_pk_bf16_f32 v213, v202, v203\n"
"v_mfma_f32_16x16x32_bf16 v[192:195], a[104:107], a[8:11], v[192:195]\n"
"v_exp_f32 v205, v205\n"
"v_mfma_f32_16x16x32_bf16 v[192:195], a[108:111], a[12:15], v[192:195]\n"
"v_cvt_pk_bf16_f32 v214, v204, v205\n"
"v_mfma_f32_16x16x32_bf16 v[196:199], a[112:115], a[12:15], v[220:223]\n"
"v_exp_f32 v206, v206\n"
"v_mfma_f32_16x16x32_bf16 v[196:199], a[116:119], a[8:11], v[196:199]\n"
"v_mfma_f32_16x16x32_bf16 v[196:199], a[120:123], a[4:7], v[196:199]\n"
"v_exp_f32 v207, v207\n"
"v_mfma_f32_16x16x32_bf16 v[196:199], a[124:127], a[0:3], v[196:199]\n"
"v_cvt_pk_bf16_f32 v215, v206, v207\n"
"s_waitcnt lgkmcnt(0)\n"
"v_mfma_f32_16x16x32_bf16 v[200:203], a[128:131], a[0:3], v[220:223]\n"
"v_exp_f32 v192, v192\n"
"v_mfma_f32_16x16x32_bf16 v[200:203], a[132:135], a[4:7], v[200:203]\n"
"v_mfma_f32_16x16x32_bf16 v[200:203], a[136:139], a[8:11], v[200:203]\n"
"v_exp_f32 v193, v193\n"
"v_mfma_f32_16x16x32_bf16 v[200:203], a[140:143], a[12:15], v[200:203]\n"
"v_cvt_pk_bf16_f32 v216, v192, v193\n"
"v_mfma_f32_16x16x32_bf16 v[204:207], a[144:147], a[12:15], v[220:223]\n"
"v_exp_f32 v194, v194\n"
"v_mfma_f32_16x16x32_bf16 v[204:207], a[148:151], a[8:11], v[204:207]\n"
"v_mfma_f32_16x16x32_bf16 v[204:207], a[152:155], a[4:7], v[204:207]\n"
"v_exp_f32 v195, v195\n"
"v_mfma_f32_16x16x32_bf16 v[204:207], a[156:159], a[0:3], v[204:207]\n"
"v_cvt_pk_bf16_f32 v217, v194, v195\n"
"v_smfmac_f32_16x16x64_bf16 a[240:243], a[248:251], v[208:215], v252\n"
"v_exp_f32 v196, v196\n"
"v_mfma_f32_16x16x32_bf16 v[128:131], v[208:211], a[160:163], v[128:131]\n"
"ds_read_b64_tr_b16 a[160:161], v246 offset:34816\n"
"ds_read_b64_tr_b16 a[162:163], v246 offset:43008\n"
"v_mfma_f32_16x16x32_bf16 v[128:131], v[212:215], a[164:167], v[128:131]\n"
"v_exp_f32 v197, v197\n"
"v_mfma_f32_16x16x32_bf16 v[132:135], v[212:215], a[168:171], v[132:135]\n"
"ds_read_b64_tr_b16 a[164:165], v246 offset:51200\n"
"v_cvt_pk_bf16_f32 v218, v196, v197\n"
"v_mfma_f32_16x16x32_bf16 v[132:135], v[208:211], a[172:175], v[132:135]\n"
"v_exp_f32 v198, v198\n"
"v_mfma_f32_16x16x32_bf16 v[136:139], v[208:211], a[176:179], v[136:139]\n"
"ds_read_b64_tr_b16 a[166:167], v246 offset:59392\n"
"ds_read_b64_tr_b16 a[168:169], v247 offset:51200\n"
"v_mfma_f32_16x16x32_bf16 v[136:139], v[212:215], a[180:183], v[136:139]\n"
"v_exp_f32 v199, v199\n"
"v_mfma_f32_16x16x32_bf16 v[140:143], v[212:215], a[184:187], v[140:143]\n"
"ds_read_b64_tr_b16 a[170:171], v247 offset:59392\n"
"v_cvt_pk_bf16_f32 v219, v198, v199\n"
"v_mfma_f32_16x16x32_bf16 v[140:143], v[208:211], a[188:191], v[140:143]\n"
"v_exp_f32 v200, v200\n"
"v_mfma_f32_16x16x32_bf16 v[144:147], v[208:211], a[192:195], v[144:147]\n"
"ds_read_b64_tr_b16 a[172:173], v247 offset:34816\n"
"ds_read_b64_tr_b16 a[174:175], v247 offset:43008\n"
"v_mfma_f32_16x16x32_bf16 v[144:147], v[212:215], a[196:199], v[144:147]\n"
"v_exp_f32 v201, v201\n"
"v_mfma_f32_16x16x32_bf16 v[148:151], v[212:215], a[200:203], v[148:151]\n"
"ds_read_b64_tr_b16 a[176:177], v246 offset:35328\n"
"ds_read_b64_tr_b16 a[178:179], v246 offset:43520\n"
"v_mfma_f32_16x16x32_bf16 v[148:151], v[208:211], a[204:207], v[148:151]\n"
"v_exp_f32 v202, v202\n"
"v_mfma_f32_16x16x32_bf16 v[152:155], v[208:211], a[208:211], v[152:155]\n"
"ds_read_b64_tr_b16 a[180:181], v246 offset:51712\n"
"ds_read_b64_tr_b16 a[182:183], v246 offset:59904\n"
"v_mfma_f32_16x16x32_bf16 v[152:155], v[212:215], a[212:215], v[152:155]\n"
"v_exp_f32 v203, v203\n"
"v_mfma_f32_16x16x32_bf16 v[156:159], v[212:215], a[216:219], v[156:159]\n"
"ds_read_b64_tr_b16 a[184:185], v247 offset:51712\n"
"ds_read_b64_tr_b16 a[186:187], v247 offset:59904\n"
"v_mfma_f32_16x16x32_bf16 v[156:159], v[208:211], a[220:223], v[156:159]\n"
"ds_read_b64_tr_b16 a[188:189], v247 offset:35328\n"
"ds_read_b64_tr_b16 a[190:191], v247 offset:43520\n"
"v_mfma_f32_16x16x32_bf16 v[192:195], a[96:99], a[16:19], v[224:227]\n"
"v_exp_f32 v204, v204\n"
"v_mfma_f32_16x16x32_bf16 v[192:195], a[100:103], a[20:23], v[192:195]\n"
"v_cvt_pk_bf16_f32 v212, v200, v201\n"
"v_cvt_pk_bf16_f32 v213, v202, v203\n"
"v_mfma_f32_16x16x32_bf16 v[192:195], a[104:107], a[24:27], v[192:195]\n"
"v_exp_f32 v205, v205\n"
"v_mov_b32 v255, 0\n"
"v_mbcnt_lo_u32_b32 v253, -1, 0\n"
"v_mfma_f32_16x16x32_bf16 v[192:195], a[108:111], a[28:31], v[192:195]\n"
"ds_read_b64_tr_b16 a[192:193], v246 offset:35840\n"
"v_cvt_pk_bf16_f32 v214, v204, v205\n"
"v_mbcnt_hi_u32_b32 v253, -1, v253\n"
"v_mfma_f32_16x16x32_bf16 v[196:199], a[112:115], a[28:31], v[224:227]\n"
"v_exp_f32 v206, v206\n"
"s_mul_i32 s59, %[wid], 24576\n"
"v_mfma_f32_16x16x32_bf16 v[196:199], a[116:119], a[24:27], v[196:199]\n"
"ds_read_b64_tr_b16 a[194:195], v246 offset:44032\n"
"ds_read_b64_tr_b16 a[196:197], v246 offset:52224\n"
"s_lshl_b32 s55, %[wid], 13\n"
"s_add_u32 s55, s55, 131072\n"
"v_mfma_f32_16x16x32_bf16 v[196:199], a[120:123], a[20:23], v[196:199]\n"
"v_exp_f32 v207, v207\n"
"v_and_b32 v254, 3, v253\n"
"v_mfma_f32_16x16x32_bf16 v[196:199], a[124:127], a[16:19], v[196:199]\n"
"ds_read_b64_tr_b16 a[198:199], v246 offset:60416\n"
"v_cvt_pk_bf16_f32 v215, v206, v207\n"
"v_lshlrev_b32 v250, 5, v254\n"
"v_mfma_f32_16x16x32_bf16 v[200:203], a[128:131], a[16:19], v[224:227]\n"
"v_exp_f32 v192, v192\n"
"v_lshlrev_b32 v249, 6, v254\n"
"v_mfma_f32_16x16x32_bf16 v[200:203], a[132:135], a[20:23], v[200:203]\n"
"ds_read_b64_tr_b16 a[200:201], v247 offset:52224\n"
"ds_read_b64_tr_b16 a[202:203], v247 offset:60416\n"
"v_bfe_u32 v254, v253, 2, 1\n"
"v_lshl_add_u32 v250, v254, 4, v250\n"
"v_mfma_f32_16x16x32_bf16 v[200:203], a[136:139], a[24:27], v[200:203]\n"
"v_exp_f32 v193, v193\n"
"v_bfe_u32 v254, v253, 3, 1\n"
"v_mfma_f32_16x16x32_bf16 v[200:203], a[140:143], a[28:31], v[200:203]\n"
"ds_read_b64_tr_b16 a[204:205], v247 offset:35840\n"
"v_cvt_pk_bf16_f32 v208, v192, v193\n"
"v_lshl_add_u32 v250, v254, 7, v250\n"
"v_mfma_f32_16x16x32_bf16 v[204:207], a[144:147], a[28:31], v[224:227]\n"
"v_exp_f32 v194, v194\n"
"v_bfe_u32 v254, v253, 2, 2\n"
"v_mfma_f32_16x16x32_bf16 v[204:207], a[148:151], a[24:27], v[204:207]\n"
"ds_read_b64_tr_b16 a[206:207], v247 offset:44032\n"
"ds_read_b64_tr_b16 a[208:209], v246 offset:36352\n"
"v_lshl_add_u32 v249, v254, 3, v249\n"
"v_lshl_add_u32 v251, v253, 2, s55\n"
"v_mfma_f32_16x16x32_bf16 v[204:207], a[152:155], a[20:23], v[204:207]\n"
"v_exp_f32 v195, v195\n"
"v_and_b32 v254, 15, v253\n"
"v_mfma_f32_16x16x32_bf16 v[204:207], a[156:159], a[16:19], v[204:207]\n"
"v_cvt_pk_bf16_f32 v209, v194, v195\n"
"s_waitcnt lgkmcnt(15)\n"
"v_lshl_add_u32 v248, v254, 3, s55\n"
"v_smfmac_f32_16x16x64_bf16 a[224:227], a[248:251], v[212:219], v252\n"
"v_exp_f32 v196, v196\n"
"v_lshrrev_b32 v254, 4, v253\n"
"v_mfma_f32_16x16x32_bf16 v[0:3], v[216:219], a[160:163], v[0:3]\n"
"ds_read_b64_tr_b16 a[210:211], v246 offset:44544\n"
"ds_read_b64_tr_b16 a[212:213], v246 offset:52736\n"
"v_lshl_add_u32 v250, v254, 8, v250\n"
"v_mul_u32_u24 v253, 288, v254\n"
"v_mfma_f32_16x16x32_bf16 v[0:3], v[212:215], a[164:167], v[0:3]\n"
"v_exp_f32 v197, v197\n"
"v_add3_u32 v249, v249, v253, s55\n"
"v_mfma_f32_16x16x32_bf16 v[4:7], v[212:215], a[168:171], v[4:7]\n"
"v_cvt_pk_bf16_f32 v210, v196, v197\n"
"s_waitcnt lgkmcnt(11)\n"
"v_lshl_add_u32 v248, v253, 2, v248\n"
"v_mfma_f32_16x16x32_bf16 v[4:7], v[216:219], a[172:175], v[4:7]\n"
"v_exp_f32 v198, v198\n"
"v_lshl_add_u32 v254, v254, 4, s55\n"
"v_mfma_f32_16x16x32_bf16 v[8:11], v[216:219], a[176:179], v[8:11]\n"
"ds_read_b64_tr_b16 a[214:215], v246 offset:60928\n"
"ds_read_b64_tr_b16 a[216:217], v247 offset:52736\n"
"ds_read_b64_tr_b16 a[218:219], v247 offset:60928\n"
"ds_read_b64_tr_b16 a[220:221], v247 offset:36352\n"
"ds_read_b64_tr_b16 a[222:223], v247 offset:44544\n"
"v_mfma_f32_16x16x32_bf16 v[8:11], v[212:215], a[180:183], v[8:11]\n"
"v_exp_f32 v199, v199\n"
"v_mfma_f32_16x16x32_bf16 v[12:15], v[212:215], a[184:187], v[12:15]\n"
"v_cvt_pk_bf16_f32 v211, v198, v199\n"
"s_waitcnt lgkmcnt(8)\n"
"v_mfma_f32_16x16x32_bf16 v[12:15], v[216:219], a[188:191], v[12:15]\n"
"v_exp_f32 v200, v200\n"
"v_mfma_f32_16x16x32_bf16 v[16:19], v[216:219], a[192:195], v[16:19]\n"
"v_accvgpr_read_b32 v220, a224\n"
"v_mfma_f32_16x16x32_bf16 v[16:19], v[212:215], a[196:199], v[16:19]\n"
"v_exp_f32 v201, v201\n"
"v_accvgpr_read_b32 v221, a225\n"
"v_mfma_f32_16x16x32_bf16 v[20:23], v[212:215], a[200:203], v[20:23]\n"
"s_waitcnt lgkmcnt(0)\n"
"v_add_f32 v220, v220, v221\n"
"v_mfma_f32_16x16x32_bf16 v[20:23], v[216:219], a[204:207], v[20:23]\n"
"v_exp_f32 v202, v202\n"
"v_add_f32 v255, v255, v220\n"
"v_mfma_f32_16x16x32_bf16 v[24:27], v[216:219], a[208:211], v[24:27]\n"
"v_rcp_f32 v220, v220\n"
"v_mfma_f32_16x16x32_bf16 v[24:27], v[212:215], a[212:215], v[24:27]\n"
"ds_write_b32 v251, v220 offset:5120\n"
"v_exp_f32 v203, v203\n"
"v_mfma_f32_16x16x32_bf16 v[28:31], v[212:215], a[216:219], v[28:31]\n"
"ds_read_b128 v[220:223], v254 offset:5120\n"
"v_mfma_f32_16x16x32_bf16 v[28:31], v[216:219], a[220:223], v[28:31]\n"
"v_mfma_f32_16x16x32_bf16 v[192:195], a[96:99], a[32:35], v[228:231]\n"
"v_exp_f32 v204, v204\n"
"s_waitcnt lgkmcnt(0)\n"
"v_mfma_f32_16x16x32_bf16 v[192:195], a[100:103], a[36:39], v[192:195]\n"
"v_cvt_pk_bf16_f32 v212, v200, v201\n"
"v_cvt_pk_bf16_f32 v213, v202, v203\n"
"v_pk_mul_f32 v[0:1], v[0:1], v[220:221]\n"
"v_pk_mul_f32 v[2:3], v[2:3], v[222:223]\n"
"v_pk_mul_f32 v[4:5], v[4:5], v[220:221]\n"
"v_pk_mul_f32 v[6:7], v[6:7], v[222:223]\n"
"v_pk_mul_f32 v[8:9], v[8:9], v[220:221]\n"
"v_pk_mul_f32 v[10:11], v[10:11], v[222:223]\n"
"v_pk_mul_f32 v[12:13], v[12:13], v[220:221]\n"
"v_pk_mul_f32 v[14:15], v[14:15], v[222:223]\n"
"v_mfma_f32_16x16x32_bf16 v[192:195], a[104:107], a[40:43], v[192:195]\n"
"v_exp_f32 v205, v205\n"
"v_cvt_pk_bf16_f32 v0, v0, v4\n"
"v_cvt_pk_bf16_f32 v8, v8, v12\n"
"v_mfma_f32_16x16x32_bf16 v[192:195], a[108:111], a[44:47], v[192:195]\n"
"v_cvt_pk_bf16_f32 v214, v204, v205\n"
"v_mfma_f32_16x16x32_bf16 v[196:199], a[112:115], a[44:47], v[228:231]\n"
"ds_write2_b32 v248, v0, v8 offset0:0 offset1:1\n"
"v_exp_f32 v206, v206\n"
"v_cvt_pk_bf16_f32 v1, v1, v5\n"
"v_cvt_pk_bf16_f32 v9, v9, v13\n"
"v_mfma_f32_16x16x32_bf16 v[196:199], a[116:119], a[40:43], v[196:199]\n"
"ds_write2_b32 v248, v1, v9 offset0:72 offset1:73\n"
"v_cvt_pk_bf16_f32 v2, v2, v6\n"
"v_cvt_pk_bf16_f32 v10, v10, v14\n"
"v_pk_mul_f32 v[16:17], v[16:17], v[220:221]\n"
"v_pk_mul_f32 v[18:19], v[18:19], v[222:223]\n"
"v_pk_mul_f32 v[20:21], v[20:21], v[220:221]\n"
"v_pk_mul_f32 v[22:23], v[22:23], v[222:223]\n"
"v_pk_mul_f32 v[24:25], v[24:25], v[220:221]\n"
"v_pk_mul_f32 v[26:27], v[26:27], v[222:223]\n"
"v_pk_mul_f32 v[28:29], v[28:29], v[220:221]\n"
"v_pk_mul_f32 v[30:31], v[30:31], v[222:223]\n"
"v_mfma_f32_16x16x32_bf16 v[196:199], a[120:123], a[36:39], v[196:199]\n"
"v_exp_f32 v207, v207\n"
"v_mfma_f32_16x16x32_bf16 v[196:199], a[124:127], a[32:35], v[196:199]\n"
"ds_write2_b32 v248, v2, v10 offset0:144 offset1:145\n"
"v_cvt_pk_bf16_f32 v215, v206, v207\n"
"v_cvt_pk_bf16_f32 v3, v3, v7\n"
"v_cvt_pk_bf16_f32 v11, v11, v15\n"
"v_mfma_f32_16x16x32_bf16 v[200:203], a[128:131], a[32:35], v[228:231]\n"
"ds_write2_b32 v248, v3, v11 offset0:216 offset1:217\n"
"v_exp_f32 v192, v192\n"
"v_cvt_pk_bf16_f32 v16, v16, v20\n"
"v_cvt_pk_bf16_f32 v24, v24, v28\n"
"v_mfma_f32_16x16x32_bf16 v[200:203], a[132:135], a[36:39], v[200:203]\n"
"v_mfma_f32_16x16x32_bf16 v[200:203], a[136:139], a[40:43], v[200:203]\n"
"ds_write2_b32 v248, v16, v24 offset0:32 offset1:33\n"
"v_exp_f32 v193, v193\n"
"v_cvt_pk_bf16_f32 v17, v17, v21\n"
"v_cvt_pk_bf16_f32 v25, v25, v29\n"
"v_mfma_f32_16x16x32_bf16 v[200:203], a[140:143], a[44:47], v[200:203]\n"
"ds_write2_b32 v248, v17, v25 offset0:104 offset1:105\n"
"v_cvt_pk_bf16_f32 v216, v192, v193\n"
"v_cvt_pk_bf16_f32 v18, v18, v22\n"
"v_cvt_pk_bf16_f32 v26, v26, v30\n"
"v_mfma_f32_16x16x32_bf16 v[204:207], a[144:147], a[44:47], v[228:231]\n"
"v_exp_f32 v194, v194\n"
"v_mfma_f32_16x16x32_bf16 v[204:207], a[148:151], a[40:43], v[204:207]\n"
"ds_write2_b32 v248, v18, v26 offset0:176 offset1:177\n"
"v_cvt_pk_bf16_f32 v19, v19, v23\n"
"v_cvt_pk_bf16_f32 v27, v27, v31\n"
"v_mfma_f32_16x16x32_bf16 v[204:207], a[152:155], a[36:39], v[204:207]\n"
"ds_write2_b32 v248, v19, v27 offset0:248 offset1:249\n"
"v_exp_f32 v195, v195\n"
"v_mfma_f32_16x16x32_bf16 v[204:207], a[156:159], a[32:35], v[204:207]\n"
"v_cvt_pk_bf16_f32 v217, v194, v195\n"
"v_smfmac_f32_16x16x64_bf16 a[228:231], a[248:251], v[208:215], v252\n"
"ds_read_b64_tr_b16 v[4:5], v249 offset:0\n"
"ds_read_b64_tr_b16 v[6:7], v249 offset:32\n"
"v_exp_f32 v196, v196\n"
"v_mfma_f32_16x16x32_bf16 v[32:35], v[208:211], a[160:163], v[32:35]\n"
"ds_read_b64_tr_b16 v[12:13], v249 offset:1152\n"
"ds_read_b64_tr_b16 v[14:15], v249 offset:1184\n"
"v_mfma_f32_16x16x32_bf16 v[32:35], v[212:215], a[164:167], v[32:35]\n"
"v_exp_f32 v197, v197\n"
"v_mfma_f32_16x16x32_bf16 v[36:39], v[212:215], a[168:171], v[36:39]\n"
"ds_read_b64_tr_b16 v[20:21], v249 offset:2304\n"
"ds_read_b64_tr_b16 v[22:23], v249 offset:2336\n"
"v_cvt_pk_bf16_f32 v218, v196, v197\n"
"v_mfma_f32_16x16x32_bf16 v[36:39], v[208:211], a[172:175], v[36:39]\n"
"ds_read_b64_tr_b16 v[28:29], v249 offset:3456\n"
"ds_read_b64_tr_b16 v[30:31], v249 offset:3488\n"
"v_exp_f32 v198, v198\n"
"v_mfma_f32_16x16x32_bf16 v[40:43], v[208:211], a[176:179], v[40:43]\n"
"v_mfma_f32_16x16x32_bf16 v[40:43], v[212:215], a[180:183], v[40:43]\n"
"v_exp_f32 v199, v199\n"
"v_mfma_f32_16x16x32_bf16 v[44:47], v[212:215], a[184:187], v[44:47]\n"
"v_cvt_pk_bf16_f32 v219, v198, v199\n"
"v_mfma_f32_16x16x32_bf16 v[44:47], v[208:211], a[188:191], v[44:47]\n"
"v_exp_f32 v200, v200\n"
"v_mfma_f32_16x16x32_bf16 v[48:51], v[208:211], a[192:195], v[48:51]\n"
"v_accvgpr_read_b32 v224, a228\n"
"v_mfma_f32_16x16x32_bf16 v[48:51], v[212:215], a[196:199], v[48:51]\n"
"s_waitcnt lgkmcnt(6)\n"
"v_exp_f32 v201, v201\n"
"s_add_u32 s58, s59, 0\n"
"buffer_store_dwordx4 v[4:7], v250, s[48:51], s58 offen sc0 sc1 nt\n"
"v_accvgpr_read_b32 v225, a229\n"
"v_mfma_f32_16x16x32_bf16 v[52:55], v[212:215], a[200:203], v[52:55]\n"
"s_waitcnt lgkmcnt(4)\n"
"buffer_store_dwordx4 v[12:15], v250, s[48:51], s58 offen offset:1024 sc0 sc1 nt\n"
"v_add_f32 v224, v224, v225\n"
"v_mfma_f32_16x16x32_bf16 v[52:55], v[208:211], a[204:207], v[52:55]\n"
"v_exp_f32 v202, v202\n"
"v_add_f32 v255, v255, v224\n"
"v_mfma_f32_16x16x32_bf16 v[56:59], v[208:211], a[208:211], v[56:59]\n"
"s_waitcnt lgkmcnt(2)\n"
"buffer_store_dwordx4 v[20:23], v250, s[48:51], s58 offen offset:2048 sc0 sc1 nt\n"
"v_rcp_f32 v224, v224\n"
"v_mfma_f32_16x16x32_bf16 v[56:59], v[212:215], a[212:215], v[56:59]\n"
"ds_write_b32 v251, v224 offset:5120\n"
"v_exp_f32 v203, v203\n"
"v_mfma_f32_16x16x32_bf16 v[60:63], v[212:215], a[216:219], v[60:63]\n"
"s_waitcnt lgkmcnt(1)\n"
"buffer_store_dwordx4 v[28:31], v250, s[48:51], s58 offen offset:3072 sc0 sc1 nt\n"
"ds_read_b128 v[224:227], v254 offset:5120\n"
"v_mfma_f32_16x16x32_bf16 v[60:63], v[208:211], a[220:223], v[60:63]\n"
"v_mfma_f32_16x16x32_bf16 v[192:195], a[96:99], a[48:51], v[232:235]\n"
"v_exp_f32 v204, v204\n"
"s_waitcnt lgkmcnt(0)\n"
"v_mfma_f32_16x16x32_bf16 v[192:195], a[100:103], a[52:55], v[192:195]\n"
"v_cvt_pk_bf16_f32 v212, v200, v201\n"
"v_cvt_pk_bf16_f32 v213, v202, v203\n"
"v_pk_mul_f32 v[32:33], v[32:33], v[224:225]\n"
"v_pk_mul_f32 v[34:35], v[34:35], v[226:227]\n"
"v_pk_mul_f32 v[36:37], v[36:37], v[224:225]\n"
"v_pk_mul_f32 v[38:39], v[38:39], v[226:227]\n"
"v_pk_mul_f32 v[40:41], v[40:41], v[224:225]\n"
"v_pk_mul_f32 v[42:43], v[42:43], v[226:227]\n"
"v_pk_mul_f32 v[44:45], v[44:45], v[224:225]\n"
"v_pk_mul_f32 v[46:47], v[46:47], v[226:227]\n"
"v_mfma_f32_16x16x32_bf16 v[192:195], a[104:107], a[56:59], v[192:195]\n"
"v_exp_f32 v205, v205\n"
"v_cvt_pk_bf16_f32 v32, v32, v36\n"
"v_cvt_pk_bf16_f32 v40, v40, v44\n"
"v_mfma_f32_16x16x32_bf16 v[192:195], a[108:111], a[60:63], v[192:195]\n"
"v_cvt_pk_bf16_f32 v214, v204, v205\n"
"v_mfma_f32_16x16x32_bf16 v[196:199], a[112:115], a[60:63], v[232:235]\n"
"ds_write2_b32 v248, v32, v40 offset0:0 offset1:1\n"
"v_exp_f32 v206, v206\n"
"v_cvt_pk_bf16_f32 v33, v33, v37\n"
"v_cvt_pk_bf16_f32 v41, v41, v45\n"
"v_mfma_f32_16x16x32_bf16 v[196:199], a[116:119], a[56:59], v[196:199]\n"
"ds_write2_b32 v248, v33, v41 offset0:72 offset1:73\n"
"v_cvt_pk_bf16_f32 v34, v34, v38\n"
"v_cvt_pk_bf16_f32 v42, v42, v46\n"
"v_pk_mul_f32 v[48:49], v[48:49], v[224:225]\n"
"v_pk_mul_f32 v[50:51], v[50:51], v[226:227]\n"
"v_pk_mul_f32 v[52:53], v[52:53], v[224:225]\n"
"v_pk_mul_f32 v[54:55], v[54:55], v[226:227]\n"
"v_pk_mul_f32 v[56:57], v[56:57], v[224:225]\n"
"v_pk_mul_f32 v[58:59], v[58:59], v[226:227]\n"
"v_pk_mul_f32 v[60:61], v[60:61], v[224:225]\n"
"v_pk_mul_f32 v[62:63], v[62:63], v[226:227]\n"
"v_mfma_f32_16x16x32_bf16 v[196:199], a[120:123], a[52:55], v[196:199]\n"
"v_exp_f32 v207, v207\n"
"v_mfma_f32_16x16x32_bf16 v[196:199], a[124:127], a[48:51], v[196:199]\n"
"ds_write2_b32 v248, v34, v42 offset0:144 offset1:145\n"
"v_cvt_pk_bf16_f32 v215, v206, v207\n"
"v_cvt_pk_bf16_f32 v35, v35, v39\n"
"v_cvt_pk_bf16_f32 v43, v43, v47\n"
"v_mfma_f32_16x16x32_bf16 v[200:203], a[128:131], a[48:51], v[232:235]\n"
"ds_write2_b32 v248, v35, v43 offset0:216 offset1:217\n"
"v_exp_f32 v192, v192\n"
"v_cvt_pk_bf16_f32 v48, v48, v52\n"
"v_cvt_pk_bf16_f32 v56, v56, v60\n"
"v_mfma_f32_16x16x32_bf16 v[200:203], a[132:135], a[52:55], v[200:203]\n"
"v_mfma_f32_16x16x32_bf16 v[200:203], a[136:139], a[56:59], v[200:203]\n"
"ds_write2_b32 v248, v48, v56 offset0:32 offset1:33\n"
"v_exp_f32 v193, v193\n"
"v_cvt_pk_bf16_f32 v49, v49, v53\n"
"v_cvt_pk_bf16_f32 v57, v57, v61\n"
"v_mfma_f32_16x16x32_bf16 v[200:203], a[140:143], a[60:63], v[200:203]\n"
"ds_write2_b32 v248, v49, v57 offset0:104 offset1:105\n"
"v_cvt_pk_bf16_f32 v208, v192, v193\n"
"v_cvt_pk_bf16_f32 v50, v50, v54\n"
"v_cvt_pk_bf16_f32 v58, v58, v62\n"
"v_mfma_f32_16x16x32_bf16 v[204:207], a[144:147], a[60:63], v[232:235]\n"
"v_exp_f32 v194, v194\n"
"v_mfma_f32_16x16x32_bf16 v[204:207], a[148:151], a[56:59], v[204:207]\n"
"ds_write2_b32 v248, v50, v58 offset0:176 offset1:177\n"
"v_cvt_pk_bf16_f32 v51, v51, v55\n"
"v_cvt_pk_bf16_f32 v59, v59, v63\n"
"v_mfma_f32_16x16x32_bf16 v[204:207], a[152:155], a[52:55], v[204:207]\n"
"ds_write2_b32 v248, v51, v59 offset0:248 offset1:249\n"
"v_exp_f32 v195, v195\n"
"v_mfma_f32_16x16x32_bf16 v[204:207], a[156:159], a[48:51], v[204:207]\n"
"v_cvt_pk_bf16_f32 v209, v194, v195\n"
"v_smfmac_f32_16x16x64_bf16 a[232:235], a[248:251], v[212:219], v252\n"
"ds_read_b64_tr_b16 v[36:37], v249 offset:0\n"
"ds_read_b64_tr_b16 v[38:39], v249 offset:32\n"
"v_exp_f32 v196, v196\n"
"v_mfma_f32_16x16x32_bf16 v[64:67], v[216:219], a[160:163], v[64:67]\n"
"ds_read_b64_tr_b16 v[44:45], v249 offset:1152\n"
"ds_read_b64_tr_b16 v[46:47], v249 offset:1184\n"
"v_mfma_f32_16x16x32_bf16 v[64:67], v[212:215], a[164:167], v[64:67]\n"
"v_exp_f32 v197, v197\n"
"v_mfma_f32_16x16x32_bf16 v[68:71], v[212:215], a[168:171], v[68:71]\n"
"ds_read_b64_tr_b16 v[52:53], v249 offset:2304\n"
"ds_read_b64_tr_b16 v[54:55], v249 offset:2336\n"
"v_cvt_pk_bf16_f32 v210, v196, v197\n"
"v_mfma_f32_16x16x32_bf16 v[68:71], v[216:219], a[172:175], v[68:71]\n"
"ds_read_b64_tr_b16 v[60:61], v249 offset:3456\n"
"ds_read_b64_tr_b16 v[62:63], v249 offset:3488\n"
"v_exp_f32 v198, v198\n"
"v_mfma_f32_16x16x32_bf16 v[72:75], v[216:219], a[176:179], v[72:75]\n"
"v_mfma_f32_16x16x32_bf16 v[72:75], v[212:215], a[180:183], v[72:75]\n"
"v_exp_f32 v199, v199\n"
"v_mfma_f32_16x16x32_bf16 v[76:79], v[212:215], a[184:187], v[76:79]\n"
"v_cvt_pk_bf16_f32 v211, v198, v199\n"
"v_mfma_f32_16x16x32_bf16 v[76:79], v[216:219], a[188:191], v[76:79]\n"
"v_exp_f32 v200, v200\n"
"v_mfma_f32_16x16x32_bf16 v[80:83], v[216:219], a[192:195], v[80:83]\n"
"v_accvgpr_read_b32 v228, a232\n"
"v_mfma_f32_16x16x32_bf16 v[80:83], v[212:215], a[196:199], v[80:83]\n"
"s_waitcnt lgkmcnt(6)\n"
"v_exp_f32 v201, v201\n"
"s_add_u32 s58, s59, 4096\n"
"buffer_store_dwordx4 v[36:39], v250, s[48:51], s58 offen sc0 sc1 nt\n"
"v_accvgpr_read_b32 v229, a233\n"
"v_mfma_f32_16x16x32_bf16 v[84:87], v[212:215], a[200:203], v[84:87]\n"
"s_waitcnt lgkmcnt(4)\n"
"buffer_store_dwordx4 v[44:47], v250, s[48:51], s58 offen offset:1024 sc0 sc1 nt\n"
"v_add_f32 v228, v228, v229\n"
"v_mfma_f32_16x16x32_bf16 v[84:87], v[216:219], a[204:207], v[84:87]\n"
"v_exp_f32 v202, v202\n"
"v_add_f32 v255, v255, v228\n"
"v_mfma_f32_16x16x32_bf16 v[88:91], v[216:219], a[208:211], v[88:91]\n"
"s_waitcnt lgkmcnt(2)\n"
"buffer_store_dwordx4 v[52:55], v250, s[48:51], s58 offen offset:2048 sc0 sc1 nt\n"
"v_rcp_f32 v228, v228\n"
"v_mfma_f32_16x16x32_bf16 v[88:91], v[212:215], a[212:215], v[88:91]\n"
"ds_write_b32 v251, v228 offset:5120\n"
"v_exp_f32 v203, v203\n"
"v_mfma_f32_16x16x32_bf16 v[92:95], v[212:215], a[216:219], v[92:95]\n"
"s_waitcnt lgkmcnt(1)\n"
"buffer_store_dwordx4 v[60:63], v250, s[48:51], s58 offen offset:3072 sc0 sc1 nt\n"
"ds_read_b128 v[228:231], v254 offset:5120\n"
"v_mfma_f32_16x16x32_bf16 v[92:95], v[216:219], a[220:223], v[92:95]\n"
"v_mfma_f32_16x16x32_bf16 v[192:195], a[96:99], a[64:67], v[236:239]\n"
"v_exp_f32 v204, v204\n"
"s_waitcnt lgkmcnt(0)\n"
"v_mfma_f32_16x16x32_bf16 v[192:195], a[100:103], a[68:71], v[192:195]\n"
"v_cvt_pk_bf16_f32 v212, v200, v201\n"
"v_cvt_pk_bf16_f32 v213, v202, v203\n"
"v_pk_mul_f32 v[64:65], v[64:65], v[228:229]\n"
"v_pk_mul_f32 v[66:67], v[66:67], v[230:231]\n"
"v_pk_mul_f32 v[68:69], v[68:69], v[228:229]\n"
"v_pk_mul_f32 v[70:71], v[70:71], v[230:231]\n"
"v_pk_mul_f32 v[72:73], v[72:73], v[228:229]\n"
"v_pk_mul_f32 v[74:75], v[74:75], v[230:231]\n"
"v_pk_mul_f32 v[76:77], v[76:77], v[228:229]\n"
"v_pk_mul_f32 v[78:79], v[78:79], v[230:231]\n"
"v_mfma_f32_16x16x32_bf16 v[192:195], a[104:107], a[72:75], v[192:195]\n"
"v_exp_f32 v205, v205\n"
"v_cvt_pk_bf16_f32 v64, v64, v68\n"
"v_cvt_pk_bf16_f32 v72, v72, v76\n"
"v_mfma_f32_16x16x32_bf16 v[192:195], a[108:111], a[76:79], v[192:195]\n"
"v_cvt_pk_bf16_f32 v214, v204, v205\n"
"v_mfma_f32_16x16x32_bf16 v[196:199], a[112:115], a[76:79], v[236:239]\n"
"ds_write2_b32 v248, v64, v72 offset0:0 offset1:1\n"
"v_exp_f32 v206, v206\n"
"v_cvt_pk_bf16_f32 v65, v65, v69\n"
"v_cvt_pk_bf16_f32 v73, v73, v77\n"
"v_mfma_f32_16x16x32_bf16 v[196:199], a[116:119], a[72:75], v[196:199]\n"
"ds_write2_b32 v248, v65, v73 offset0:72 offset1:73\n"
"v_cvt_pk_bf16_f32 v66, v66, v70\n"
"v_cvt_pk_bf16_f32 v74, v74, v78\n"
"v_pk_mul_f32 v[80:81], v[80:81], v[228:229]\n"
"v_pk_mul_f32 v[82:83], v[82:83], v[230:231]\n"
"v_pk_mul_f32 v[84:85], v[84:85], v[228:229]\n"
"v_pk_mul_f32 v[86:87], v[86:87], v[230:231]\n"
"v_pk_mul_f32 v[88:89], v[88:89], v[228:229]\n"
"v_pk_mul_f32 v[90:91], v[90:91], v[230:231]\n"
"v_pk_mul_f32 v[92:93], v[92:93], v[228:229]\n"
"v_pk_mul_f32 v[94:95], v[94:95], v[230:231]\n"
"v_mfma_f32_16x16x32_bf16 v[196:199], a[120:123], a[68:71], v[196:199]\n"
"v_exp_f32 v207, v207\n"
"v_mfma_f32_16x16x32_bf16 v[196:199], a[124:127], a[64:67], v[196:199]\n"
"ds_write2_b32 v248, v66, v74 offset0:144 offset1:145\n"
"v_cvt_pk_bf16_f32 v215, v206, v207\n"
"v_cvt_pk_bf16_f32 v67, v67, v71\n"
"v_cvt_pk_bf16_f32 v75, v75, v79\n"
"v_mfma_f32_16x16x32_bf16 v[200:203], a[128:131], a[64:67], v[236:239]\n"
"ds_write2_b32 v248, v67, v75 offset0:216 offset1:217\n"
"v_exp_f32 v192, v192\n"
"v_cvt_pk_bf16_f32 v80, v80, v84\n"
"v_cvt_pk_bf16_f32 v88, v88, v92\n"
"v_mfma_f32_16x16x32_bf16 v[200:203], a[132:135], a[68:71], v[200:203]\n"
"v_mfma_f32_16x16x32_bf16 v[200:203], a[136:139], a[72:75], v[200:203]\n"
"ds_write2_b32 v248, v80, v88 offset0:32 offset1:33\n"
"v_exp_f32 v193, v193\n"
"v_cvt_pk_bf16_f32 v81, v81, v85\n"
"v_cvt_pk_bf16_f32 v89, v89, v93\n"
"v_mfma_f32_16x16x32_bf16 v[200:203], a[140:143], a[76:79], v[200:203]\n"
"ds_write2_b32 v248, v81, v89 offset0:104 offset1:105\n"
"v_cvt_pk_bf16_f32 v216, v192, v193\n"
"v_cvt_pk_bf16_f32 v82, v82, v86\n"
"v_cvt_pk_bf16_f32 v90, v90, v94\n"
"v_mfma_f32_16x16x32_bf16 v[204:207], a[144:147], a[76:79], v[236:239]\n"
"v_exp_f32 v194, v194\n"
"v_mfma_f32_16x16x32_bf16 v[204:207], a[148:151], a[72:75], v[204:207]\n"
"ds_write2_b32 v248, v82, v90 offset0:176 offset1:177\n"
"v_cvt_pk_bf16_f32 v83, v83, v87\n"
"v_cvt_pk_bf16_f32 v91, v91, v95\n"
"v_mfma_f32_16x16x32_bf16 v[204:207], a[152:155], a[68:71], v[204:207]\n"
"ds_write2_b32 v248, v83, v91 offset0:248 offset1:249\n"
"v_exp_f32 v195, v195\n"
"v_mfma_f32_16x16x32_bf16 v[204:207], a[156:159], a[64:67], v[204:207]\n"
"v_cvt_pk_bf16_f32 v217, v194, v195\n"
"v_smfmac_f32_16x16x64_bf16 a[236:239], a[248:251], v[208:215], v252\n"
"ds_read_b64_tr_b16 v[68:69], v249 offset:0\n"
"ds_read_b64_tr_b16 v[70:71], v249 offset:32\n"
"v_exp_f32 v196, v196\n"
"v_mfma_f32_16x16x32_bf16 v[96:99], v[208:211], a[160:163], v[96:99]\n"
"ds_read_b64_tr_b16 v[76:77], v249 offset:1152\n"
"ds_read_b64_tr_b16 v[78:79], v249 offset:1184\n"
"v_mfma_f32_16x16x32_bf16 v[96:99], v[212:215], a[164:167], v[96:99]\n"
"v_exp_f32 v197, v197\n"
"v_mfma_f32_16x16x32_bf16 v[100:103], v[212:215], a[168:171], v[100:103]\n"
"ds_read_b64_tr_b16 v[84:85], v249 offset:2304\n"
"ds_read_b64_tr_b16 v[86:87], v249 offset:2336\n"
"v_cvt_pk_bf16_f32 v218, v196, v197\n"
"v_mfma_f32_16x16x32_bf16 v[100:103], v[208:211], a[172:175], v[100:103]\n"
"ds_read_b64_tr_b16 v[92:93], v249 offset:3456\n"
"ds_read_b64_tr_b16 v[94:95], v249 offset:3488\n"
"v_exp_f32 v198, v198\n"
"v_mfma_f32_16x16x32_bf16 v[104:107], v[208:211], a[176:179], v[104:107]\n"
"v_mfma_f32_16x16x32_bf16 v[104:107], v[212:215], a[180:183], v[104:107]\n"
"v_exp_f32 v199, v199\n"
"v_mfma_f32_16x16x32_bf16 v[108:111], v[212:215], a[184:187], v[108:111]\n"
"v_cvt_pk_bf16_f32 v219, v198, v199\n"
"v_mfma_f32_16x16x32_bf16 v[108:111], v[208:211], a[188:191], v[108:111]\n"
"v_exp_f32 v200, v200\n"
"v_mfma_f32_16x16x32_bf16 v[112:115], v[208:211], a[192:195], v[112:115]\n"
"v_accvgpr_read_b32 v232, a236\n"
"v_mfma_f32_16x16x32_bf16 v[112:115], v[212:215], a[196:199], v[112:115]\n"
"s_waitcnt lgkmcnt(6)\n"
"v_exp_f32 v201, v201\n"
"s_add_u32 s58, s59, 8192\n"
"buffer_store_dwordx4 v[68:71], v250, s[48:51], s58 offen sc0 sc1 nt\n"
"v_accvgpr_read_b32 v233, a237\n"
"v_mfma_f32_16x16x32_bf16 v[116:119], v[212:215], a[200:203], v[116:119]\n"
"s_waitcnt lgkmcnt(4)\n"
"buffer_store_dwordx4 v[76:79], v250, s[48:51], s58 offen offset:1024 sc0 sc1 nt\n"
"v_add_f32 v232, v232, v233\n"
"v_mfma_f32_16x16x32_bf16 v[116:119], v[208:211], a[204:207], v[116:119]\n"
"v_exp_f32 v202, v202\n"
"v_add_f32 v255, v255, v232\n"
"v_mfma_f32_16x16x32_bf16 v[120:123], v[208:211], a[208:211], v[120:123]\n"
"s_waitcnt lgkmcnt(2)\n"
"buffer_store_dwordx4 v[84:87], v250, s[48:51], s58 offen offset:2048 sc0 sc1 nt\n"
"v_rcp_f32 v232, v232\n"
"v_mfma_f32_16x16x32_bf16 v[120:123], v[212:215], a[212:215], v[120:123]\n"
"ds_write_b32 v251, v232 offset:5120\n"
"v_exp_f32 v203, v203\n"
"v_mfma_f32_16x16x32_bf16 v[124:127], v[212:215], a[216:219], v[124:127]\n"
"s_waitcnt lgkmcnt(1)\n"
"buffer_store_dwordx4 v[92:95], v250, s[48:51], s58 offen offset:3072 sc0 sc1 nt\n"
"ds_read_b128 v[232:235], v254 offset:5120\n"
"v_mfma_f32_16x16x32_bf16 v[124:127], v[208:211], a[220:223], v[124:127]\n"
"v_exp_f32 v204, v204\n"
"v_exp_f32 v205, v205\n"
"v_cvt_pk_bf16_f32 v212, v200, v201\n"
"v_cvt_pk_bf16_f32 v213, v202, v203\n"
"v_exp_f32 v206, v206\n"
"v_exp_f32 v207, v207\n"
"v_cvt_pk_bf16_f32 v214, v204, v205\n"
"v_cvt_pk_bf16_f32 v215, v206, v207\n"
"s_nop 1\n"
"v_smfmac_f32_16x16x64_bf16 a[240:243], a[248:251], v[212:219], v252\n"
"s_waitcnt lgkmcnt(0)\n"
"v_pk_mul_f32 v[96:97], v[96:97], v[232:233]\n"
"v_pk_mul_f32 v[98:99], v[98:99], v[234:235]\n"
"v_pk_mul_f32 v[100:101], v[100:101], v[232:233]\n"
"v_pk_mul_f32 v[102:103], v[102:103], v[234:235]\n"
"v_pk_mul_f32 v[104:105], v[104:105], v[232:233]\n"
"v_pk_mul_f32 v[106:107], v[106:107], v[234:235]\n"
"v_pk_mul_f32 v[108:109], v[108:109], v[232:233]\n"
"v_pk_mul_f32 v[110:111], v[110:111], v[234:235]\n"
"v_mfma_f32_16x16x32_bf16 v[128:131], v[216:219], a[160:163], v[128:131]\n"
"v_cvt_pk_bf16_f32 v96, v96, v100\n"
"v_cvt_pk_bf16_f32 v104, v104, v108\n"
"v_mfma_f32_16x16x32_bf16 v[128:131], v[212:215], a[164:167], v[128:131]\n"
"ds_write2_b32 v248, v96, v104 offset0:0 offset1:1\n"
"v_cvt_pk_bf16_f32 v97, v97, v101\n"
"v_cvt_pk_bf16_f32 v105, v105, v109\n"
"v_mfma_f32_16x16x32_bf16 v[132:135], v[212:215], a[168:171], v[132:135]\n"
"ds_write2_b32 v248, v97, v105 offset0:72 offset1:73\n"
"v_cvt_pk_bf16_f32 v98, v98, v102\n"
"v_cvt_pk_bf16_f32 v106, v106, v110\n"
"v_pk_mul_f32 v[112:113], v[112:113], v[232:233]\n"
"v_pk_mul_f32 v[114:115], v[114:115], v[234:235]\n"
"v_pk_mul_f32 v[116:117], v[116:117], v[232:233]\n"
"v_pk_mul_f32 v[118:119], v[118:119], v[234:235]\n"
"v_pk_mul_f32 v[120:121], v[120:121], v[232:233]\n"
"v_pk_mul_f32 v[122:123], v[122:123], v[234:235]\n"
"v_pk_mul_f32 v[124:125], v[124:125], v[232:233]\n"
"v_pk_mul_f32 v[126:127], v[126:127], v[234:235]\n"
"v_mfma_f32_16x16x32_bf16 v[132:135], v[216:219], a[172:175], v[132:135]\n"
"ds_write2_b32 v248, v98, v106 offset0:144 offset1:145\n"
"v_cvt_pk_bf16_f32 v99, v99, v103\n"
"v_cvt_pk_bf16_f32 v107, v107, v111\n"
"ds_write2_b32 v248, v99, v107 offset0:216 offset1:217\n"
"v_cvt_pk_bf16_f32 v112, v112, v116\n"
"v_cvt_pk_bf16_f32 v120, v120, v124\n"
"v_mfma_f32_16x16x32_bf16 v[136:139], v[216:219], a[176:179], v[136:139]\n"
"ds_write2_b32 v248, v112, v120 offset0:32 offset1:33\n"
"v_cvt_pk_bf16_f32 v113, v113, v117\n"
"v_cvt_pk_bf16_f32 v121, v121, v125\n"
"v_mfma_f32_16x16x32_bf16 v[136:139], v[212:215], a[180:183], v[136:139]\n"
"ds_write2_b32 v248, v113, v121 offset0:104 offset1:105\n"
"v_cvt_pk_bf16_f32 v114, v114, v118\n"
"v_cvt_pk_bf16_f32 v122, v122, v126\n"
"v_mfma_f32_16x16x32_bf16 v[140:143], v[212:215], a[184:187], v[140:143]\n"
"ds_write2_b32 v248, v114, v122 offset0:176 offset1:177\n"
"v_cvt_pk_bf16_f32 v115, v115, v119\n"
"v_cvt_pk_bf16_f32 v123, v123, v127\n"
"v_mfma_f32_16x16x32_bf16 v[140:143], v[216:219], a[188:191], v[140:143]\n"
"ds_write2_b32 v248, v115, v123 offset0:248 offset1:249\n"
"ds_read_b64_tr_b16 v[100:101], v249 offset:0\n"
"ds_read_b64_tr_b16 v[102:103], v249 offset:32\n"
"v_mfma_f32_16x16x32_bf16 v[144:147], v[216:219], a[192:195], v[144:147]\n"
"ds_read_b64_tr_b16 v[108:109], v249 offset:1152\n"
"ds_read_b64_tr_b16 v[110:111], v249 offset:1184\n"
"v_accvgpr_read_b32 v236, a240\n"
"v_mfma_f32_16x16x32_bf16 v[144:147], v[212:215], a[196:199], v[144:147]\n"
"ds_read_b64_tr_b16 v[116:117], v249 offset:2304\n"
"ds_read_b64_tr_b16 v[118:119], v249 offset:2336\n"
"v_accvgpr_read_b32 v237, a241\n"
"v_mfma_f32_16x16x32_bf16 v[148:151], v[212:215], a[200:203], v[148:151]\n"
"ds_read_b64_tr_b16 v[124:125], v249 offset:3456\n"
"ds_read_b64_tr_b16 v[126:127], v249 offset:3488\n"
"v_add_f32 v236, v236, v237\n"
"v_mfma_f32_16x16x32_bf16 v[148:151], v[216:219], a[204:207], v[148:151]\n"
"v_add_f32 v255, v255, v236\n"
"v_mfma_f32_16x16x32_bf16 v[152:155], v[216:219], a[208:211], v[152:155]\n"
"v_rcp_f32 v236, v236\n"
"v_mfma_f32_16x16x32_bf16 v[152:155], v[212:215], a[212:215], v[152:155]\n"
"ds_write_b32 v251, v236 offset:5120\n"
"v_mfma_f32_16x16x32_bf16 v[156:159], v[212:215], a[216:219], v[156:159]\n"
"ds_read_b128 v[236:239], v254 offset:5120\n"
"v_mfma_f32_16x16x32_bf16 v[156:159], v[216:219], a[220:223], v[156:159]\n"
"s_add_u32 s58, s59, 12288\n"
"s_waitcnt lgkmcnt(8)\n"
"buffer_store_dwordx4 v[100:103], v250, s[48:51], s58 offen sc0 sc1 nt\n"
"s_waitcnt lgkmcnt(0)\n"
"v_pk_mul_f32 v[128:129], v[128:129], v[236:237]\n"
"v_pk_mul_f32 v[130:131], v[130:131], v[238:239]\n"
"v_pk_mul_f32 v[132:133], v[132:133], v[236:237]\n"
"v_pk_mul_f32 v[134:135], v[134:135], v[238:239]\n"
"v_pk_mul_f32 v[136:137], v[136:137], v[236:237]\n"
"v_pk_mul_f32 v[138:139], v[138:139], v[238:239]\n"
"v_pk_mul_f32 v[140:141], v[140:141], v[236:237]\n"
"v_pk_mul_f32 v[142:143], v[142:143], v[238:239]\n"
"v_cvt_pk_bf16_f32 v128, v128, v132\n"
"v_cvt_pk_bf16_f32 v136, v136, v140\n"
"ds_write2_b32 v248, v128, v136 offset0:0 offset1:1\n"
"buffer_store_dwordx4 v[108:111], v250, s[48:51], s58 offen offset:1024 sc0 sc1 nt\n"
"v_cvt_pk_bf16_f32 v129, v129, v133\n"
"v_cvt_pk_bf16_f32 v137, v137, v141\n"
"ds_write2_b32 v248, v129, v137 offset0:72 offset1:73\n"
"v_cvt_pk_bf16_f32 v130, v130, v134\n"
"v_cvt_pk_bf16_f32 v138, v138, v142\n"
"v_pk_mul_f32 v[144:145], v[144:145], v[236:237]\n"
"v_pk_mul_f32 v[146:147], v[146:147], v[238:239]\n"
"v_pk_mul_f32 v[148:149], v[148:149], v[236:237]\n"
"v_pk_mul_f32 v[150:151], v[150:151], v[238:239]\n"
"v_pk_mul_f32 v[152:153], v[152:153], v[236:237]\n"
"v_pk_mul_f32 v[154:155], v[154:155], v[238:239]\n"
"v_pk_mul_f32 v[156:157], v[156:157], v[236:237]\n"
"buffer_store_dwordx4 v[116:119], v250, s[48:51], s58 offen offset:2048 sc0 sc1 nt\n"
"v_pk_mul_f32 v[158:159], v[158:159], v[238:239]\n"
"ds_write2_b32 v248, v130, v138 offset0:144 offset1:145\n"
"v_cvt_pk_bf16_f32 v131, v131, v135\n"
"v_cvt_pk_bf16_f32 v139, v139, v143\n"
"ds_write2_b32 v248, v131, v139 offset0:216 offset1:217\n"
"v_cvt_pk_bf16_f32 v144, v144, v148\n"
"v_cvt_pk_bf16_f32 v152, v152, v156\n"
"ds_write2_b32 v248, v144, v152 offset0:32 offset1:33\n"
"v_cvt_pk_bf16_f32 v145, v145, v149\n"
"v_cvt_pk_bf16_f32 v153, v153, v157\n"
"ds_write2_b32 v248, v145, v153 offset0:104 offset1:105\n"
"v_cvt_pk_bf16_f32 v146, v146, v150\n"
"buffer_store_dwordx4 v[124:127], v250, s[48:51], s58 offen offset:3072 sc0 sc1 nt\n"
"v_cvt_pk_bf16_f32 v154, v154, v158\n"
"ds_write2_b32 v248, v146, v154 offset0:176 offset1:177\n"
"v_cvt_pk_bf16_f32 v147, v147, v151\n"
"v_cvt_pk_bf16_f32 v155, v155, v159\n"
"ds_write2_b32 v248, v147, v155 offset0:248 offset1:249\n"
"ds_read_b64_tr_b16 v[132:133], v249 offset:0\n"
"ds_read_b64_tr_b16 v[134:135], v249 offset:32\n"
"ds_read_b64_tr_b16 v[140:141], v249 offset:1152\n"
"ds_read_b64_tr_b16 v[142:143], v249 offset:1184\n"
"ds_read_b64_tr_b16 v[148:149], v249 offset:2304\n"
"ds_read_b64_tr_b16 v[150:151], v249 offset:2336\n"
"ds_read_b64_tr_b16 v[156:157], v249 offset:3456\n"
"ds_read_b64_tr_b16 v[158:159], v249 offset:3488\n"
"s_add_u32 s58, s59, 16384\n"
"s_waitcnt lgkmcnt(6)\n"
"buffer_store_dwordx4 v[132:135], v250, s[48:51], s58 offen sc0 sc1 nt\n"
"s_waitcnt lgkmcnt(4)\n"
"buffer_store_dwordx4 v[140:143], v250, s[48:51], s58 offen offset:1024 sc0 sc1 nt\n"
"s_waitcnt lgkmcnt(2)\n"
"buffer_store_dwordx4 v[148:151], v250, s[48:51], s58 offen offset:2048 sc0 sc1 nt\n"
"s_waitcnt lgkmcnt(0)\n"
"buffer_store_dwordx4 v[156:159], v250, s[48:51], s58 offen offset:3072 sc0 sc1 nt\n"
"L_join_%=:\n"
"s_nop 7\n"
"v_cmp_nge_f32 vcc, 0x4e800000, v255\n"
"s_lshl_b32 s55, %[wid], 2\n"
"s_add_u32 s55, s55, 136704\n"
"v_mov_b32 v253, s55\n"
"s_or_b32 s55, vcc_lo, vcc_hi\n"
"v_mov_b32 v254, s55\n"
"ds_write_b32 v253, v254\n"
"v_mov_b32 v253, 136704\n"
"s_waitcnt lgkmcnt(0)\n"
"s_barrier\n"
"ds_read_b128 v[192:195], v253\n"
"s_waitcnt lgkmcnt(0)\n"
"v_or3_b32 v192, v192, v193, v194\n"
"v_or_b32 v192, v192, v195\n"
"s_nop 0\n"
"v_readfirstlane_b32 %[flag], v192\n"
"s_nop 4\n"
"s_cmp_lg_u32 %[flag], 0\n"
"s_cbranch_scc1 L_end_%=\n"
"s_endpgm\n"
"L_end_%=:\n"
      : [flag] "=&s"(flag), "+{v220}"(m0), "+{v224}"(m1), "+{v228}"(m2), "+{v232}"(m3), "+{v236}"(m4), "+{v240}"(m5)
      : [karg] "s"(__builtin_amdgcn_kernarg_segment_ptr()), [wid] "s"(wid), [blk] "s"(head), [ldsb] "s"((unsigned)(size_t)lds)
      :
        "memory", "scc", "vcc", "s16", "s17", "s18", "s19", "s20", "s21", "s22", "s23", "s24", "s25", "s26", "s27", "s28", "s36", "s37", "s38", "s39", "s40", "s41", "s42", "s43",
        "s44", "s45", "s46", "s47", "s48", "s49", "s50", "s51", "s52", "s53", "s54", "s55", "s56", "s57", "s58", "s59", "v0", "v1", "v2", "v3", "v4", "v5", "v6", "v7",
        "v8", "v9", "v10", "v11", "v12", "v13", "v14", "v15", "v16", "v17", "v18", "v19", "v20", "v21", "v22", "v23", "v24", "v25", "v26", "v27", "v28", "v29", "v30", "v31",
        "v32", "v33", "v34", "v35", "v36", "v37", "v38", "v39", "v40", "v41", "v42", "v43", "v44", "v45", "v46", "v47", "v48", "v49", "v50", "v51", "v52", "v53", "v54", "v55",
        "v56", "v57", "v58", "v59", "v60", "v61", "v62", "v63", "v64", "v65", "v66", "v67", "v68", "v69", "v70", "v71", "v72", "v73", "v74", "v75", "v76", "v77", "v78", "v79",
        "v80", "v81", "v82", "v83", "v84", "v85", "v86", "v87", "v88", "v89", "v90", "v91", "v92", "v93", "v94", "v95", "v96", "v97", "v98", "v99", "v100", "v101", "v102", "v103",
        "v104", "v105", "v106", "v107", "v108", "v109", "v110", "v111", "v112", "v113", "v114", "v115", "v116", "v117", "v118", "v119", "v120", "v121", "v122", "v123", "v124", "v125", "v126", "v127",
        "v128", "v129", "v130", "v131", "v132", "v133", "v134", "v135", "v136", "v137", "v138", "v139", "v140", "v141", "v142", "v143", "v144", "v145", "v146", "v147", "v148", "v149", "v150", "v151",
        "v152", "v153", "v154", "v155", "v156", "v157", "v158", "v159", "v160", "v161", "v162", "v163", "v164", "v165", "v166", "v167", "v168", "v169", "v170", "v171", "v172", "v173", "v174", "v175",
        "v176", "v177", "v178", "v179", "v180", "v181", "v182", "v183", "v184", "v185", "v186", "v187", "v188", "v189", "v190", "v191", "v192", "v193", "v194", "v195", "v196", "v197", "v198", "v199",
        "v200", "v201", "v202", "v203", "v204", "v205", "v206", "v207", "v208", "v209", "v210", "v211", "v212", "v213", "v214", "v215", "v216", "v217", "v218", "v219", "v221", "v222", "v223", "v225",
        "v226", "v227", "v229", "v230", "v231", "v233", "v234", "v235", "v237", "v238", "v239", "v241", "v242", "v243", "v244", "v245", "v246", "v247", "v248", "v249", "v250", "v251", "v252", "v253",
        "v254", "v255", "a0", "a1", "a2", "a3", "a4", "a5", "a6", "a7", "a8", "a9", "a10", "a11", "a12", "a13", "a14", "a15", "a16", "a17", "a18", "a19", "a20", "a21",
        "a22", "a23", "a24", "a25", "a26", "a27", "a28", "a29", "a30", "a31", "a32", "a33", "a34", "a35", "a36", "a37", "a38", "a39", "a40", "a41", "a42", "a43", "a44", "a45",
        "a46", "a47", "a48", "a49", "a50", "a51", "a52", "a53", "a54", "a55", "a56", "a57", "a58", "a59", "a60", "a61", "a62", "a63", "a64", "a65", "a66", "a67", "a68", "a69",
        "a70", "a71", "a72", "a73", "a74", "a75", "a76", "a77", "a78", "a79", "a80", "a81", "a82", "a83", "a84", "a85", "a86", "a87", "a88", "a89", "a90", "a91", "a92", "a93",
        "a94", "a95", "a96", "a97", "a98", "a99", "a100", "a101", "a102", "a103", "a104", "a105", "a106", "a107", "a108", "a109", "a110", "a111", "a112", "a113", "a114", "a115", "a116", "a117",
        "a118", "a119", "a120", "a121", "a122", "a123", "a124", "a125", "a126", "a127", "a128", "a129", "a130", "a131", "a132", "a133", "a134", "a135", "a136", "a137", "a138", "a139", "a140", "a141",
        "a142", "a143", "a144", "a145", "a146", "a147", "a148", "a149", "a150", "a151", "a152", "a153", "a154", "a155", "a156", "a157", "a158", "a159", "a160", "a161", "a162", "a163", "a164", "a165",
        "a166", "a167", "a168", "a169", "a170", "a171", "a172", "a173", "a174", "a175", "a176", "a177", "a178", "a179", "a180", "a181", "a182", "a183", "a184", "a185", "a186", "a187", "a188", "a189",
        "a190", "a191", "a192", "a193", "a194", "a195", "a196", "a197", "a198", "a199", "a200", "a201", "a202", "a203", "a204", "a205", "a206", "a207", "a208", "a209", "a210", "a211", "a212", "a213",
        "a214", "a215", "a216", "a217", "a218", "a219", "a220", "a221", "a222", "a223", "a224", "a225", "a226", "a227", "a228", "a229", "a230", "a231", "a232", "a233", "a234", "a235", "a236", "a237",
        "a238", "a239", "a240", "a241", "a242", "a243", "a244", "a245", "a246", "a247", "a248", "a249", "a250", "a251", "a252", "a253", "a254", "a255");
    if (pass || !flag) break;
    __builtin_amdgcn_s_barrier();
    const char *qh, *kh;
    asm volatile("s_load_dwordx2 %0, %2, 0\n\ts_load_dwordx2 %1, %2, 8\n\ts_waitcnt vmcnt(0) lgkmcnt(0)"
                 : "=&s"(qh), "=&s"(kh) : "s"(__builtin_amdgcn_kernarg_segment_ptr()) : "memory");
    qh += (size_t)head * (SQ * HD * 2);
    kh += (size_t)head * (SK * HD * 2);
    const unsigned lane = lane_id();
    const int r0 = (int)wid * 96;
#pragma nounroll
    for (int b = 0; b < 6; ++b) {
      const float m = true_row_max(qh, kh, r0 + 16 * b, lane);
      m0 = b == 0 ? m : m0; m1 = b == 1 ? m : m1; m2 = b == 2 ? m : m2;
      m3 = b == 3 ? m : m3; m4 = b == 4 ? m : m4; m5 = b == 5 ? m : m5;
    }
  }
}

extern "C" void kernel_launch(void* const* d_in, const int* in_sizes, int n_in, void* d_out, int out_size, void* d_ws, size_t ws_size,
                              hipStream_t stream) {
  mha_fwd<<<dim3(NHEAD), dim3(256), 0, stream>>>((const char*)d_in[0], (const char*)d_in[1], (const char*)d_in[2], (char*)d_out);
}
```
